```python
import torch

def _softmax(x, axis=-1):
    return torch.softmax(x, dim=axis)

def _gelu(x):
    return torch.nn.functional.gelu(x, approximate='tanh')

def _silu(x):
    return torch.nn.functional.silu(x)

def _sigmoid(x):
    return torch.sigmoid(x)

def _layernorm(x, axis=-1, eps=1e-05):
    m = x.mean(dim=axis, keepdim=True)
    v = x.var(dim=axis, keepdim=True, unbiased=False)
    return (x - m) / torch.sqrt(v + eps)

def _scatter_add(tgt, idx, src, axis=0):
    return tgt.clone().index_add_(axis, idx, src)

def _segment_sum(src, seg, n):
    out = torch.zeros((n,) + src.shape[1:], dtype=src.dtype)
    return out.index_add_(0, seg, src)

def _pad(x, pw):
    flat = tuple((v for pair in reversed(pw) for v in pair))
    return torch.nn.functional.pad(x, flat)
kernel_name = 'recurrent_scan__memory_aug_recurrent_mem_tr__glu_gated_swiglu__prompt'

def setup_inputs():

    _w = lambda a: (lambda t: t.to(torch.bfloat16) if t.is_floating_point() else t)(torch.as_tensor(a))

    def _A_setup():
        B, S, Dm = (1, 2048, 448)
        H, Dh = (16, 256)
        M, seg = (8, 32)
        seg = min(seg, S)
        S = S // seg * seg
        return {'x': _w((lambda *s: torch.randn(*s, dtype=torch.bfloat16) * 0.1)(B, S, Dm)), 'mem0': _w((lambda *s: torch.randn(*s, dtype=torch.bfloat16) * 1.6)(B, M, Dm)), 'Wq': _w((lambda *s: torch.randn(*s, dtype=torch.bfloat16) * 0.8)(Dm, H, Dh) * Dm ** (-0.5)), 'Wk': _w((lambda *s: torch.randn(*s, dtype=torch.bfloat16) * 0.8)(Dm, H, Dh) * Dm ** (-0.5)), 'Wv': _w((lambda *s: torch.randn(*s, dtype=torch.bfloat16) * 1.6)(Dm, H, Dh) * Dm ** (-0.5)), 'Wo': _w((lambda *s: torch.randn(*s, dtype=torch.bfloat16) * 1.6)(H, Dh, Dm) * (H * Dh) ** (-0.5)), 'seg_len': seg}
    _A_in = _A_setup()

    def _B_setup():
        B, S, D, F = (1, 2048, 448, 1792)
        return {'x': _w((lambda *s: torch.randn(*s, dtype=torch.bfloat16) * 0.1)(B, S, D)), 'w1': _w((lambda *s: torch.randn(*s, dtype=torch.bfloat16) * 0.4)(D, F) * (2.0 / D) ** 0.5), 'w3': _w((lambda *s: torch.randn(*s, dtype=torch.bfloat16) * 0.4)(D, F) * (2.0 / D) ** 0.5), 'w2': _w((lambda *s: torch.randn(*s, dtype=torch.bfloat16) * 0.4)(F, D) * (2.0 / F) ** 0.5)}
    _B_in = _B_setup()
    return {'A_x': _A_in['x'], 'A_mem0': _A_in['mem0'], 'A_Wq': _A_in['Wq'], 'A_Wk': _A_in['Wk'], 'A_Wv': _A_in['Wv'], 'A_Wo': _A_in['Wo'], 'A_seg_len': _A_in['seg_len'], 'B_x': _B_in['x'], 'B_w1': _B_in['w1'], 'B_w3': _B_in['w3'], 'B_w2': _B_in['w2']}

def reference(A_x, A_mem0, A_Wq, A_Wk, A_Wv, A_Wo, A_seg_len, B_x, B_w1, B_w3, B_w2):

    def _A_ref(x, mem0, Wq, Wk, Wv, Wo, seg_len):
        B, S, Dm = x.shape
        H, Dh = (Wq.shape[1], Wq.shape[2])
        M = mem0.shape[1]
        scale = Dh ** (-0.5)
        n_seg = S // seg_len
        L = M + seg_len
        idx = torch.arange(L)
        mask = (idx[:, None] < M) | (idx[:, None] >= idx[None, :])
        mem = mem0
        outs = []
        for s in range(n_seg):
            xs = x[:, s * seg_len:(s + 1) * seg_len, :].clone()
            h = torch.concatenate([mem, xs], axis=1)
            q = torch.einsum('btd,dhe->bhte', h, Wq)
            k = torch.einsum('btd,dhe->bhte', h, Wk)
            v = torch.einsum('btd,dhe->bhte', h, Wv)
            scores = torch.einsum('bhqe,bhke->bhqk', q, k) * scale
            scores = torch.where(mask[None, None], scores, -1000000000.0)
            p = _softmax(scores, axis=-1)
            o = torch.einsum('bhqk,bhke->bhqe', p, v)
            o = torch.einsum('bhte,hed->btd', o, Wo)
            mem = o[:, :M, :].clone()
            outs.append(o[:, M:, :])
        return torch.concatenate(outs, axis=1)

    def _B_ref(x, w1, w3, w2):
        gate = torch.einsum('bsd,df->bsf', x, w1)
        up = torch.einsum('bsd,df->bsf', x, w3)
        h = _silu(gate) * up
        out = torch.einsum('bsf,fd->bsd', h, w2)
        return out
    x = A_x
    xn = _layernorm(x, axis=-1)
    s = _A_ref(xn, A_mem0, A_Wq, A_Wk, A_Wv, A_Wo, A_seg_len)
    g = _silu(_B_ref(xn, B_w1, B_w3, B_w2))
    return x + s * g
```

```cpp
#include <hip/hip_runtime.h>
#include <hip/hip_bf16.h>
#include <stdint.h>


typedef __bf16 bf4 __attribute__((ext_vector_type(4)));
typedef __bf16 bf8 __attribute__((ext_vector_type(8)));
typedef __bf16 bf16v __attribute__((ext_vector_type(16)));
typedef __bf16 bf2 __attribute__((ext_vector_type(2)));
typedef short s4 __attribute__((ext_vector_type(4)));
typedef short s8 __attribute__((ext_vector_type(8)));
typedef float f4 __attribute__((ext_vector_type(4)));
typedef float f2 __attribute__((ext_vector_type(2)));
typedef unsigned u4 __attribute__((ext_vector_type(4)));
typedef unsigned u2 __attribute__((ext_vector_type(2)));
typedef unsigned u8v __attribute__((ext_vector_type(8)));
typedef __amdgpu_buffer_rsrc_t rsrc_t;
#define DI __device__ __forceinline__
#define LDS_AS __attribute__((address_space(3)))

constexpr int DM = 448, NH = 16, FF = 1792, NSEG = 64;
constexpr int SC1 = 16;

constexpr unsigned OFF_SLOT = 0;
constexpr unsigned OFF_MISC = 64u * 65536u;
constexpr unsigned OFF_WC = OFF_MISC + 65536u;
constexpr unsigned OFF_XNF = OFF_WC + 14336u * 448u * 2u;
constexpr unsigned OFF_W2F = OFF_XNF + 128u * 14u * 1024u;
constexpr unsigned OFF_KQF = OFF_W2F + 28u * 56u * 1024u;
constexpr unsigned OFF_VOT = OFF_KQF + 128u * 16u * 14u * 1024u;
constexpr unsigned OFF_HF = OFF_VOT + 64u * 16u * 448u * 64u;
constexpr unsigned OFF_KQM = OFF_HF + 128u * 56u * 1024u;
constexpr unsigned OFF_UG = OFF_KQM + 64u * 16u * 8u * 896u;
constexpr unsigned WS_BYTES = OFF_UG + 64u * 32u * 224u * 16u;

constexpr unsigned SLOT_MEM = 0, SLOT_S = 16384, SLOT_E = 32768;

DI rsrc_t mk_rsrc(const void* p, unsigned bytes) { return __builtin_amdgcn_make_buffer_rsrc((void*)p, 0, (int)bytes, 0x00020000); }
template <int AUX> DI u4 ld128(rsrc_t r, unsigned vo, unsigned so) { return __builtin_amdgcn_raw_buffer_load_b128(r, (int)vo, (int)so, AUX); }
template <int AUX> DI u2 ld64(rsrc_t r, unsigned vo, unsigned so) { return __builtin_amdgcn_raw_buffer_load_b64(r, (int)vo, (int)so, AUX); }
template <int AUX> DI unsigned ld32(rsrc_t r, unsigned vo, unsigned so) { return __builtin_amdgcn_raw_buffer_load_b32(r, (int)vo, (int)so, AUX); }
template <int AUX> DI void st128(u4 v, rsrc_t r, unsigned vo, unsigned so) { __builtin_amdgcn_raw_buffer_store_b128(v, r, (int)vo, (int)so, AUX); }
template <int AUX> DI void st64(u2 v, rsrc_t r, unsigned vo, unsigned so) { __builtin_amdgcn_raw_buffer_store_b64(v, r, (int)vo, (int)so, AUX); }
template <int AUX> DI void st32(unsigned v, rsrc_t r, unsigned vo, unsigned so) { __builtin_amdgcn_raw_buffer_store_b32(v, r, (int)vo, (int)so, AUX); }

DI unsigned pk(float a, float b) { bf2 v = {(__bf16)a, (__bf16)b}; return __builtin_bit_cast(unsigned, v); }
DI float bflo(unsigned v) { return __builtin_bit_cast(float, v << 16); }
DI float bfhi(unsigned v) { return __builtin_bit_cast(float, v & 0xffff0000u); }
DI f4 mfma(u4 a, u4 b, f4 c) { return __builtin_amdgcn_mfma_f32_16x16x32_bf16(__builtin_bit_cast(bf8, a), __builtin_bit_cast(bf8, b), c, 0, 0, 0); }
DI f4 smf64(u4 a, u8v b, f4 c, int idx) { return __builtin_amdgcn_smfmac_f32_16x16x64_bf16(__builtin_bit_cast(bf8, a), __builtin_bit_cast(bf16v, b), c, idx, 0, 0); }
DI f4 smf32(u2 a, u4 b, f4 c, int idx) { return __builtin_amdgcn_smfmac_f32_16x16x32_bf16(__builtin_bit_cast(s4, a), __builtin_bit_cast(s8, b), c, idx, 0, 0); }
DI u8v cat(u4 a, u4 b) { return __builtin_shufflevector(a, b, 0, 1, 2, 3, 4, 5, 6, 7); }
DI u2 trread(unsigned lds_off) { return __builtin_bit_cast(u2, __builtin_amdgcn_ds_read_tr16_b64_v4i16((LDS_AS s4*)(uintptr_t)lds_off)); }
DI u4 ldsr128(unsigned o) { return *(LDS_AS u4*)(uintptr_t)o; }
DI u2 ldsr64(unsigned o) { return *(LDS_AS u2*)(uintptr_t)o; }
DI void ldsw128(unsigned o, u4 v) { *(LDS_AS u4*)(uintptr_t)o = v; }
DI void ldsw64(unsigned o, u2 v) { *(LDS_AS u2*)(uintptr_t)o = v; }
DI void ldsw32(unsigned o, unsigned v) { *(LDS_AS unsigned*)(uintptr_t)o = v; }
DI void wg_barrier() {
    __builtin_amdgcn_fence(__ATOMIC_RELEASE, "workgroup", "local");
    __builtin_amdgcn_s_barrier();
    __builtin_amdgcn_fence(__ATOMIC_ACQUIRE, "workgroup", "local");
}
template <int CTRL> DI float dppf(float v) { return __builtin_bit_cast(float, __builtin_amdgcn_update_dpp(0, __builtin_bit_cast(int, v), CTRL, 0xf, 0xf, true)); }
DI float ex2(float x) { return __builtin_amdgcn_exp2f(x); }
DI float silu(float x) { return x * __builtin_amdgcn_rcpf(1.0f + ex2(-1.4426950408889634f * x)); }
DI unsigned fbits(float x) { return __builtin_bit_cast(unsigned, x); }
DI float bitsf(unsigned x) { return __builtin_bit_cast(float, x); }
DI unsigned canon(unsigned v) { return v < 0xFFFFFFFEu ? v : 0xFFFFFFFEu; }
DI unsigned umax4(u4 v) { return max(max(v.x, v.y), max(v.z, v.w)); }

DI int col_of(int P, int sg) {
    const int u = sg >> 1, b = sg & 1, Pp = P >> 1, hf = P & 1;
    return (u < 4) ? (16 * Pp + 4 * u + 2 * hf + b) : (16 * (16 + 3 * (Pp >> 2) + u - 4) + 4 * (Pp & 3) + 2 * hf + b);
}

__global__ void __launch_bounds__(256, 1) k1_prep(const __bf16* __restrict__ x, const __bf16* __restrict__ mem0,
                                                  const __bf16* __restrict__ Wq, const __bf16* __restrict__ Wk,
                                                  const __bf16* __restrict__ Wv, const __bf16* __restrict__ Wo,
                                                  const __bf16* __restrict__ w2, unsigned char* __restrict__ ws) {
    __shared__ __attribute__((aligned(16))) unsigned char smem[256 * 144];
    const int tid = threadIdx.x, lane = tid & 63, wv = __builtin_amdgcn_readfirstlane(tid >> 6);
    const int g = lane >> 4, n = lane & 15;
    const int xcd = blockIdx.x & 7, idx = blockIdx.x >> 3;
    if (xcd != 0) {
        const int w = (xcd - 1) * 32 + idx;
        const bool typeB = w >= 112;
        const int wl = typeB ? w - 112 : w;
        const int h = wl / 7, r0 = 64 * (wl % 7);
        const __bf16* Am = (typeB ? Wv : Wk) + h * 256 + 8 * g;
        u4 bfr[4][8];
        if (typeB) {
            const __bf16* src = Wo + (size_t)h * 256 * DM + r0;
            u4 t[8];
#pragma unroll
            for (int i = 0; i < 8; ++i) { const int id = tid + 256 * i; t[i] = *(const u4*)(src + (id >> 3) * DM + (id & 7) * 8); }
#pragma unroll
            for (int i = 0; i < 8; ++i) { const int id = tid + 256 * i; *(u4*)(smem + (id >> 3) * 144 + (id & 7) * 16) = t[i]; }
        } else {
#pragma unroll
            for (int t = 0; t < 4; ++t)
#pragma unroll
                for (int ks = 0; ks < 8; ++ks) bfr[t][ks] = *(const u4*)(Wq + (size_t)(r0 + 16 * t + n) * 4096 + h * 256 + 32 * ks + 8 * g);
        }
        u4 afr[7][8];
#pragma unroll
        for (int t = 0; t < 7; ++t) {
            const int c = 112 * wv + (t < 6 ? 32 * (t >> 1) + 8 * (n >> 2) + 4 * (t & 1) + (n & 3) : 96 + n);
#pragma unroll
            for (int ks = 0; ks < 8; ++ks) afr[t][ks] = *(const u4*)(Am + (size_t)c * 4096 + 32 * ks);
        }
        if (typeB) {
            __syncthreads();
            const unsigned base = (unsigned)(uintptr_t)(LDS_AS unsigned char*)smem + (8 * g + (n >> 2)) * 144 + (n & 3) * 8;
#pragma unroll
            for (int t = 0; t < 4; ++t)
#pragma unroll
                for (int ks = 0; ks < 8; ++ks) {
                    const u2 lo = trread(base + ks * 32 * 144 + t * 32), hi = trread(base + (ks * 32 + 4) * 144 + t * 32);
                    bfr[t][ks] = u4{lo.x, lo.y, hi.x, hi.y};
                }
        }
        f4 acc[7][4];
#pragma unroll
        for (int t = 0; t < 7; ++t)
#pragma unroll
            for (int q = 0; q < 4; ++q) {
                f4 c = {0.f, 0.f, 0.f, 0.f};
#pragma unroll
                for (int ks = 0; ks < 8; ++ks) c = mfma(afr[t][ks], bfr[q][ks], c);
                acc[t][q] = c;
            }
        const float sc = typeB ? 1.0f : (1.4426950408889634f / 16.0f);
        __bf16* Wc = (__bf16*)(ws + OFF_WC);
#pragma unroll
        for (int q = 0; q < 4; ++q) {
            __bf16* row = Wc + (size_t)(w * 64 + 16 * q + n) * DM + 112 * wv;
#pragma unroll
            for (int p = 0; p < 3; ++p) {
                const f4 a = acc[2 * p][q] * sc, b = acc[2 * p + 1][q] * sc;
                *(u4*)(row + 32 * p + 8 * g) = u4{pk(a[0], a[1]), pk(a[2], a[3]), pk(b[0], b[1]), pk(b[2], b[3])};
            }
            const f4 a = acc[6][q] * sc;
            *(u2*)(row + 96 + 4 * g) = u2{pk(a[0], a[1]), pk(a[2], a[3])};
        }
        return;
    }
    {
        const int tile = 4 * idx + wv;
        const __bf16* src = x + (size_t)(16 * tile + n) * DM + 8 * g;
        u4 v[14];
#pragma unroll
        for (int ks = 0; ks < 14; ++ks) v[ks] = *(const u4*)(src + 32 * ks);
        float s = 0.f;
#pragma unroll
        for (int ks = 0; ks < 14; ++ks)
#pragma unroll
            for (int e = 0; e < 4; ++e) s += bflo(v[ks][e]) + bfhi(v[ks][e]);
        s += __shfl_xor(s, 16); s += __shfl_xor(s, 32);
        const float mean = s * (1.0f / DM);
        float q = 0.f;
#pragma unroll
        for (int ks = 0; ks < 14; ++ks)
#pragma unroll
            for (int e = 0; e < 4; ++e) { const float a = bflo(v[ks][e]) - mean, b = bfhi(v[ks][e]) - mean; q += a * a + b * b; }
        q += __shfl_xor(q, 16); q += __shfl_xor(q, 32);
        const float rs = 1.0f / sqrtf(q * (1.0f / DM) + 1e-5f);
        u4* dst = (u4*)(ws + OFF_XNF) + (size_t)tile * 14 * 64 + lane;
#pragma unroll
        for (int ks = 0; ks < 14; ++ks) {
            u4 o;
#pragma unroll
            for (int e = 0; e < 4; ++e) o[e] = pk((bflo(v[ks][e]) - mean) * rs, (bfhi(v[ks][e]) - mean) * rs);
            dst[ks * 64] = o;
        }
    }
    {
        for (int it = tid; it < 7 * 224; it += 256) {
            const int q = it / 224, dp = it % 224, fg = 7 * idx + q;
            const unsigned* src = (const unsigned*)(w2 + (size_t)(8 * fg) * DM) + dp;
            unsigned r[8];
#pragma unroll
            for (int e = 0; e < 8; ++e) r[e] = src[e * 224];
            u4 lo, hi;
#pragma unroll
            for (int e = 0; e < 4; ++e) {
                lo[e] = (r[2 * e] & 0xffffu) | (r[2 * e + 1] << 16);
                hi[e] = (r[2 * e] >> 16) | (r[2 * e + 1] & 0xffff0000u);
            }
            const int d = 2 * dp;
            u4* dst = (u4*)(ws + OFF_W2F) + ((size_t)(d >> 4) * 56 + (fg >> 2)) * 64 + (fg & 3) * 16 + (d & 15);
            dst[0] = lo; dst[1] = hi;
        }
    }
    {
        const u4 ones = {0xFFFFFFFFu, 0xFFFFFFFFu, 0xFFFFFFFFu, 0xFFFFFFFFu};
        for (int sl = 0; sl < 2; ++sl) {
            const int s = 2 * idx + sl;
            const unsigned sel = 0;
            unsigned char* base = ws + OFF_SLOT + (size_t)s * 65536 + sel * 8192;
            for (int sb = 0; sb < 4; ++sb)
#pragma unroll
                for (int i = 0; i < 2; ++i) {
                    u4 v = ones;
                    const int gidx = tid + 256 * i;
                    if (s == 0 && sb == 0) {
                        const int P = gidx >> 4, q = (gidx >> 3) & 1, j = gidx & 7;
#pragma unroll
                        for (int e = 0; e < 4; ++e) {
                            const int u = 4 * q + e;
                            v[e] = u < 7 ? canon(*(const unsigned*)(mem0 + j * DM + col_of(P, 2 * u))) : 0u;
                        }
                    }
                    *(u4*)(base + sb * 16384 + gidx * 16) = v;
                }
            if (tid == 0) ((unsigned*)(ws + OFF_MISC))[s] = sel;
        }
    }
}

DI void worker_phase1(int w, const __bf16* __restrict__ w1, const __bf16* __restrict__ w3, rsrc_t rs, unsigned ldsb) {
    const int tid = threadIdx.x, lane = tid & 63, wv = __builtin_amdgcn_readfirstlane(tid >> 6);
    const int g = lane >> 4, n = lane & 15;
    const bool typeB = w >= 112;
    const int wl = typeB ? w - 112 : w;
    const int h = wl / 7, blk = wl % 7;
    for (int c = tid; c < DM; c += 256) {
        const u4 a = *(const u4*)(w1 + (size_t)c * FF + 8 * w), b = *(const u4*)(w3 + (size_t)c * FF + 8 * w);
        ldsw128(ldsb + c * 32, u4{a.x, b.x, a.y, b.y});
        ldsw128(ldsb + c * 32 + 16, u4{a.z, b.z, a.w, b.w});
    }
    u4 W[5][14];
#pragma unroll
    for (int t = 0; t < 4; ++t) {
        const int r = typeB ? (16 * t + n) : (32 * (t >> 1) + 8 * (n >> 2) + 4 * (t & 1) + (n & 3));
        const unsigned vo = OFF_WC + (unsigned)(w * 64 + r) * 896u + 16u * g;
#pragma unroll
        for (int ks = 0; ks < 14; ++ks) W[t][ks] = ld128<0>(rs, vo + 64 * ks, 0);
    }
    wg_barrier();
    {
        const unsigned base = ldsb + (8 * g + (n >> 2)) * 32 + (n & 3) * 8;
#pragma unroll
        for (int ks = 0; ks < 14; ++ks) {
            const u2 lo = trread(base + ks * 1024), hi = trread(base + ks * 1024 + 128);
            W[4][ks] = u4{lo.x, lo.y, hi.x, hi.y};
        }
    }
    int tok[2];
#pragma unroll
    for (int ab = 0; ab < 2; ++ab) tok[ab] = typeB ? (8 * (n >> 2) + 4 * ab + (n & 3)) : (16 * ab + n);
    unsigned xo[2], ho[2];
#pragma unroll
    for (int ab = 0; ab < 2; ++ab) {
        xo[ab] = OFF_XNF + (tok[ab] >> 4) * 14336u + (g * 16 + (tok[ab] & 15)) * 16u;
        ho[ab] = OFF_HF + (tok[ab] >> 4) * 57344u + (unsigned)w * 256u + (tok[ab] & 15) * 16u + 4u * g;
    }
    const unsigned ko = OFF_KQF + (unsigned)(h * 14 + 2 * blk) * 1024u + lane * 16u;
    const unsigned vo = OFF_VOT + (unsigned)(h * 448 + 64 * blk + n) * 64u + g * 16u;
    for (int r = 0; r < 16; ++r) {
        const int seg = 4 * r + wv;
        f4 acc[5][2];
#pragma unroll
        for (int t = 0; t < 5; ++t) { acc[t][0] = f4{0.f, 0.f, 0.f, 0.f}; acc[t][1] = f4{0.f, 0.f, 0.f, 0.f}; }
        u4 xf[2][14];
#pragma unroll
        for (int ks = 0; ks < 14; ++ks) {
            xf[0][ks] = ld128<0>(rs, xo[0] + ks * 1024, seg * 28672u);
            xf[1][ks] = ld128<0>(rs, xo[1] + ks * 1024, seg * 28672u);
        }
        if (typeB) {
#pragma unroll
            for (int ks = 0; ks < 14; ++ks)
#pragma unroll
                for (int ab = 0; ab < 2; ++ab) {
#pragma unroll
                    for (int t = 0; t < 4; ++t) acc[t][ab] = mfma(xf[ab][ks], W[t][ks], acc[t][ab]);
                    acc[4][ab] = mfma(W[4][ks], xf[ab][ks], acc[4][ab]);
                }
#pragma unroll
            for (int t = 0; t < 4; ++t) {
                const f4 a = acc[t][0], b = acc[t][1];
                st128<SC1>(u4{pk(a[0], a[1]), pk(a[2], a[3]), pk(b[0], b[1]), pk(b[2], b[3])}, rs, vo + t * 1024, seg * 458752u);
            }
        } else {
#pragma unroll
            for (int ks = 0; ks < 14; ++ks)
#pragma unroll
                for (int ab = 0; ab < 2; ++ab) {
#pragma unroll
                    for (int t = 0; t < 5; ++t) acc[t][ab] = mfma(W[t][ks], xf[ab][ks], acc[t][ab]);
                }
#pragma unroll
            for (int p = 0; p < 2; ++p)
#pragma unroll
                for (int ab = 0; ab < 2; ++ab) {
                    const f4 a = acc[2 * p][ab], b = acc[2 * p + 1][ab];
                    st128<SC1>(u4{pk(a[0], a[1]), pk(a[2], a[3]), pk(b[0], b[1]), pk(b[2], b[3])}, rs, ko + p * 1024 + ab * 229376u, seg * 458752u);
                }
        }
#pragma unroll
        for (int ab = 0; ab < 2; ++ab) {
            const f4 a = acc[4][ab];
            st32<SC1>(pk(silu(a[0]) * a[2], silu(a[1]) * a[3]), rs, ho[ab], seg * 114688u);
        }
    }
}

__global__ void __launch_bounds__(256, 1) k2_workers(const __bf16* __restrict__ w1, const __bf16* __restrict__ w3, unsigned char* __restrict__ ws) {
    __shared__ __attribute__((aligned(16))) unsigned char smem[DM * 32];
    const int xcd = blockIdx.x & 7, idx = blockIdx.x >> 3;
    if (xcd == 0) return;
    worker_phase1((xcd - 1) * 32 + idx, w1, w3, mk_rsrc(ws, WS_BYTES), (unsigned)(uintptr_t)(LDS_AS unsigned char*)smem);
}

constexpr unsigned L_IMG = 0;
constexpr unsigned L_TT = 7168;
constexpr unsigned L_ZERO = L_TT + 224 * 16;
constexpr unsigned L_UL = L_TT + 240 * 16;
constexpr unsigned L_RED = L_UL + 4096;
constexpr unsigned L_WL = L_RED + 2048;
constexpr unsigned L_REC_BYTES = L_WL + 5 * 14336;

template <int W> struct RecCfg;
template <> struct RecCfg<0> { enum { A0 = 0, NA = 4, B0 = 0, NBR = 2, NBL = 1, L0 = 0 }; };
template <> struct RecCfg<1> { enum { A0 = 4, NA = 4, B0 = 3, NBR = 2, NBL = 1, L0 = 1 }; };
template <> struct RecCfg<2> { enum { A0 = 8, NA = 3, B0 = 6, NBR = 3, NBL = 1, L0 = 2 }; };
template <> struct RecCfg<3> { enum { A0 = 11, NA = 3, B0 = 10, NBR = 2, NBL = 2, L0 = 3 }; };

template <int W>
DI void rec_wave(const int P, rsrc_t rs, const unsigned lb) {
    typedef RecCfg<W> C;
    constexpr int NA = C::NA, NBR = C::NBR, NBL = C::NBL, NB = NBR + NBL;
    const int lane = threadIdx.x & 63, g = lane >> 4, n = lane & 15;
    const int hA = P >> 1, kh = P & 1;
    u8v WA[NA][7], WB[NBR][7];
#pragma unroll
    for (int t = 0; t < NA; ++t) {
        const unsigned vo = OFF_WC + (unsigned)(448 * hA + 224 * kh + 16 * (C::A0 + t) + n) * 896u + 16u * g;
#pragma unroll
        for (int k = 0; k < 7; ++k) WA[t][k] = cat(ld128<0>(rs, vo + 128 * k, 0), ld128<0>(rs, vo + 128 * k + 64, 0));
    }
    unsigned ulo[NB];
#pragma unroll
    for (int t = 0; t < NB; ++t) {
        const int id = 16 * (C::B0 + t) + n, h = id / 14, sg = id % 14;
        const unsigned vo = OFF_WC + (unsigned)(7168 + 448 * h + col_of(P, sg)) * 896u + 16u * g;
        ulo[t] = lb + L_UL + (((h >> 1) * 2 + (g >> 1)) * 16 + sg) * 16 + (2 * (g & 1) + (h & 1)) * 4;
#pragma unroll
        for (int k = 0; k < 7; ++k) {
            const u4 lo = ld128<0>(rs, vo + 128 * k, 0), hi = ld128<0>(rs, vo + 128 * k + 64, 0);
            if (t < NBR) WB[t < NBR ? t : 0][k] = cat(lo, hi);
            else {
                ldsw128(lb + L_WL + (C::L0 + t - NBR) * 14336 + k * 2048 + lane * 16, lo);
                ldsw128(lb + L_WL + (C::L0 + t - NBR) * 14336 + k * 2048 + 1024 + lane * 16, hi);
            }
        }
    }
    if (W == 0) {
        if (lane < 16) ldsw128(lb + L_ZERO + lane * 16, u4{0, 0, 0, 0});
#pragma unroll
        for (int i = 0; i < 4; ++i) ldsw128(lb + L_UL + i * 1024 + lane * 16, u4{0, 0, 0, 0});
    }
    const unsigned long long selm = __ballot(ld32<0>(rs, OFF_MISC + lane * 4, 0) != 0);
    const int idxT = (n & 2) ? 0xEEEE : 0x4444;
    const int tid = W * 64 + lane;
    const unsigned g_mem = SLOT_MEM + (tid >> 3) * 256 + (tid & 7) * 16;
    const int gj = tid & 7, gP = tid >> 3;
    const int grho = 4 * (gj >> 1) + 2 * (gP & 1) + (gj & 1);
    const unsigned l_q0 = lb + L_IMG + 256 * (gP >> 1) + 16 * grho;
    const unsigned l_q1 = lb + L_IMG + 256 * (16 + 3 * (gP >> 3)) + 16 * grho + 4 * ((gP >> 1) & 3);
    const unsigned l_af = lb + L_IMG + lane * 16;
    const unsigned l_tt = lb + L_TT + (16 * C::A0 + n) * 16 + 4 * g;
    const unsigned g_ug = OFF_UG + (unsigned)P * 3584u + (16 * C::B0 + n) * 16u + 4u * g;
    const int xe = (g >> 1) ^ (n >> 3);
    const int ecu = 2 * (4 * W + xe) + (g >> 1);
    const unsigned g_e = SLOT_E + (ecu >> 4) * 16384 + (ecu & 15) * 512 + 32 * (n & 7) + 16 * (g & 1);
    const int xs = (n >> 1) & 1, is = 2 * (n >> 2) + (n & 1);
    const int hl = 4 * W + 2 * (g >> 1) + xs;
    const int mcu = 2 * hl + ((g & 1) ^ xs);
    const unsigned g_ml = SLOT_E + (mcu >> 4) * 16384 + (mcu & 15) * 512 + 256 + 8 * is;
    const unsigned g_s = SLOT_S + 512 * hl + 32 * is + 16 * (g & 1);
    const int idxO = (is & 2) ? 0xEEEE : 0x4444, idxU = xs ? 0xEE : 0x44;
    const unsigned hm = (is & 1) ? 0xFFFF0000u : 0x0000FFFFu;
    const int qstar = 2 * (is >> 2) + xs;
    const u4 fmask = {qstar == 0 ? hm : 0u, qstar == 1 ? hm : 0u, qstar == 2 ? hm : 0u, qstar == 3 ? hm : 0u};
    const unsigned em0 = xe == 0 ? 0xFFFFFFFFu : 0u, em1 = ~em0;
    const unsigned g_vo = OFF_VOT + (unsigned)(4 * W * 448 + col_of(P, n < 14 ? n : 13)) * 64u + 16u * g;
    const unsigned g_kq = OFF_KQF + (unsigned)(kh * 16 + hA) * 14336u + lane * 16u;
    const unsigned l_uop = lb + L_UL + W * 1024 + lane * 16;
    const unsigned l_redw = lb + L_RED + W * 512 + (2 * g) * 64 + n * 4;
    int budget = 1 << 20;

    u4 VO[4], VOn[4];
    u8v KQ[7];
#pragma unroll
    for (int hh = 0; hh < 4; ++hh) VOn[hh] = ld128<SC1>(rs, g_vo + hh * 28672, 0);
    if (W == 3) {
#pragma unroll
        for (int k = 0; k < 7; ++k) KQ[k] = cat(ld128<SC1>(rs, g_kq + 2048 * k, 0), ld128<SC1>(rs, g_kq + 2048 * k + 1024, 0));
    }

    for (int s = 0; s < NSEG; ++s) {
        const unsigned slot = OFF_SLOT + (unsigned)s * 65536u + (unsigned)((selm >> s) & 1) * 8192u;
        {
            u4 q0, q1;
            for (;;) {
                q0 = ld128<SC1>(rs, g_mem, slot);
                q1 = ld128<SC1>(rs, g_mem + 128, slot);
                const bool bad = max(umax4(q0), umax4(q1)) == 0xFFFFFFFFu;
                if (!__any(bad) || --budget < 0) break;
                asm volatile("" ::: "memory");
            }
            ldsw128(l_q0, q0);
            ldsw32(l_q1, q1.x); ldsw32(l_q1 + 256, q1.y); ldsw32(l_q1 + 512, q1.z);
        }
        wg_barrier();
        u4 af[7];
#pragma unroll
        for (int k = 0; k < 7; ++k) af[k] = ldsr128(l_af + k * 1024);
        const int sn = s + 1 < NSEG ? s + 1 : s;
#pragma unroll
        for (int hh = 0; hh < 4; ++hh) { VO[hh] = VOn[hh]; VOn[hh] = ld128<SC1>(rs, g_vo + hh * 28672, sn * 458752u); }
        if (W == 3) {
            f4 c = {0.f, 0.f, 0.f, 0.f};
#pragma unroll
            for (int k = 0; k < 7; ++k) c = smf64(af[k], KQ[k], c, idxT);
#pragma unroll
            for (int k = 0; k < 7; ++k) KQ[k] = cat(ld128<SC1>(rs, g_kq + 2048 * k, sn * 458752u), ld128<SC1>(rs, g_kq + 2048 * k + 1024, sn * 458752u));
            const float s0 = c[0] + c[2], s1 = c[1] + c[3];
            float m0 = fmaxf(s0, dppf<0x128>(s0)), m1 = fmaxf(s1, dppf<0x128>(s1));
            m0 = fmaxf(m0, dppf<0x124>(m0)); m1 = fmaxf(m1, dppf<0x124>(m1));
            m0 = fmaxf(m0, dppf<0x4E>(m0)); m1 = fmaxf(m1, dppf<0x4E>(m1));
            m0 = fmaxf(m0, dppf<0xB1>(m0)); m1 = fmaxf(m1, dppf<0xB1>(m1));
            const float e0 = ex2(s0 - m0), e1 = ex2(s1 - m1);
            const bool odd = n & 1;
            const float y = dppf<0xB1>(odd ? e0 : e1);
            const unsigned ev = canon(odd ? pk(y, e1) : pk(e0, y));
            const unsigned eb = SLOT_E + (P >> 4) * 16384 + (P & 15) * 512;
            st32<SC1>(ev, rs, eb + 32 * (2 * g + (n & 1)) + 4 * (n >> 1), slot);
            float l0 = e0 + dppf<0x128>(e0), l1 = e1 + dppf<0x128>(e1);
            l0 += dppf<0x124>(l0); l1 += dppf<0x124>(l1);
            l0 += dppf<0x4E>(l0); l1 += dppf<0x4E>(l1);
            l0 += dppf<0xB1>(l0); l1 += dppf<0xB1>(l1);
            if (n < 2) st64<SC1>(u2{canon(fbits(odd ? m1 : m0)), canon(fbits(odd ? l1 : l0))}, rs, eb + 256 + 8 * (2 * g + n), slot);
        }
        {
            f4 c[NA];
#pragma unroll
            for (int t = 0; t < NA; ++t) c[t] = f4{0.f, 0.f, 0.f, 0.f};
#pragma unroll
            for (int k = 0; k < 7; ++k)
#pragma unroll
                for (int t = 0; t < NA; ++t) c[t] = smf64(af[k], WA[t][k], c[t], idxT);
#pragma unroll
            for (int t = 0; t < NA; ++t) ldsw32(l_tt + t * 256, pk(c[t][0] + c[t][2], c[t][1] + c[t][3]));
        }
        wg_barrier();
        if (W == 2) {
            const int kp = (n >> 1) & 1;
            const unsigned la = lb + L_IMG + 256 * (14 * kh + 7 * kp + (g >> 1)) + 16 * (4 * (n >> 2) + 2 * (g & 1) + (n & 1));
            const int q = n >> 2, p = n & 3;
            const unsigned lt = lb + L_TT + (16 * (7 * (p >> 1) + (g >> 1)) + 4 * (q >> 1) + 2 * (g & 1) + (q & 1)) * 16 + 8 * (p & 1);
            f4 c = {0.f, 0.f, 0.f, 0.f};
#pragma unroll
            for (int st = 0; st < 4; ++st) {
                const u4 a = ldsr128((st == 3 && g >= 2) ? lb + L_ZERO : la + st * 512);
                const u2 lo = trread(lt + st * 512), hi = trread(lt + st * 512 + 128);
                c = mfma(a, u4{lo.x, lo.y, hi.x, hi.y}, c);
            }
            const float xx = n < 8 ? c[0] : c[3], yy = n < 8 ? c[1] : c[2];
            st32<SC1>(canon(fbits(xx + dppf<0x128>(yy))), rs, SLOT_S + P * 256 + lane * 4, slot);
        }
        {
            f4 c[NB];
#pragma unroll
            for (int t = 0; t < NB; ++t) c[t] = f4{0.f, 0.f, 0.f, 0.f};
#pragma unroll
            for (int k = 0; k < 7; ++k) {
#pragma unroll
                for (int t = 0; t < NBR; ++t) c[t] = smf64(af[k], WB[t][k], c[t], idxT);
#pragma unroll
                for (int t = 0; t < NBL; ++t) {
                    const unsigned o = lb + L_WL + (C::L0 + t) * 14336 + k * 2048 + lane * 16;
                    c[NBR + t] = smf64(af[k], cat(ldsr128(o), ldsr128(o + 1024)), c[NBR + t], idxT);
                }
            }
#pragma unroll
            for (int t = 0; t < NB; ++t) {
                const unsigned v = pk(c[t][0] + c[t][2], c[t][1] + c[t][3]);
                ldsw32(ulo[t], v);
                st32<SC1>(v, rs, g_ug + t * 256, s * 114688u);
            }
        }
        if (W == 0) {
            const int q = n >> 2, p = n & 3;
            const unsigned lt = lb + L_TT + (112 * (p >> 1) + 8 * g + q) * 16 + 8 * (p & 1);
            const unsigned go = OFF_KQM + (unsigned)(hA * 8 + (n & 7)) * 896u + (224 * kh + 112 * (n >> 3) + 8 * g) * 2u;
#pragma unroll
            for (int st = 0; st < 4; ++st) {
                const u2 lo = trread(lt + st * 512), hi = trread(lt + st * 512 + 64);
                if (st < 3 || g < 2) st128<SC1>(u4{lo.x, lo.y, hi.x, hi.y}, rs, go + st * 64, s * 114688u);
            }
        }
        if (s == NSEG - 1) break;
        wg_barrier();
        const u4 uop = ldsr128(l_uop);
        u8v oop;
        float mc, lc;
        {
            u4 e0, e1; u2 ml;
            for (;;) {
                e0 = ld128<SC1>(rs, g_e, slot);
                e1 = ld128<SC1>(rs, g_e + 2048, slot);
                ml = ld64<SC1>(rs, g_ml, slot);
                const bool bad = max(max(umax4(e0), umax4(e1)), max(ml.x, ml.y)) == 0xFFFFFFFFu;
                if (!__any(bad) || --budget < 0) break;
                asm volatile("" ::: "memory");
            }
            mc = bitsf(ml.x); lc = bitsf(ml.y);
            const f4 z = {0.f, 0.f, 0.f, 0.f};
            const f4 o0 = mfma(e0 & em0, VO[0], z), o1 = mfma(e0 & em1, VO[1], z), o2 = mfma(e1 & em0, VO[2], z), o3 = mfma(e1 & em1, VO[3], z);
            oop = u8v{pk(o0[0], o0[1]), pk(o0[2], o0[3]), pk(o1[0], o1[1]), pk(o1[2], o1[3]),
                      pk(o2[0], o2[1]), pk(o2[2], o2[3]), pk(o3[0], o3[1]), pk(o3[2], o3[3])};
        }
        {
            u4 sa, sb;
            for (;;) {
                sa = ld128<SC1>(rs, g_s, slot);
                sb = ld128<SC1>(rs, g_s + 256, slot);
                const bool bad = max(umax4(sa), umax4(sb)) == 0xFFFFFFFFu;
                if (!__any(bad) || --budget < 0) break;
                asm volatile("" ::: "memory");
            }
            const f4 sv = __builtin_bit_cast(f4, sa) + __builtin_bit_cast(f4, sb);
            const float ml_ = fmaxf(fmaxf(fmaxf(sv[0], sv[1]), fmaxf(sv[2], sv[3])), mc);
            const float p0 = ex2(sv[0] - ml_), p1 = ex2(sv[1] - ml_), p2 = ex2(sv[2] - ml_), p3 = ex2(sv[3] - ml_), be = ex2(mc - ml_);
            const float sum = ((p0 + p1) + (p2 + p3)) + be * lc;
            const auto mm = __builtin_amdgcn_permlane16_swap(fbits(ml_), fbits(ml_), false, false);
            const auto ss = __builtin_amdgcn_permlane16_swap(fbits(sum), fbits(sum), false, false);
            const float we = ex2(bitsf(mm[0]) - ml_), wo = ex2(bitsf(mm[1]) - ml_);
            const float r = __builtin_amdgcn_rcpf(we * bitsf(ss[0]) + wo * bitsf(ss[1]));
            const float f = be * r;
            const unsigned fd = pk(f, f);
            f4 c = {0.f, 0.f, 0.f, 0.f};
            c = smf32(u2{pk(p0 * r, p1 * r), pk(p2 * r, p3 * r)}, uop, c, idxU);
            c = smf64(fmask & fd, oop, c, idxO);
            ldsw32(l_redw, fbits(c[0] + c[2]));
            ldsw32(l_redw + 64, fbits(c[1] + c[3]));
        }
        wg_barrier();
        if (W == 0) {
            const int i = lane >> 3, u = lane & 7;
            const unsigned o = lb + L_RED + i * 64 + u * 8;
            const f2 a = __builtin_bit_cast(f2, ldsr64(o)), b = __builtin_bit_cast(f2, ldsr64(o + 512));
            const f2 c = __builtin_bit_cast(f2, ldsr64(o + 1024)), d = __builtin_bit_cast(f2, ldsr64(o + 1536));
            const f2 t = (a + b) + (c + d);
            const unsigned nslot = OFF_SLOT + (unsigned)(s + 1) * 65536u + (unsigned)((selm >> (s + 1)) & 1) * 8192u;
            st32<SC1>(canon(pk(t[0], t[1])), rs, SLOT_MEM + P * 256 + (u >> 2) * 128 + i * 16 + (u & 3) * 4, nslot);
        }
    }
}

__global__ void __launch_bounds__(256, 1) k3_rec(unsigned char* __restrict__ ws) {
    __shared__ __attribute__((aligned(16))) unsigned char smem[L_REC_BYTES];
    const int xcd = blockIdx.x & 7, P = blockIdx.x >> 3;
    if (xcd != 0) return;
    const rsrc_t rs = mk_rsrc(ws, WS_BYTES);
    const unsigned lb = (unsigned)(uintptr_t)(LDS_AS unsigned char*)smem;
    const int wv = __builtin_amdgcn_readfirstlane(threadIdx.x >> 6);
    if (wv == 0) rec_wave<0>(P, rs, lb);
    else if (wv == 1) rec_wave<1>(P, rs, lb);
    else if (wv == 2) rec_wave<2>(P, rs, lb);
    else rec_wave<3>(P, rs, lb);
}

constexpr unsigned L_OUT_BYTES = 2 * 4 * 14 * 1024;

DI void out_task(int seg, int cg, const __bf16* __restrict__ x, __bf16* __restrict__ out, rsrc_t rs, unsigned lb) {
    const int tid = threadIdx.x, lane = tid & 63, wv = __builtin_amdgcn_readfirstlane(tid >> 6);
    const int g = lane >> 4, n = lane & 15;
    const f4 z = {0.f, 0.f, 0.f, 0.f};
    {
        f4 acc[7][2];
#pragma unroll
        for (int dt = 0; dt < 7; ++dt) { acc[dt][0] = z; acc[dt][1] = z; }
        const unsigned ho = OFF_HF + (unsigned)(2 * seg * 56 + 14 * wv) * 1024u + lane * 16u;
        const unsigned wo = OFF_W2F + (unsigned)(7 * cg * 56 + 14 * wv) * 1024u + lane * 16u;
#pragma unroll 2
        for (int ks = 0; ks < 14; ++ks) {
            const u4 h0 = ld128<SC1>(rs, ho + ks * 1024, 0), h1 = ld128<SC1>(rs, ho + ks * 1024, 57344);
#pragma unroll
            for (int dt = 0; dt < 7; ++dt) {
                const u4 a = ld128<0>(rs, wo + ks * 1024, dt * 57344);
                acc[dt][0] = mfma(a, h0, acc[dt][0]);
                acc[dt][1] = mfma(a, h1, acc[dt][1]);
            }
        }
#pragma unroll
        for (int dt = 0; dt < 7; ++dt)
#pragma unroll
            for (int tt = 0; tt < 2; ++tt) ldsw128(lb + (wv * 14 + dt * 2 + tt) * 1024 + lane * 16, __builtin_bit_cast(u4, acc[dt][tt]));
    }
    u4 xq[2][14];
#pragma unroll
    for (int tt = 0; tt < 2; ++tt)
#pragma unroll
        for (int ks = 0; ks < 14; ++ks) xq[tt][ks] = ld128<0>(rs, OFF_XNF + (tt * 14 + ks) * 1024 + lane * 16, seg * 28672u);
    f4 sx[4][2][2];
    constexpr float NEG = -1.0e9f * 1.4426950408889634f;
#pragma unroll
    for (int hh = 0; hh < 4; ++hh) {
#pragma unroll
        for (int ab = 0; ab < 2; ++ab) {
            const int tok = 8 * (n >> 2) + 4 * ab + (n & 3);
            const unsigned ko = OFF_KQF + (unsigned)((tok >> 4) * 16 + 4 * wv + hh) * 14336u + (g * 16 + (tok & 15)) * 16u;
            f4 c0 = z, c1 = z;
#pragma unroll
            for (int ks = 0; ks < 14; ++ks) {
                const u4 a = ld128<SC1>(rs, ko + ks * 1024, seg * 458752u);
                c0 = mfma(a, xq[0][ks], c0);
                c1 = mfma(a, xq[1][ks], c1);
            }
#pragma unroll
            for (int r = 0; r < 4; ++r) {
                const int key = 8 * g + 4 * ab + r;
                c0[r] = key <= n ? c0[r] : NEG;
                c1[r] = key <= 16 + n ? c1[r] : NEG;
            }
            sx[hh][0][ab] = c0; sx[hh][1][ab] = c1;
        }
    }
    f4 sm[2][2];
#pragma unroll
    for (int ab = 0; ab < 2; ++ab) {
        const unsigned to = OFF_KQM + (unsigned)((4 * wv + (n >> 2)) * 8 + 4 * ab + (n & 3)) * 896u + g * 16u;
        f4 c0 = z, c1 = z;
#pragma unroll
        for (int ks = 0; ks < 14; ++ks) {
            const u4 a = ld128<SC1>(rs, to + ks * 64, seg * 114688u);
            c0 = mfma(a, xq[0][ks], c0);
            c1 = mfma(a, xq[1][ks], c1);
        }
        sm[0][ab] = c0; sm[1][ab] = c1;
    }
    u4 px[4][2], pm[2];
#pragma unroll
    for (int tt = 0; tt < 2; ++tt) {
        const f4 ma = sm[tt][0], mb = sm[tt][1];
        const float mmax = fmaxf(fmaxf(fmaxf(ma[0], ma[1]), fmaxf(ma[2], ma[3])), fmaxf(fmaxf(mb[0], mb[1]), fmaxf(mb[2], mb[3])));
        float mown = 0.f, rown = 0.f;
#pragma unroll
        for (int hh = 0; hh < 4; ++hh) {
            const f4 a = sx[hh][tt][0], b = sx[hh][tt][1];
            float m = fmaxf(fmaxf(fmaxf(a[0], a[1]), fmaxf(a[2], a[3])), fmaxf(fmaxf(b[0], b[1]), fmaxf(b[2], b[3])));
            if (g == hh) m = fmaxf(m, mmax);
            m = fmaxf(m, __shfl_xor(m, 16));
            m = fmaxf(m, __shfl_xor(m, 32));
            float e[8];
#pragma unroll
            for (int r = 0; r < 4; ++r) { e[r] = ex2(a[r] - m); e[4 + r] = ex2(b[r] - m); }
            float l = ((e[0] + e[1]) + (e[2] + e[3])) + ((e[4] + e[5]) + (e[6] + e[7]));
            if (g == hh) {
#pragma unroll
                for (int r = 0; r < 4; ++r) l += ex2(ma[r] - m) + ex2(mb[r] - m);
            }
            l += __shfl_xor(l, 16);
            l += __shfl_xor(l, 32);
            const float rr = 1.0f / l;
            if (g == hh) { mown = m; rown = rr; }
            px[hh][tt] = u4{pk(e[0] * rr, e[1] * rr), pk(e[2] * rr, e[3] * rr), pk(e[4] * rr, e[5] * rr), pk(e[6] * rr, e[7] * rr)};
        }
        pm[tt] = u4{pk(ex2(ma[0] - mown) * rown, ex2(ma[1] - mown) * rown), pk(ex2(ma[2] - mown) * rown, ex2(ma[3] - mown) * rown),
                    pk(ex2(mb[0] - mown) * rown, ex2(mb[1] - mown) * rown), pk(ex2(mb[2] - mown) * rown, ex2(mb[3] - mown) * rown)};
    }
    {
#pragma unroll
        for (int dt = 0; dt < 7; ++dt) {
            const int d = 112 * cg + 16 * dt + n;
            f4 c0 = z, c1 = z;
#pragma unroll
            for (int hh = 0; hh < 4; ++hh) {
                const u4 a = ld128<SC1>(rs, OFF_VOT + (unsigned)((4 * wv + hh) * 448 + d) * 64u + g * 16u, seg * 458752u);
                c0 = mfma(a, px[hh][0], c0);
                c1 = mfma(a, px[hh][1], c1);
            }
            const int cp = d >> 4, m = (d >> 2) & 3, hf = (d >> 1) & 1, b = d & 1;
            const int rel = cp - 16;
            const int Pp = cp < 16 ? cp : (4 * (rel / 3) + m), u = cp < 16 ? m : 4 + rel % 3;
            const u4 a = ld128<SC1>(rs, OFF_UG + (unsigned)(2 * Pp + hf) * 3584u + (unsigned)(14 * (4 * wv + g) + 2 * u + b) * 16u, seg * 114688u);
            c0 = mfma(a, pm[0], c0);
            c1 = mfma(a, pm[1], c1);
            ldsw128(lb + 57344 + (wv * 14 + dt * 2) * 1024 + lane * 16, __builtin_bit_cast(u4, c0));
            ldsw128(lb + 57344 + (wv * 14 + dt * 2 + 1) * 1024 + lane * 16, __builtin_bit_cast(u4, c1));
        }
    }
    wg_barrier();
    for (int it = tid; it < 14 * 64; it += 256) {
        f4 gs = z, ss = z;
#pragma unroll
        for (int w = 0; w < 4; ++w) {
            gs += __builtin_bit_cast(f4, ldsr128(lb + w * 14336 + it * 16));
            ss += __builtin_bit_cast(f4, ldsr128(lb + 57344 + w * 14336 + it * 16));
        }
        const int tl = it >> 6, l = it & 63;
        const int t = 32 * seg + 16 * (tl & 1) + (l & 15), d = 112 * cg + 16 * (tl >> 1) + 4 * (l >> 4);
        const u2 xv = *(const u2*)(x + (size_t)t * DM + d);
        const float o0 = bflo(xv.x) + ss[0] * silu(gs[0]), o1 = bfhi(xv.x) + ss[1] * silu(gs[1]);
        const float o2 = bflo(xv.y) + ss[2] * silu(gs[2]), o3 = bfhi(xv.y) + ss[3] * silu(gs[3]);
        *(u2*)(out + (size_t)t * DM + d) = u2{pk(o0, o1), pk(o2, o3)};
    }
}

__global__ void __launch_bounds__(256, 1) k4_out(const __bf16* __restrict__ x, __bf16* __restrict__ out, unsigned char* __restrict__ ws) {
    __shared__ __attribute__((aligned(16))) unsigned char smem[L_OUT_BYTES];
    out_task(blockIdx.x >> 2, blockIdx.x & 3, x, out, mk_rsrc(ws, WS_BYTES), (unsigned)(uintptr_t)(LDS_AS unsigned char*)smem);
}

extern "C" void kernel_launch(void* const* d_in, const int* in_sizes, int n_in, void* d_out, int out_size, void* d_ws, size_t ws_size,
                              hipStream_t stream) {
    const __bf16* x = (const __bf16*)d_in[0];
    const __bf16* mem0 = (const __bf16*)d_in[1];
    const __bf16* Wq = (const __bf16*)d_in[2];
    const __bf16* Wk = (const __bf16*)d_in[3];
    const __bf16* Wv = (const __bf16*)d_in[4];
    const __bf16* Wo = (const __bf16*)d_in[5];
    const __bf16* w1 = (const __bf16*)d_in[8];
    const __bf16* w3 = (const __bf16*)d_in[9];
    const __bf16* w2 = (const __bf16*)d_in[10];
    unsigned char* ws = (unsigned char*)(((uintptr_t)d_ws + 65535) & ~(uintptr_t)65535);
    k1_prep<<<256, 256, 0, stream>>>(x, mem0, Wq, Wk, Wv, Wo, w2, ws);
    k2_workers<<<256, 256, 0, stream>>>(w1, w3, ws);
    k3_rec<<<256, 256, 0, stream>>>(ws);
    k4_out<<<256, 256, 0, stream>>>(x, (__bf16*)d_out, ws);
}
```

```cpp
#include <hip/hip_runtime.h>
#include <hip/hip_bf16.h>
#include <stdint.h>

typedef __bf16 bf4 __attribute__((ext_vector_type(4)));
typedef __bf16 bf8 __attribute__((ext_vector_type(8)));
typedef __bf16 bf16v __attribute__((ext_vector_type(16)));
typedef __bf16 bf2 __attribute__((ext_vector_type(2)));
typedef short s4 __attribute__((ext_vector_type(4)));
typedef short s8 __attribute__((ext_vector_type(8)));
typedef float f4 __attribute__((ext_vector_type(4)));
typedef float f2 __attribute__((ext_vector_type(2)));
typedef unsigned u4 __attribute__((ext_vector_type(4)));
typedef unsigned u2 __attribute__((ext_vector_type(2)));
typedef unsigned u8v __attribute__((ext_vector_type(8)));
typedef __amdgpu_buffer_rsrc_t rsrc_t;
#define DI __device__ __forceinline__
#define LDS_AS __attribute__((address_space(3)))

constexpr int DM = 448, NH = 16, FF = 1792, NSEG = 64;
constexpr int SC1 = 16;

constexpr unsigned OFF_SLOT = 0;
constexpr unsigned OFF_MISC = 64u << 20;
constexpr unsigned OFF_FLAG = OFF_MISC + 65536u;
constexpr unsigned OFF_RFL = OFF_MISC + 262144u;
constexpr unsigned OFF_SIDE = OFF_MISC + (1u << 20);
constexpr unsigned OFF_WC = OFF_SIDE + 64u * 262144u;
constexpr unsigned OFF_XNF = OFF_WC + 448u * 28672u;
constexpr unsigned OFF_KQF = OFF_XNF + 128u * 14u * 1024u;
constexpr unsigned OFF_VOT = OFF_KQF + 128u * 16u * 14u * 1024u;
constexpr unsigned OFF_HF = OFF_VOT + 64u * 16u * 28u * 1024u;
constexpr unsigned OFF_SXX = OFF_HF + 128u * 56u * 1024u;
constexpr unsigned OFF_G = OFF_SXX + 64u * 16u * 3u * 1024u;
constexpr unsigned WS_BYTES = OFF_G + 64u * 4u * 14u * 1024u;
static_assert(OFF_XNF == 97779712u && OFF_HF == 158334976u, "layout");
constexpr unsigned OOR = 0x40000000u;

#define HCLS 0
constexpr unsigned BLK = 32768, XFLIP = 0x84000;
constexpr unsigned B_MEMR = 0, B_S = 2 * BLK, B_ML = 4 * BLK;
constexpr unsigned B_MEMH = HCLS ? 21 * BLK : 14 * BLK, B_TRG = B_MEMH + BLK;
__host__ __device__ constexpr unsigned oblk(int q) { return (unsigned)q * BLK; }

DI rsrc_t mk_rsrc(const void* p, unsigned bytes) { return __builtin_amdgcn_make_buffer_rsrc((void*)p, 0, (int)bytes, 0x00020000); }
template <int AUX> DI u4 ld128(rsrc_t r, unsigned vo, unsigned so) { return __builtin_amdgcn_raw_buffer_load_b128(r, (int)vo, (int)so, AUX); }
template <int AUX> DI u2 ld64(rsrc_t r, unsigned vo, unsigned so) { return __builtin_amdgcn_raw_buffer_load_b64(r, (int)vo, (int)so, AUX); }
template <int AUX> DI unsigned ld32(rsrc_t r, unsigned vo, unsigned so) { return __builtin_amdgcn_raw_buffer_load_b32(r, (int)vo, (int)so, AUX); }
template <int AUX> DI void st128(u4 v, rsrc_t r, unsigned vo, unsigned so) { __builtin_amdgcn_raw_buffer_store_b128(v, r, (int)vo, (int)so, AUX); }
template <int AUX> DI void st64(u2 v, rsrc_t r, unsigned vo, unsigned so) { __builtin_amdgcn_raw_buffer_store_b64(v, r, (int)vo, (int)so, AUX); }
template <int AUX> DI void st32(unsigned v, rsrc_t r, unsigned vo, unsigned so) { __builtin_amdgcn_raw_buffer_store_b32(v, r, (int)vo, (int)so, AUX); }

DI unsigned pk(float a, float b) { bf2 v = {(__bf16)a, (__bf16)b}; return __builtin_bit_cast(unsigned, v); }
DI float bflo(unsigned v) { return __builtin_bit_cast(float, v << 16); }
DI float bfhi(unsigned v) { return __builtin_bit_cast(float, v & 0xffff0000u); }
DI f4 mfma(u4 a, u4 b, f4 c) { return __builtin_amdgcn_mfma_f32_16x16x32_bf16(__builtin_bit_cast(bf8, a), __builtin_bit_cast(bf8, b), c, 0, 0, 0); }
DI f4 smf64(u4 a, u8v b, f4 c, int idx) { return __builtin_amdgcn_smfmac_f32_16x16x64_bf16(__builtin_bit_cast(bf8, a), __builtin_bit_cast(bf16v, b), c, idx, 0, 0); }
DI f4 smf32(u2 a, u4 b, f4 c, int idx) { return __builtin_amdgcn_smfmac_f32_16x16x32_bf16(__builtin_bit_cast(s4, a), __builtin_bit_cast(s8, b), c, idx, 0, 0); }
DI u8v cat(u4 a, u4 b) { return __builtin_shufflevector(a, b, 0, 1, 2, 3, 4, 5, 6, 7); }
DI u2 trread(unsigned lds_off) { return __builtin_bit_cast(u2, __builtin_amdgcn_ds_read_tr16_b64_v4i16((LDS_AS s4*)(uintptr_t)lds_off)); }
DI u4 ldsr128(unsigned o) { return *(LDS_AS u4*)(uintptr_t)o; }
DI u2 ldsr64(unsigned o) { return *(LDS_AS u2*)(uintptr_t)o; }
DI void ldsw128(unsigned o, u4 v) { *(LDS_AS u4*)(uintptr_t)o = v; }
DI void ldsw64(unsigned o, u2 v) { *(LDS_AS u2*)(uintptr_t)o = v; }
DI void ldsw32(unsigned o, unsigned v) { *(LDS_AS unsigned*)(uintptr_t)o = v; }
DI void wg_barrier() {
    __builtin_amdgcn_fence(__ATOMIC_RELEASE, "workgroup", "local");
    __builtin_amdgcn_s_barrier();
    __builtin_amdgcn_fence(__ATOMIC_ACQUIRE, "workgroup", "local");
}
template <int CTRL> DI float dppf(float v) { return __builtin_bit_cast(float, __builtin_amdgcn_update_dpp(0, __builtin_bit_cast(int, v), CTRL, 0xf, 0xf, true)); }
DI float ex2(float x) { return __builtin_amdgcn_exp2f(x); }
DI float silu(float x) { return x * __builtin_amdgcn_rcpf(1.0f + ex2(-1.4426950408889634f * x)); }
DI unsigned fbits(float x) { return __builtin_bit_cast(unsigned, x); }
DI float bitsf(unsigned x) { return __builtin_bit_cast(float, x); }
DI unsigned canon(unsigned v) { return v < 0xFFFFFFFEu ? v : 0xFFFFFFFEu; }
DI unsigned umax4(u4 v) { return max(max(v.x, v.y), max(v.z, v.w)); }

#define KTS(i)
template <class T> DI T ldnt(const T* p) { return __builtin_nontemporal_load(p); }
DI u4 pk8(f4 a, f4 b) { return u4{pk(a[0], a[1]), pk(a[2], a[3]), pk(b[0], b[1]), pk(b[2], b[3])}; }
DI u2 pk4(f4 a) { return u2{pk(a[0], a[1]), pk(a[2], a[3])}; }

#define K1_DEPTH 2
#define K1_PF 3
#define K1_NT 0
#define K1_MAP 1
#define K1_ROT 0
#define K1_IL 2
#define K1_PFN 152
#define K1_NF 3
#define K1_NP 2
#define K1_ST 0
__global__ void __launch_bounds__(256, 1) k1_prep(const __bf16* __restrict__ x, const __bf16* __restrict__ mem0,
                                                  const __bf16* __restrict__ Wq, const __bf16* __restrict__ Wk,
                                                  const __bf16* __restrict__ Wv, const __bf16* __restrict__ Wo,
                                                  unsigned char* __restrict__ ws) {
    __shared__ __attribute__((aligned(16))) unsigned char smem[36864 + 8 * 8448];
    const int tid = threadIdx.x, lane = tid & 63, wv = __builtin_amdgcn_readfirstlane(tid >> 6);
    const int g = lane >> 4, n = lane & 15;
    const int xcd = blockIdx.x & 7, idx = blockIdx.x >> 3;
    const unsigned lb = (unsigned)(uintptr_t)(LDS_AS unsigned char*)smem;
    KTS(0);
    unsigned c1 = 0;
    if (tid < K1_PFN) {
        unsigned plo, phi;
        asm volatile("s_getpc_b64 s[40:41]\n\ts_mov_b32 %0, s40\n\ts_mov_b32 %1, s41" : "=&s"(plo), "=&s"(phi) :: "s40", "s41");
        c1 = *(const __attribute__((address_space(1))) unsigned*)((((uintptr_t)phi << 32) | plo) + tid * 128);
    }
    if (xcd) {
        const int j_ = (xcd - 1) * 8 + (idx >> 2), w = 7 * (4 * (j_ / 7) + (idx & 3)) + j_ % 7;
        const bool typeB = w >= 112;
        const int wl = typeB ? w - 112 : w;
        const int h = wl / 7, r0 = 64 * (wl % 7);
        u4 rs_[8];
        if (typeB) {
            const __bf16* src = Wo + (size_t)h * 256 * DM + r0;
#pragma unroll
            for (int i = 0; i < 8; ++i) { const int id = tid + 256 * i; rs_[i] = ldnt((const u4*)(src + (id >> 3) * DM + (id & 7) * 8)); }
        } else {
            const __bf16* src = Wq + (size_t)r0 * 4096 + h * 256;
#pragma unroll
            for (int i = 0; i < 8; ++i) { const int id = tid + 256 * i; rs_[i] = ldnt((const u4*)(src + (id >> 5) * 4096 + (id & 31) * 8)); }
        }
        const __bf16* Am = (typeB ? Wv : Wk) + (size_t)(112 * wv + (lane >> 5)) * 4096 + h * 256 + (lane & 31) * 8;
        const int rot = K1_ROT ? (wl % 7) % 3 : 0, sw = K1_ROT > 1 ? ((wl % 7) / 3) & 1 : 0;
        auto pp = [&](const int t) { const int p = (t >> 1) + rot; return p >= 3 ? p - 3 : p; };
        constexpr int RD = K1_DEPTH;
        u4 cs[RD][8];
        auto ldt = [&](const int t) {
#pragma unroll
            for (int i = 0; i < 8; ++i) {
                const int c = t < 6 ? 32 * pp(t) + 4 * ((t & 1) ^ sw) + 8 * (i >> 1) + 2 * (i & 1) : 96 + 2 * i;
                cs[t % RD][i] = K1_NT ? ldnt((const u4*)(Am + c * 4096)) : *(const u4*)(Am + c * 4096);
            }
        };
#pragma unroll
        for (int t = 0; t < RD; ++t) ldt(t);
        __builtin_amdgcn_sched_barrier(0);
        {
            const unsigned wa = typeB ? lb + (tid >> 3) * 144 + (tid & 7) * 16 : lb + (tid >> 5) * 528 + (tid & 31) * 16;
            const unsigned wsd = typeB ? 32 * 144 : 8 * 528;
#pragma unroll
            for (int i = 0; i < 8; ++i) ldsw128(wa + i * wsd, rs_[i]);
        }
        asm volatile("" :: "v"(c1));
        wg_barrier();
        KTS(1);
        u4 bfr[4][8];
        if (typeB) {
            const unsigned base = lb + (8 * g + (n >> 2)) * 144 + (n & 3) * 8;
#pragma unroll
            for (int t = 0; t < 4; ++t)
#pragma unroll
                for (int ks = 0; ks < 8; ++ks) {
                    const u2 lo = trread(base + ks * 32 * 144 + t * 32), hi = trread(base + (ks * 32 + 4) * 144 + t * 32);
                    bfr[t][ks] = u4{lo.x, lo.y, hi.x, hi.y};
                }
        } else {
#pragma unroll
            for (int t = 0; t < 4; ++t)
#pragma unroll
                for (int ks = 0; ks < 8; ++ks) bfr[t][ks] = ldsr128(lb + (16 * t + n) * 528 + ks * 64 + g * 16);
        }
        const float sc = typeB ? 1.0f : (1.4426950408889634f / 16.0f);
        const unsigned cb = lb + 36864 + wv * 16896;
        const unsigned cw = cb + (lane >> 5) * 528 + (lane & 31) * 16, cr = cb + n * 528 + g * 16;
        const rsrc_t rw = mk_rsrc(ws + OFF_WC + (size_t)(2 * w) * 28672, 57344);
        const unsigned dst = (14 * wv) * 512 + n * 16;
        f4 prev[4];
        u4 af[2][8];
        auto stage = [&](const int t) {
#pragma unroll
            for (int i = 0; i < 8; ++i) ldsw128(cw + (t & 1) * 8448 + i * 1056, cs[t % RD][i]);
            if (t + RD < 7) ldt(t + RD);
#pragma unroll
            for (int ks = 0; ks < 8; ++ks) af[t & 1][ks] = ldsr128(cr + (t & 1) * 8448 + ks * 64);
        };
        stage(0);
#pragma unroll
        for (int t = 0; t < 7; ++t) {
            __builtin_amdgcn_sched_barrier(0);
            if (t < 6) stage(t + 1);
            f4 acc[4];
#pragma unroll
            for (int q = 0; q < 4; ++q) acc[q] = f4{0.f, 0.f, 0.f, 0.f};
#pragma unroll
            for (int ks = 0; ks < 8; ++ks)
#pragma unroll
                for (int q = 0; q < 4; ++q) acc[q] = mfma(af[t & 1][ks], bfr[q][ks], acc[q]);
            if (t < 6) {
#pragma unroll
                for (int i = 0; i < 8; ++i) { __builtin_amdgcn_sched_group_barrier(0x200, 1, 0); __builtin_amdgcn_sched_group_barrier(0x008, K1_IL, 0); }
#pragma unroll
                for (int i = 0; i < 8; ++i) { __builtin_amdgcn_sched_group_barrier(0x100, 1, 0); __builtin_amdgcn_sched_group_barrier(0x020, 1, 0); __builtin_amdgcn_sched_group_barrier(0x008, 4 - K1_IL, 0); }
            }
#pragma unroll
            for (int q = 0; q < 4; ++q) {
                const unsigned d = dst + (q >> 1) * 28672 + (q & 1) * 256;
                if (t == 6) st64<0>(pk4(acc[q] * sc), rw, d + (12 + (g >> 1)) * 512 + (g & 1) * 8, 0);
                else if (t & 1) { const u4 o = pk8(prev[q] * sc, acc[q] * sc); if (t < K1_ST) st128<SC1>(o, rw, d + g * 512 + pp(t) * 2048, 0); else st128<0>(sw ? u4{o[2], o[3], o[0], o[1]} : o, rw, d + g * 512 + pp(t) * 2048, 0); }
                else prev[q] = acc[q];
            }
            if (t == 0) KTS(2);
        }
        KTS(3);
        KTS(4);
        return;
    }
    const int mi = idx;
    const u4 ones = {0xFFFFFFFFu, 0xFFFFFFFFu, 0xFFFFFFFFu, 0xFFFFFFFFu};
    const rsrc_t rp = mk_rsrc(ws + OFF_SLOT + ((size_t)mi << 21) + 31 * BLK, BLK);
    u4 v[2][14];
    unsigned cw = 0;
    if (wv == 2 && lane < 27) {
        unsigned plo, phi;
        asm volatile("s_getpc_b64 s[40:41]\n\ts_add_u32 %0, s40, _Z7k2_mainPKDF16bS0_S0_S0_PDF16bPh@rel32@lo+4\n\ts_addc_u32 %1, s41, _Z7k2_mainPKDF16bS0_S0_S0_PDF16bPh@rel32@hi+12"
                     : "=&s"(plo), "=&s"(phi) :: "scc", "s40", "s41");
        const uintptr_t pc = ((uintptr_t)phi << 32) | plo;
        cw = *(const __attribute__((address_space(1))) unsigned*)(pc + (mi * 27 + lane) * 128);
    }
    if (wv == 0) {
        auto rt = [&](const unsigned vo) {
            unsigned long long t0, t1; unsigned d;
            asm volatile("s_memtime %0\n\ts_waitcnt lgkmcnt(0)\n\tbuffer_load_dword %2, %3, %4, 0 offen sc1\n\ts_waitcnt vmcnt(0)\n\ts_memtime %1\n\ts_waitcnt lgkmcnt(0)"
                         : "=&s"(t0), "=&s"(t1), "=&v"(d) : "v"(vo), "s"(rp) : "memory");
            return (unsigned)(t1 - t0);
        };
#pragma unroll
        for (int j = 0; j <= K1_NP; ++j) st128<SC1>(ones, rp, (lane >> 3) * 4096 + (lane & 7) * 16 + (j < K1_NP ? j * 256 : 1792), 0);
        asm volatile("s_waitcnt vmcnt(0)" ::: "memory");
        KTS(1);
        unsigned f0 = rt(1792), f1 = rt(8192 + 1792);
        f0 = min(f0, rt(4096 + 1792)); f1 = min(f1, rt(12288 + 1792));
        f0 = min(f0, rt(16384 + 1792)); f1 = min(f1, rt(24576 + 1792));
        const unsigned c13 = f1 < f0 ? 8192 : 0;
        unsigned l[4] = {~0u, ~0u, ~0u, ~0u};
#pragma unroll 1
        for (int i = 0; i < K1_NP; ++i) {
#pragma unroll
            for (int c = 0; c < 4; ++c) l[c] = min(l[c], rt(c13 + (c & 1) * 4096 + (c >> 1) * 16384 + i * 256));
        }
        const unsigned cap = min(min(l[0], l[1]), min(l[2], l[3])) + 80;
#pragma unroll
        for (int c = 0; c < 4; ++c) l[c] = min(l[c], cap);
        const unsigned sel = c13 + (l[1] + l[3] < l[0] + l[2] ? 4096 : 0) + (l[2] + l[3] < l[0] + l[1] ? 0 : 16384);
        if (lane < 2) ((unsigned*)(ws + OFF_MISC))[2 * mi + lane] = ((unsigned)(2 * mi + lane) << 20) + (sel ^ (lane * 4096));
        KTS(2);
    } else {
#pragma unroll
        for (int r = 0; r < 2; ++r) {
            const __bf16* src = x + (size_t)(64 * mi + (r ? 0 : 16 * wv) + n) * DM + 8 * g;
            if (r == 0 || wv == 1) {
#pragma unroll
                for (int ks = 0; ks < 14; ++ks) v[r][ks] = ldnt((const u4*)(src + 32 * ks));
            }
        }
    }
    if (wv) {
        typedef float f2v __attribute__((ext_vector_type(2)));
#pragma unroll
        for (int r = 0; r < 2; ++r)
            if (r == 0 || wv == 1) {
                f2v e[56], s2[4] = {{0.f, 0.f}, {0.f, 0.f}, {0.f, 0.f}, {0.f, 0.f}};
#pragma unroll
                for (int i = 0; i < 56; ++i) { e[i] = f2v{bflo(v[r][i >> 2][i & 3]), bfhi(v[r][i >> 2][i & 3])}; s2[i & 3] += e[i]; }
                const f2v t2 = (s2[0] + s2[1]) + (s2[2] + s2[3]);
                float s = t2.x + t2.y;
                s += __shfl_xor(s, 16); s += __shfl_xor(s, 32);
                const float mean = s * (1.0f / DM);
                f2v q2[4] = {{0.f, 0.f}, {0.f, 0.f}, {0.f, 0.f}, {0.f, 0.f}};
#pragma unroll
                for (int i = 0; i < 56; ++i) { e[i] -= mean; q2[i & 3] += e[i] * e[i]; }
                const f2v u2_ = (q2[0] + q2[1]) + (q2[2] + q2[3]);
                float q = u2_.x + u2_.y;
                q += __shfl_xor(q, 16); q += __shfl_xor(q, 32);
                const float rs = 1.0f / sqrtf(q * (1.0f / DM) + 1e-5f);
                u4* dst = (u4*)(ws + OFF_XNF) + (size_t)(4 * mi + (r ? 0 : wv)) * 14 * 64 + lane;
#pragma unroll
                for (int ks = 0; ks < 14; ++ks) {
                    u4 o;
#pragma unroll
                    for (int j = 0; j < 4; ++j) { const f2v y = e[4 * ks + j] * rs; o[j] = pk(y.x, y.y); }
                    dst[ks * 64] = o;
                }
                __builtin_amdgcn_sched_barrier(0);
            }
    }
    if (wv >= 2) {
        auto fill = [&](const int vt) {
    u4 im[2] = {ones, ones};
    if (mi == 20) im[0] = im[1] = u4{0u, 0u, 0u, 0u};
    if (mi == 14 || mi == 15 || mi == 19) {
        const unsigned short* m0 = (const unsigned short*)mem0 + (2 * (n >> 2) + (n & 1)) * DM + (n & 2) + 16 * (vt >> 4);
#pragma unroll
        for (int j = 0; j < 2; ++j) {
            const int part = mi == 19 ? j : mi - 14;
            if (part == 0 || vt < 192) {
#pragma unroll
                for (int e = 0; e < 4; ++e) im[j][e] = canon(*(const unsigned*)(m0 + 256 * part + 4 * e));
            }
        }
    }
#pragma unroll
        for (int r = 0; r < 2; ++r) {
            const int b = mi + 32 * r, k = b % 21;
            unsigned char* o = ws + OFF_SLOT + ((size_t)(b / 21) << 20) + (k < 14 ? k * BLK : (k < 19 ? (k + 2) * BLK : B_MEMH + (k - 19) * BLK)) + vt * 16;
            if (b < 42) {
#pragma unroll
                for (int i = 0; i < 8; ++i) *(u4*)(o + i * 4096) = im[i & 1];
            }
        }
        };
        fill(tid);
        fill(tid - 128);
        if (wv == 2) *(u4*)(ws + OFF_FLAG + mi * 1024 + lane * 16) = u4{0u, 0u, 0u, 0u};
        if (wv == 3) *(unsigned*)(ws + OFF_RFL + mi * 8192 + lane * 128) = 0u;
        if (wv == 2 && mi == 0) *(u4*)(ws + OFF_FLAG + 32768 + lane * 16) = ones;
    }
    asm volatile("" :: "v"(cw), "v"(c1));
    KTS(3);
    KTS(4);
}

#define WTS(i)
#define OUT_SLEEP 4
template <int SL> DI void wait_flags(rsrc_t rs, unsigned vo) {
#pragma unroll 1
    for (int i = 0; i < (1 << 17); ++i) {
        asm volatile("" ::: "memory");
        const unsigned v = ld32<SC1>(rs, vo, 0);
        if (__ballot(v == 0u) == 0ull) break;
        __builtin_amdgcn_s_sleep(SL);
    }
    asm volatile("" ::: "memory");
}
constexpr unsigned L_WORK_BYTES = 114688;
DI unsigned lane_id() {
    unsigned l;
    asm volatile("v_mbcnt_lo_u32_b32 %0, -1, 0\n\tv_mbcnt_hi_u32_b32 %0, -1, %0" : "=v"(l));
    __builtin_assume(l < 64u);
    return l;
}

template <int ROLE> DI void stage1(const unsigned set, const unsigned rep, const unsigned nrep, const __bf16* w1, const __bf16* w3,
                                   unsigned char* ws, const unsigned tab, const unsigned wv) {
    const unsigned lane = lane_id(), tid = wv * 64 + lane;
    const unsigned g = lane >> 4, n = lane & 15;
    const unsigned pb = 7 * (wv >> 1) + 4 * (wv & 1), ph = 7 * (wv >> 1) + 3, half = wv & 1;
    const unsigned lo = (unsigned)(uintptr_t)ws, hi = (unsigned)((uintptr_t)ws >> 32);
    const unsigned vL = lane * 16, vF = tid == 0 ? OFF_FLAG + (ROLE == 0 ? 0 : (ROLE == 1 ? 64 : 256)) + set * 4 : OOR;
    if (ROLE == 0) {
        const unsigned q0 = tid >> 4, q1 = q0 + 16;
        asm volatile(
        "s_mov_b32 s40, %0\n"
        "s_and_b32 s41, %1, 0xffff\n"
        "s_mov_b32 s42, %2\n"
        "s_mov_b32 s43, 0x00020000\n"
        "s_mov_b32 s44, %3\n"
        "s_mul_i32 s45, %6, 7168\n"
        "s_add_u32 s46, s45, 97779712\n"
        "s_mov_b32 s74, 0xbfb8aa3b\n"
        "s_mov_b32 s75, 0xbfb8aa3b\n"
        "v_mov_b32 v16, %14\n"
        "v_mov_b32 v17, %15\n"
        "v_mov_b32 v18, %16\n"
        "v_mov_b32 v19, %17\n"
        "v_mov_b32 v20, %18\n"
        "v_mov_b32 v21, v16\n"
        "s_cmp_lt_u32 %6, 2\n"
        "s_cselect_b32 s68, 0x84000, 0\n"
        "s_cselect_b32 s69, 0, 0x1000\n"
        "v_mov_b32 v27, %5\n"
        "s_mul_i32 s57, %6, 3072\n"
        "s_add_u32 s57, s57, 57344\n"
        "v_add_u32 v24, s57, v16\n"
        "s_lshl_b32 s57, %6, 10\n"
        "v_add_u32 v25, s57, v16\n"
        "v_add_u32 v26, 57344, v25\n"
        "s_mul_i32 s57, s44, 28672\n"
        "s_add_u32 s57, s57, s46\n"
        "s_add_u32 s58, s57, 4096\n"
        "s_mov_b32 m0, s45\n"
        "s_nop 0\n"
        "buffer_load_dwordx4 v16, s[40:43], s57 offen offset:0 lds\n"
        "buffer_load_dwordx4 v16, s[40:43], s57 offen offset:1024 lds\n"
        "buffer_load_dwordx4 v16, s[40:43], s57 offen offset:2048 lds\n"
        "buffer_load_dwordx4 v16, s[40:43], s57 offen offset:3072 lds\n"
        "s_add_u32 m0, s45, 4096\n"
        "s_nop 0\n"
        "buffer_load_dwordx4 v16, s[40:43], s58 offen offset:0 lds\n"
        "buffer_load_dwordx4 v16, s[40:43], s58 offen offset:1024 lds\n"
        "buffer_load_dwordx4 v16, s[40:43], s58 offen offset:2048 lds\n"
        "s_mov_b32 s60, %7\n"
        "s_add_u32 s61, s60, 28672\n"
        "s_add_u32 s62, s60, 57344\n"
        "s_mov_b32 s63, %8\n"
        "buffer_load_dwordx4 a[0:3], v17, s[40:43], s60 offen offset:0\n"
        "buffer_load_dwordx4 a[4:7], v17, s[40:43], s60 offen offset:64\n"
        "buffer_load_dwordx4 a[8:11], v17, s[40:43], s61 offen offset:0\n"
        "buffer_load_dwordx4 a[12:15], v17, s[40:43], s61 offen offset:64\n"
        "buffer_load_dwordx4 a[16:19], v17, s[40:43], s62 offen offset:0\n"
        "buffer_load_dwordx4 a[20:23], v17, s[40:43], s62 offen offset:64\n"
        "buffer_load_dwordx4 a[24:27], v17, s[40:43], s63 offen offset:0\n"
        "buffer_load_dwordx4 a[28:31], v17, s[40:43], s60 offen offset:2048\n"
        "buffer_load_dwordx4 a[32:35], v17, s[40:43], s60 offen offset:2112\n"
        "buffer_load_dwordx4 a[36:39], v17, s[40:43], s61 offen offset:2048\n"
        "buffer_load_dwordx4 a[40:43], v17, s[40:43], s61 offen offset:2112\n"
        "buffer_load_dwordx4 a[44:47], v17, s[40:43], s62 offen offset:2048\n"
        "buffer_load_dwordx4 a[48:51], v17, s[40:43], s62 offen offset:2112\n"
        "buffer_load_dwordx4 a[52:55], v17, s[40:43], s63 offen offset:2048\n"
        "s_add_u32 s60, s60, 4096\n"
        "s_add_u32 s61, s61, 4096\n"
        "s_add_u32 s62, s62, 4096\n"
        "s_add_u32 s63, s63, 4096\n"
        "buffer_load_dwordx4 a[56:59], v17, s[40:43], s60 offen offset:0\n"
        "buffer_load_dwordx4 a[60:63], v17, s[40:43], s60 offen offset:64\n"
        "buffer_load_dwordx4 a[64:67], v17, s[40:43], s61 offen offset:0\n"
        "buffer_load_dwordx4 a[68:71], v17, s[40:43], s61 offen offset:64\n"
        "buffer_load_dwordx4 a[72:75], v17, s[40:43], s62 offen offset:0\n"
        "buffer_load_dwordx4 a[76:79], v17, s[40:43], s62 offen offset:64\n"
        "buffer_load_dwordx4 a[80:83], v17, s[40:43], s63 offen offset:0\n"
        "buffer_load_dwordx4 a[84:87], v17, s[40:43], s60 offen offset:2048\n"
        "buffer_load_dwordx4 a[88:91], v17, s[40:43], s60 offen offset:2112\n"
        "buffer_load_dwordx4 a[92:95], v17, s[40:43], s61 offen offset:2048\n"
        "buffer_load_dwordx4 a[96:99], v17, s[40:43], s61 offen offset:2112\n"
        "buffer_load_dwordx4 a[100:103], v17, s[40:43], s62 offen offset:2048\n"
        "buffer_load_dwordx4 a[104:107], v17, s[40:43], s62 offen offset:2112\n"
        "buffer_load_dwordx4 a[108:111], v17, s[40:43], s63 offen offset:2048\n"
        "s_add_u32 s60, s60, 4096\n"
        "s_add_u32 s61, s61, 4096\n"
        "s_add_u32 s62, s62, 4096\n"
        "s_add_u32 s63, s63, 4096\n"
        "buffer_load_dwordx4 a[112:115], v17, s[40:43], s60 offen offset:0\n"
        "buffer_load_dwordx4 a[116:119], v17, s[40:43], s60 offen offset:64\n"
        "buffer_load_dwordx4 a[120:123], v17, s[40:43], s61 offen offset:0\n"
        "buffer_load_dwordx4 a[124:127], v17, s[40:43], s61 offen offset:64\n"
        "buffer_load_dwordx4 a[128:131], v17, s[40:43], s62 offen offset:0\n"
        "buffer_load_dwordx4 a[132:135], v17, s[40:43], s62 offen offset:64\n"
        "buffer_load_dwordx4 a[136:139], v17, s[40:43], s63 offen offset:0\n"
        "buffer_load_dwordx4 a[140:143], v17, s[40:43], s60 offen offset:2048\n"
        "buffer_load_dwordx4 a[144:147], v17, s[40:43], s60 offen offset:2112\n"
        "buffer_load_dwordx4 a[148:151], v17, s[40:43], s61 offen offset:2048\n"
        "buffer_load_dwordx4 a[152:155], v17, s[40:43], s61 offen offset:2112\n"
        "buffer_load_dwordx4 a[156:159], v17, s[40:43], s62 offen offset:2048\n"
        "buffer_load_dwordx4 a[160:163], v17, s[40:43], s62 offen offset:2112\n"
        "buffer_load_dwordx4 a[164:167], v17, s[40:43], s63 offen offset:2048\n"
        "s_add_u32 s60, s60, 4096\n"
        "s_add_u32 s61, s61, 4096\n"
        "s_add_u32 s62, s62, 4096\n"
        "s_add_u32 s63, s63, 4096\n"
        "buffer_load_dwordx4 a[168:171], v17, s[40:43], s60 offen offset:0\n"
        "buffer_load_dwordx4 a[172:175], v17, s[40:43], s60 offen offset:64\n"
        "buffer_load_dwordx4 a[176:179], v17, s[40:43], s61 offen offset:0\n"
        "buffer_load_dwordx4 a[180:183], v17, s[40:43], s61 offen offset:64\n"
        "buffer_load_dwordx4 a[184:187], v17, s[40:43], s62 offen offset:0\n"
        "buffer_load_dwordx4 a[188:191], v17, s[40:43], s62 offen offset:64\n"
        "buffer_load_dwordx4 a[192:195], v17, s[40:43], s63 offen offset:0\n"
        "buffer_load_dwordx4 a[196:199], v17, s[40:43], s60 offen offset:2048\n"
        "buffer_load_dwordx4 a[200:203], v17, s[40:43], s60 offen offset:2112\n"
        "buffer_load_dwordx4 a[204:207], v17, s[40:43], s61 offen offset:2048\n"
        "buffer_load_dwordx4 a[208:211], v17, s[40:43], s61 offen offset:2112\n"
        "buffer_load_dwordx4 a[212:215], v17, s[40:43], s62 offen offset:2048\n"
        "buffer_load_dwordx4 a[216:219], v17, s[40:43], s62 offen offset:2112\n"
        "buffer_load_dwordx4 a[220:223], v17, s[40:43], s63 offen offset:2048\n"
        "s_add_u32 s60, s60, 4096\n"
        "s_add_u32 s61, s61, 4096\n"
        "s_add_u32 s62, s62, 4096\n"
        "s_add_u32 s63, s63, 4096\n"
        "buffer_load_dwordx4 a[224:227], v17, s[40:43], s60 offen offset:0\n"
        "buffer_load_dwordx4 a[228:231], v17, s[40:43], s60 offen offset:64\n"
        "buffer_load_dwordx4 a[232:235], v17, s[40:43], s61 offen offset:0\n"
        "buffer_load_dwordx4 a[236:239], v17, s[40:43], s61 offen offset:64\n"
        "buffer_load_dwordx4 a[240:243], v17, s[40:43], s62 offen offset:0\n"
        "buffer_load_dwordx4 a[244:247], v17, s[40:43], s62 offen offset:64\n"
        "buffer_load_dwordx4 a[248:251], v17, s[40:43], s63 offen offset:0\n"
        "buffer_load_dwordx4 a[252:255], v17, s[40:43], s60 offen offset:2048\n"
        "buffer_load_dwordx4 v[120:123], v17, s[40:43], s60 offen offset:2112\n"
        "buffer_load_dwordx4 v[124:127], v17, s[40:43], s61 offen offset:2048\n"
        "buffer_load_dwordx4 v[128:131], v17, s[40:43], s61 offen offset:2112\n"
        "buffer_load_dwordx4 v[132:135], v17, s[40:43], s62 offen offset:2048\n"
        "buffer_load_dwordx4 v[136:139], v17, s[40:43], s62 offen offset:2112\n"
        "buffer_load_dwordx4 v[140:143], v17, s[40:43], s63 offen offset:2048\n"
        "s_add_u32 s60, s60, 4096\n"
        "s_add_u32 s61, s61, 4096\n"
        "s_add_u32 s62, s62, 4096\n"
        "s_add_u32 s63, s63, 4096\n"
        "buffer_load_dwordx4 v[144:147], v17, s[40:43], s60 offen offset:0\n"
        "buffer_load_dwordx4 v[148:151], v17, s[40:43], s60 offen offset:64\n"
        "buffer_load_dwordx4 v[152:155], v17, s[40:43], s61 offen offset:0\n"
        "buffer_load_dwordx4 v[156:159], v17, s[40:43], s61 offen offset:64\n"
        "buffer_load_dwordx4 v[160:163], v17, s[40:43], s62 offen offset:0\n"
        "buffer_load_dwordx4 v[164:167], v17, s[40:43], s62 offen offset:64\n"
        "buffer_load_dwordx4 v[168:171], v17, s[40:43], s63 offen offset:0\n"
        "buffer_load_dwordx4 v[172:175], v17, s[40:43], s60 offen offset:2048\n"
        "buffer_load_dwordx4 v[176:179], v17, s[40:43], s60 offen offset:2112\n"
        "buffer_load_dwordx4 v[180:183], v17, s[40:43], s61 offen offset:2048\n"
        "buffer_load_dwordx4 v[184:187], v17, s[40:43], s61 offen offset:2112\n"
        "buffer_load_dwordx4 v[188:191], v17, s[40:43], s62 offen offset:2048\n"
        "buffer_load_dwordx4 v[192:195], v17, s[40:43], s62 offen offset:2112\n"
        "buffer_load_dwordx4 v[196:199], v17, s[40:43], s63 offen offset:2048\n"
        "s_add_u32 s60, s60, 4096\n"
        "s_add_u32 s61, s61, 4096\n"
        "s_add_u32 s62, s62, 4096\n"
        "s_add_u32 s63, s63, 4096\n"
        "buffer_load_dwordx4 v[200:203], v17, s[40:43], s60 offen offset:0\n"
        "buffer_load_dwordx4 v[204:207], v17, s[40:43], s60 offen offset:64\n"
        "buffer_load_dwordx4 v[208:211], v17, s[40:43], s61 offen offset:0\n"
        "buffer_load_dwordx4 v[212:215], v17, s[40:43], s61 offen offset:64\n"
        "buffer_load_dwordx4 v[216:219], v17, s[40:43], s62 offen offset:0\n"
        "buffer_load_dwordx4 v[220:223], v17, s[40:43], s62 offen offset:64\n"
        "buffer_load_dwordx4 v[224:227], v17, s[40:43], s63 offen offset:0\n"
        "buffer_load_dwordx4 v[228:231], v17, s[40:43], s60 offen offset:2048\n"
        "buffer_load_dwordx4 v[232:235], v17, s[40:43], s60 offen offset:2112\n"
        "buffer_load_dwordx4 v[236:239], v17, s[40:43], s61 offen offset:2048\n"
        "buffer_load_dwordx4 v[240:243], v17, s[40:43], s61 offen offset:2112\n"
        "buffer_load_dwordx4 v[244:247], v17, s[40:43], s62 offen offset:2048\n"
        "buffer_load_dwordx4 v[248:251], v17, s[40:43], s62 offen offset:2112\n"
        "buffer_load_dwordx4 v[252:255], v17, s[40:43], s63 offen offset:2048\n"
        "s_add_u32 s60, s60, 4096\n"
        "s_add_u32 s61, s61, 4096\n"
        "s_add_u32 s62, s62, 4096\n"
        "s_add_u32 s63, s63, 4096\n"
        "s_waitcnt vmcnt(63)\n"
        "s_mov_b32 s59, 28672\n"
        "s_barrier\n"
        ".Lw0_loop_%=:\n"
        "ds_read_b128 v[88:91], v21 offset:0\n"
        "ds_read_b128 v[92:95], v21 offset:14336\n"
        "s_add_u32 s45, s45, s59\n"
        "s_sub_u32 s59, 0, s59\n"
        "s_add_u32 s47, s44, %4\n"
        "s_min_u32 s48, s47, 63\n"
        "s_mul_i32 s57, s48, 28672\n"
        "s_add_u32 s57, s57, s46\n"
        "s_add_u32 s58, s57, 4096\n"
        "s_mov_b32 m0, s45\n"
        "s_nop 0\n"
        "buffer_load_dwordx4 v16, s[40:43], s57 offen offset:0 lds\n"
        "buffer_load_dwordx4 v16, s[40:43], s57 offen offset:1024 lds\n"
        "buffer_load_dwordx4 v16, s[40:43], s57 offen offset:2048 lds\n"
        "buffer_load_dwordx4 v16, s[40:43], s57 offen offset:3072 lds\n"
        "s_add_u32 m0, s45, 4096\n"
        "s_nop 0\n"
        "buffer_load_dwordx4 v16, s[40:43], s58 offen offset:0 lds\n"
        "buffer_load_dwordx4 v16, s[40:43], s58 offen offset:1024 lds\n"
        "buffer_load_dwordx4 v16, s[40:43], s58 offen offset:2048 lds\n"
        "v_mov_b32 v104, -1\n"
        "v_mov_b32 v105, -1\n"
        "v_mov_b32 v106, -1\n"
        "v_mov_b32 v107, -1\n"
        "s_cmp_lt_u32 s44, 2\n"
        "s_cselect_b32 s58, 63, s44\n"
        "v_readlane_b32 s57, v27, s44\n"
        "v_readlane_b32 s58, v27, s58\n"
        "s_andn2_b32 s57, s57, 0x1000\n"
        "s_xor_b32 s58, s58, s68\n"
        "s_andn2_b32 s58, s58, s69\n"
        "s_nop 1\n"
        "buffer_store_dwordx4 v[104:107], %19, s[40:43], s58 offen sc1\n"
        "buffer_store_dwordx4 v[104:107], v19, s[40:43], s57 offen sc1\n"
        "buffer_store_dwordx4 v[104:107], v20, s[40:43], s57 offen sc1\n"
        "v_add_u32 v22, %11, v21\n"
        "v_add_u32 v23, %12, v21\n"
        "s_mul_i32 s57, s44, 458752\n"
        "s_add_u32 s50, s57, %9\n"
        "s_add_u32 s51, s57, %10\n"
        "s_add_u32 s52, s50, 229376\n"
        "s_add_u32 s53, s51, 229376\n"
        "s_lshl_b32 s54, s44, 9\n"
        "s_mul_i32 s55, s44, 49152\n"
        "s_add_u32 s55, s55, %13\n"
        "ds_read_b128 v[96:99], v21 offset:1024\n"
        "ds_read_b128 v[100:103], v21 offset:15360\n"
        "s_waitcnt lgkmcnt(2)\n"
        "v_mfma_f32_16x16x32_bf16 v[32:35], a[0:3], v[88:91], 0\n"
        "v_mfma_f32_16x16x32_bf16 v[36:39], a[4:7], v[88:91], 0\n"
        "v_mfma_f32_16x16x32_bf16 v[40:43], a[8:11], v[88:91], 0\n"
        "v_mfma_f32_16x16x32_bf16 v[44:47], a[12:15], v[88:91], 0\n"
        "v_mfma_f32_16x16x32_bf16 v[48:51], a[16:19], v[88:91], 0\n"
        "v_mfma_f32_16x16x32_bf16 v[52:55], a[20:23], v[88:91], 0\n"
        "v_mfma_f32_16x16x32_bf16 v[56:59], a[24:27], v[88:91], 0\n"
        "v_mfma_f32_16x16x32_bf16 v[60:63], a[0:3], v[92:95], 0\n"
        "v_mfma_f32_16x16x32_bf16 v[64:67], a[4:7], v[92:95], 0\n"
        "v_mfma_f32_16x16x32_bf16 v[68:71], a[8:11], v[92:95], 0\n"
        "v_mfma_f32_16x16x32_bf16 v[72:75], a[12:15], v[92:95], 0\n"
        "v_mfma_f32_16x16x32_bf16 v[76:79], a[16:19], v[92:95], 0\n"
        "v_mfma_f32_16x16x32_bf16 v[80:83], a[20:23], v[92:95], 0\n"
        "v_mfma_f32_16x16x32_bf16 v[84:87], a[24:27], v[92:95], 0\n"
        "ds_read_b128 v[88:91], v21 offset:2048\n"
        "ds_read_b128 v[92:95], v21 offset:16384\n"
        "s_waitcnt lgkmcnt(2)\n"
        "v_mfma_f32_16x16x32_bf16 v[32:35], a[28:31], v[96:99], v[32:35]\n"
        "v_mfma_f32_16x16x32_bf16 v[36:39], a[32:35], v[96:99], v[36:39]\n"
        "v_mfma_f32_16x16x32_bf16 v[40:43], a[36:39], v[96:99], v[40:43]\n"
        "v_mfma_f32_16x16x32_bf16 v[44:47], a[40:43], v[96:99], v[44:47]\n"
        "v_mfma_f32_16x16x32_bf16 v[48:51], a[44:47], v[96:99], v[48:51]\n"
        "v_mfma_f32_16x16x32_bf16 v[52:55], a[48:51], v[96:99], v[52:55]\n"
        "v_mfma_f32_16x16x32_bf16 v[56:59], a[52:55], v[96:99], v[56:59]\n"
        "v_mfma_f32_16x16x32_bf16 v[60:63], a[28:31], v[100:103], v[60:63]\n"
        "v_mfma_f32_16x16x32_bf16 v[64:67], a[32:35], v[100:103], v[64:67]\n"
        "v_mfma_f32_16x16x32_bf16 v[68:71], a[36:39], v[100:103], v[68:71]\n"
        "v_mfma_f32_16x16x32_bf16 v[72:75], a[40:43], v[100:103], v[72:75]\n"
        "v_mfma_f32_16x16x32_bf16 v[76:79], a[44:47], v[100:103], v[76:79]\n"
        "v_mfma_f32_16x16x32_bf16 v[80:83], a[48:51], v[100:103], v[80:83]\n"
        "v_mfma_f32_16x16x32_bf16 v[84:87], a[52:55], v[100:103], v[84:87]\n"
        "ds_read_b128 v[96:99], v21 offset:3072\n"
        "ds_read_b128 v[100:103], v21 offset:17408\n"
        "s_waitcnt lgkmcnt(2)\n"
        "v_mfma_f32_16x16x32_bf16 v[32:35], a[56:59], v[88:91], v[32:35]\n"
        "v_mfma_f32_16x16x32_bf16 v[36:39], a[60:63], v[88:91], v[36:39]\n"
        "v_mfma_f32_16x16x32_bf16 v[40:43], a[64:67], v[88:91], v[40:43]\n"
        "v_mfma_f32_16x16x32_bf16 v[44:47], a[68:71], v[88:91], v[44:47]\n"
        "v_mfma_f32_16x16x32_bf16 v[48:51], a[72:75], v[88:91], v[48:51]\n"
        "v_mfma_f32_16x16x32_bf16 v[52:55], a[76:79], v[88:91], v[52:55]\n"
        "v_mfma_f32_16x16x32_bf16 v[56:59], a[80:83], v[88:91], v[56:59]\n"
        "v_mfma_f32_16x16x32_bf16 v[60:63], a[56:59], v[92:95], v[60:63]\n"
        "v_mfma_f32_16x16x32_bf16 v[64:67], a[60:63], v[92:95], v[64:67]\n"
        "v_mfma_f32_16x16x32_bf16 v[68:71], a[64:67], v[92:95], v[68:71]\n"
        "v_mfma_f32_16x16x32_bf16 v[72:75], a[68:71], v[92:95], v[72:75]\n"
        "v_mfma_f32_16x16x32_bf16 v[76:79], a[72:75], v[92:95], v[76:79]\n"
        "v_mfma_f32_16x16x32_bf16 v[80:83], a[76:79], v[92:95], v[80:83]\n"
        "v_mfma_f32_16x16x32_bf16 v[84:87], a[80:83], v[92:95], v[84:87]\n"
        "ds_read_b128 v[88:91], v21 offset:4096\n"
        "ds_read_b128 v[92:95], v21 offset:18432\n"
        "s_waitcnt lgkmcnt(2)\n"
        "v_mfma_f32_16x16x32_bf16 v[32:35], a[84:87], v[96:99], v[32:35]\n"
        "v_mfma_f32_16x16x32_bf16 v[36:39], a[88:91], v[96:99], v[36:39]\n"
        "v_mfma_f32_16x16x32_bf16 v[40:43], a[92:95], v[96:99], v[40:43]\n"
        "v_mfma_f32_16x16x32_bf16 v[44:47], a[96:99], v[96:99], v[44:47]\n"
        "v_mfma_f32_16x16x32_bf16 v[48:51], a[100:103], v[96:99], v[48:51]\n"
        "v_mfma_f32_16x16x32_bf16 v[52:55], a[104:107], v[96:99], v[52:55]\n"
        "v_mfma_f32_16x16x32_bf16 v[56:59], a[108:111], v[96:99], v[56:59]\n"
        "v_mfma_f32_16x16x32_bf16 v[60:63], a[84:87], v[100:103], v[60:63]\n"
        "v_mfma_f32_16x16x32_bf16 v[64:67], a[88:91], v[100:103], v[64:67]\n"
        "v_mfma_f32_16x16x32_bf16 v[68:71], a[92:95], v[100:103], v[68:71]\n"
        "v_mfma_f32_16x16x32_bf16 v[72:75], a[96:99], v[100:103], v[72:75]\n"
        "v_mfma_f32_16x16x32_bf16 v[76:79], a[100:103], v[100:103], v[76:79]\n"
        "v_mfma_f32_16x16x32_bf16 v[80:83], a[104:107], v[100:103], v[80:83]\n"
        "v_mfma_f32_16x16x32_bf16 v[84:87], a[108:111], v[100:103], v[84:87]\n"
        "ds_read_b128 v[96:99], v21 offset:5120\n"
        "ds_read_b128 v[100:103], v21 offset:19456\n"
        "s_waitcnt lgkmcnt(2)\n"
        "v_mfma_f32_16x16x32_bf16 v[32:35], a[112:115], v[88:91], v[32:35]\n"
        "v_mfma_f32_16x16x32_bf16 v[36:39], a[116:119], v[88:91], v[36:39]\n"
        "v_mfma_f32_16x16x32_bf16 v[40:43], a[120:123], v[88:91], v[40:43]\n"
        "v_mfma_f32_16x16x32_bf16 v[44:47], a[124:127], v[88:91], v[44:47]\n"
        "v_mfma_f32_16x16x32_bf16 v[48:51], a[128:131], v[88:91], v[48:51]\n"
        "v_mfma_f32_16x16x32_bf16 v[52:55], a[132:135], v[88:91], v[52:55]\n"
        "v_mfma_f32_16x16x32_bf16 v[56:59], a[136:139], v[88:91], v[56:59]\n"
        "v_mfma_f32_16x16x32_bf16 v[60:63], a[112:115], v[92:95], v[60:63]\n"
        "v_mfma_f32_16x16x32_bf16 v[64:67], a[116:119], v[92:95], v[64:67]\n"
        "v_mfma_f32_16x16x32_bf16 v[68:71], a[120:123], v[92:95], v[68:71]\n"
        "v_mfma_f32_16x16x32_bf16 v[72:75], a[124:127], v[92:95], v[72:75]\n"
        "v_mfma_f32_16x16x32_bf16 v[76:79], a[128:131], v[92:95], v[76:79]\n"
        "v_mfma_f32_16x16x32_bf16 v[80:83], a[132:135], v[92:95], v[80:83]\n"
        "v_mfma_f32_16x16x32_bf16 v[84:87], a[136:139], v[92:95], v[84:87]\n"
        "ds_read_b128 v[88:91], v21 offset:6144\n"
        "ds_read_b128 v[92:95], v21 offset:20480\n"
        "s_waitcnt lgkmcnt(2)\n"
        "v_mfma_f32_16x16x32_bf16 v[32:35], a[140:143], v[96:99], v[32:35]\n"
        "v_mfma_f32_16x16x32_bf16 v[36:39], a[144:147], v[96:99], v[36:39]\n"
        "v_mfma_f32_16x16x32_bf16 v[40:43], a[148:151], v[96:99], v[40:43]\n"
        "v_mfma_f32_16x16x32_bf16 v[44:47], a[152:155], v[96:99], v[44:47]\n"
        "v_mfma_f32_16x16x32_bf16 v[48:51], a[156:159], v[96:99], v[48:51]\n"
        "v_mfma_f32_16x16x32_bf16 v[52:55], a[160:163], v[96:99], v[52:55]\n"
        "v_mfma_f32_16x16x32_bf16 v[56:59], a[164:167], v[96:99], v[56:59]\n"
        "v_mfma_f32_16x16x32_bf16 v[60:63], a[140:143], v[100:103], v[60:63]\n"
        "v_mfma_f32_16x16x32_bf16 v[64:67], a[144:147], v[100:103], v[64:67]\n"
        "v_mfma_f32_16x16x32_bf16 v[68:71], a[148:151], v[100:103], v[68:71]\n"
        "v_mfma_f32_16x16x32_bf16 v[72:75], a[152:155], v[100:103], v[72:75]\n"
        "v_mfma_f32_16x16x32_bf16 v[76:79], a[156:159], v[100:103], v[76:79]\n"
        "v_mfma_f32_16x16x32_bf16 v[80:83], a[160:163], v[100:103], v[80:83]\n"
        "v_mfma_f32_16x16x32_bf16 v[84:87], a[164:167], v[100:103], v[84:87]\n"
        "ds_read_b128 v[96:99], v21 offset:7168\n"
        "ds_read_b128 v[100:103], v21 offset:21504\n"
        "s_waitcnt vmcnt(59) lgkmcnt(2)\n"
        "v_mfma_f32_16x16x32_bf16 v[32:35], a[168:171], v[88:91], v[32:35]\n"
        "v_mfma_f32_16x16x32_bf16 v[36:39], a[172:175], v[88:91], v[36:39]\n"
        "v_mfma_f32_16x16x32_bf16 v[40:43], a[176:179], v[88:91], v[40:43]\n"
        "v_mfma_f32_16x16x32_bf16 v[44:47], a[180:183], v[88:91], v[44:47]\n"
        "v_mfma_f32_16x16x32_bf16 v[48:51], a[184:187], v[88:91], v[48:51]\n"
        "v_mfma_f32_16x16x32_bf16 v[52:55], a[188:191], v[88:91], v[52:55]\n"
        "v_mfma_f32_16x16x32_bf16 v[56:59], a[192:195], v[88:91], v[56:59]\n"
        "v_mfma_f32_16x16x32_bf16 v[60:63], a[168:171], v[92:95], v[60:63]\n"
        "v_mfma_f32_16x16x32_bf16 v[64:67], a[172:175], v[92:95], v[64:67]\n"
        "v_mfma_f32_16x16x32_bf16 v[68:71], a[176:179], v[92:95], v[68:71]\n"
        "v_mfma_f32_16x16x32_bf16 v[72:75], a[180:183], v[92:95], v[72:75]\n"
        "v_mfma_f32_16x16x32_bf16 v[76:79], a[184:187], v[92:95], v[76:79]\n"
        "v_mfma_f32_16x16x32_bf16 v[80:83], a[188:191], v[92:95], v[80:83]\n"
        "v_mfma_f32_16x16x32_bf16 v[84:87], a[192:195], v[92:95], v[84:87]\n"
        "ds_read_b128 v[88:91], v21 offset:8192\n"
        "ds_read_b128 v[92:95], v21 offset:22528\n"
        "s_waitcnt vmcnt(52) lgkmcnt(2)\n"
        "v_mfma_f32_16x16x32_bf16 v[32:35], a[196:199], v[96:99], v[32:35]\n"
        "v_mfma_f32_16x16x32_bf16 v[36:39], a[200:203], v[96:99], v[36:39]\n"
        "v_mfma_f32_16x16x32_bf16 v[40:43], a[204:207], v[96:99], v[40:43]\n"
        "v_mfma_f32_16x16x32_bf16 v[44:47], a[208:211], v[96:99], v[44:47]\n"
        "v_mfma_f32_16x16x32_bf16 v[48:51], a[212:215], v[96:99], v[48:51]\n"
        "v_mfma_f32_16x16x32_bf16 v[52:55], a[216:219], v[96:99], v[52:55]\n"
        "v_mfma_f32_16x16x32_bf16 v[56:59], a[220:223], v[96:99], v[56:59]\n"
        "v_mfma_f32_16x16x32_bf16 v[60:63], a[196:199], v[100:103], v[60:63]\n"
        "v_mfma_f32_16x16x32_bf16 v[64:67], a[200:203], v[100:103], v[64:67]\n"
        "v_mfma_f32_16x16x32_bf16 v[68:71], a[204:207], v[100:103], v[68:71]\n"
        "v_mfma_f32_16x16x32_bf16 v[72:75], a[208:211], v[100:103], v[72:75]\n"
        "v_mfma_f32_16x16x32_bf16 v[76:79], a[212:215], v[100:103], v[76:79]\n"
        "v_mfma_f32_16x16x32_bf16 v[80:83], a[216:219], v[100:103], v[80:83]\n"
        "v_mfma_f32_16x16x32_bf16 v[84:87], a[220:223], v[100:103], v[84:87]\n"
        "ds_read_b128 v[96:99], v21 offset:9216\n"
        "ds_read_b128 v[100:103], v21 offset:23552\n"
        "s_waitcnt vmcnt(45) lgkmcnt(2)\n"
        "v_mfma_f32_16x16x32_bf16 v[32:35], a[224:227], v[88:91], v[32:35]\n"
        "v_mfma_f32_16x16x32_bf16 v[36:39], a[228:231], v[88:91], v[36:39]\n"
        "v_mfma_f32_16x16x32_bf16 v[40:43], a[232:235], v[88:91], v[40:43]\n"
        "v_mfma_f32_16x16x32_bf16 v[44:47], a[236:239], v[88:91], v[44:47]\n"
        "v_mfma_f32_16x16x32_bf16 v[48:51], a[240:243], v[88:91], v[48:51]\n"
        "v_mfma_f32_16x16x32_bf16 v[52:55], a[244:247], v[88:91], v[52:55]\n"
        "v_mfma_f32_16x16x32_bf16 v[56:59], a[248:251], v[88:91], v[56:59]\n"
        "v_mfma_f32_16x16x32_bf16 v[60:63], a[224:227], v[92:95], v[60:63]\n"
        "v_mfma_f32_16x16x32_bf16 v[64:67], a[228:231], v[92:95], v[64:67]\n"
        "v_mfma_f32_16x16x32_bf16 v[68:71], a[232:235], v[92:95], v[68:71]\n"
        "v_mfma_f32_16x16x32_bf16 v[72:75], a[236:239], v[92:95], v[72:75]\n"
        "v_mfma_f32_16x16x32_bf16 v[76:79], a[240:243], v[92:95], v[76:79]\n"
        "v_mfma_f32_16x16x32_bf16 v[80:83], a[244:247], v[92:95], v[80:83]\n"
        "v_mfma_f32_16x16x32_bf16 v[84:87], a[248:251], v[92:95], v[84:87]\n"
        "ds_read_b128 v[88:91], v21 offset:10240\n"
        "ds_read_b128 v[92:95], v21 offset:24576\n"
        "s_waitcnt vmcnt(38) lgkmcnt(2)\n"
        "v_mfma_f32_16x16x32_bf16 v[32:35], a[252:255], v[96:99], v[32:35]\n"
        "v_mfma_f32_16x16x32_bf16 v[36:39], v[120:123], v[96:99], v[36:39]\n"
        "v_mfma_f32_16x16x32_bf16 v[40:43], v[124:127], v[96:99], v[40:43]\n"
        "v_mfma_f32_16x16x32_bf16 v[44:47], v[128:131], v[96:99], v[44:47]\n"
        "v_mfma_f32_16x16x32_bf16 v[48:51], v[132:135], v[96:99], v[48:51]\n"
        "v_mfma_f32_16x16x32_bf16 v[52:55], v[136:139], v[96:99], v[52:55]\n"
        "v_mfma_f32_16x16x32_bf16 v[56:59], v[140:143], v[96:99], v[56:59]\n"
        "v_mfma_f32_16x16x32_bf16 v[60:63], a[252:255], v[100:103], v[60:63]\n"
        "v_mfma_f32_16x16x32_bf16 v[64:67], v[120:123], v[100:103], v[64:67]\n"
        "v_mfma_f32_16x16x32_bf16 v[68:71], v[124:127], v[100:103], v[68:71]\n"
        "v_mfma_f32_16x16x32_bf16 v[72:75], v[128:131], v[100:103], v[72:75]\n"
        "v_mfma_f32_16x16x32_bf16 v[76:79], v[132:135], v[100:103], v[76:79]\n"
        "v_mfma_f32_16x16x32_bf16 v[80:83], v[136:139], v[100:103], v[80:83]\n"
        "v_mfma_f32_16x16x32_bf16 v[84:87], v[140:143], v[100:103], v[84:87]\n"
        "ds_read_b128 v[96:99], v21 offset:11264\n"
        "ds_read_b128 v[100:103], v21 offset:25600\n"
        "s_waitcnt vmcnt(31) lgkmcnt(2)\n"
        "v_mfma_f32_16x16x32_bf16 v[32:35], v[144:147], v[88:91], v[32:35]\n"
        "v_mfma_f32_16x16x32_bf16 v[36:39], v[148:151], v[88:91], v[36:39]\n"
        "v_mfma_f32_16x16x32_bf16 v[40:43], v[152:155], v[88:91], v[40:43]\n"
        "v_mfma_f32_16x16x32_bf16 v[44:47], v[156:159], v[88:91], v[44:47]\n"
        "v_mfma_f32_16x16x32_bf16 v[48:51], v[160:163], v[88:91], v[48:51]\n"
        "v_mfma_f32_16x16x32_bf16 v[52:55], v[164:167], v[88:91], v[52:55]\n"
        "v_mfma_f32_16x16x32_bf16 v[56:59], v[168:171], v[88:91], v[56:59]\n"
        "v_mfma_f32_16x16x32_bf16 v[60:63], v[144:147], v[92:95], v[60:63]\n"
        "v_mfma_f32_16x16x32_bf16 v[64:67], v[148:151], v[92:95], v[64:67]\n"
        "v_mfma_f32_16x16x32_bf16 v[68:71], v[152:155], v[92:95], v[68:71]\n"
        "v_mfma_f32_16x16x32_bf16 v[72:75], v[156:159], v[92:95], v[72:75]\n"
        "v_mfma_f32_16x16x32_bf16 v[76:79], v[160:163], v[92:95], v[76:79]\n"
        "v_mfma_f32_16x16x32_bf16 v[80:83], v[164:167], v[92:95], v[80:83]\n"
        "v_mfma_f32_16x16x32_bf16 v[84:87], v[168:171], v[92:95], v[84:87]\n"
        "ds_read_b128 v[88:91], v21 offset:12288\n"
        "ds_read_b128 v[92:95], v21 offset:26624\n"
        "s_waitcnt vmcnt(24) lgkmcnt(2)\n"
        "v_mfma_f32_16x16x32_bf16 v[32:35], v[172:175], v[96:99], v[32:35]\n"
        "v_mfma_f32_16x16x32_bf16 v[36:39], v[176:179], v[96:99], v[36:39]\n"
        "v_mfma_f32_16x16x32_bf16 v[40:43], v[180:183], v[96:99], v[40:43]\n"
        "v_mfma_f32_16x16x32_bf16 v[44:47], v[184:187], v[96:99], v[44:47]\n"
        "v_mfma_f32_16x16x32_bf16 v[48:51], v[188:191], v[96:99], v[48:51]\n"
        "v_mfma_f32_16x16x32_bf16 v[52:55], v[192:195], v[96:99], v[52:55]\n"
        "v_mfma_f32_16x16x32_bf16 v[56:59], v[196:199], v[96:99], v[56:59]\n"
        "v_mfma_f32_16x16x32_bf16 v[60:63], v[172:175], v[100:103], v[60:63]\n"
        "v_mfma_f32_16x16x32_bf16 v[64:67], v[176:179], v[100:103], v[64:67]\n"
        "v_mfma_f32_16x16x32_bf16 v[68:71], v[180:183], v[100:103], v[68:71]\n"
        "v_mfma_f32_16x16x32_bf16 v[72:75], v[184:187], v[100:103], v[72:75]\n"
        "v_mfma_f32_16x16x32_bf16 v[76:79], v[188:191], v[100:103], v[76:79]\n"
        "v_mfma_f32_16x16x32_bf16 v[80:83], v[192:195], v[100:103], v[80:83]\n"
        "v_mfma_f32_16x16x32_bf16 v[84:87], v[196:199], v[100:103], v[84:87]\n"
        "ds_read_b128 v[96:99], v21 offset:13312\n"
        "ds_read_b128 v[100:103], v21 offset:27648\n"
        "s_waitcnt vmcnt(17) lgkmcnt(2)\n"
        "v_mfma_f32_16x16x32_bf16 v[32:35], v[200:203], v[88:91], v[32:35]\n"
        "v_mfma_f32_16x16x32_bf16 v[36:39], v[204:207], v[88:91], v[36:39]\n"
        "v_mfma_f32_16x16x32_bf16 v[40:43], v[208:211], v[88:91], v[40:43]\n"
        "v_mfma_f32_16x16x32_bf16 v[44:47], v[212:215], v[88:91], v[44:47]\n"
        "v_mfma_f32_16x16x32_bf16 v[48:51], v[216:219], v[88:91], v[48:51]\n"
        "v_mfma_f32_16x16x32_bf16 v[52:55], v[220:223], v[88:91], v[52:55]\n"
        "v_mfma_f32_16x16x32_bf16 v[56:59], v[224:227], v[88:91], v[56:59]\n"
        "v_mfma_f32_16x16x32_bf16 v[60:63], v[200:203], v[92:95], v[60:63]\n"
        "v_mfma_f32_16x16x32_bf16 v[64:67], v[204:207], v[92:95], v[64:67]\n"
        "v_mfma_f32_16x16x32_bf16 v[68:71], v[208:211], v[92:95], v[68:71]\n"
        "v_mfma_f32_16x16x32_bf16 v[72:75], v[212:215], v[92:95], v[72:75]\n"
        "v_mfma_f32_16x16x32_bf16 v[76:79], v[216:219], v[92:95], v[76:79]\n"
        "v_mfma_f32_16x16x32_bf16 v[80:83], v[220:223], v[92:95], v[80:83]\n"
        "v_mfma_f32_16x16x32_bf16 v[84:87], v[224:227], v[92:95], v[84:87]\n"
        "ds_read_b128 v[104:107], v22 offset:14336\n"
        "ds_read_b128 v[108:111], v22 offset:15360\n"
        "ds_read_b128 v[112:115], v22 offset:16384\n"
        "ds_read_b64 v[116:117], v23 offset:14336\n"
        "ds_read_b64 v[118:119], v23\n"
        "ds_read_b128 v[88:91], v22 offset:0\n"
        "ds_read_b128 v[92:95], v22 offset:1024\n"
        "s_waitcnt vmcnt(10) lgkmcnt(7)\n"
        "v_mfma_f32_16x16x32_bf16 v[32:35], v[228:231], v[96:99], v[32:35]\n"
        "v_mfma_f32_16x16x32_bf16 v[36:39], v[232:235], v[96:99], v[36:39]\n"
        "v_mfma_f32_16x16x32_bf16 v[40:43], v[236:239], v[96:99], v[40:43]\n"
        "v_mfma_f32_16x16x32_bf16 v[44:47], v[240:243], v[96:99], v[44:47]\n"
        "v_mfma_f32_16x16x32_bf16 v[48:51], v[244:247], v[96:99], v[48:51]\n"
        "v_mfma_f32_16x16x32_bf16 v[52:55], v[248:251], v[96:99], v[52:55]\n"
        "v_mfma_f32_16x16x32_bf16 v[56:59], v[252:255], v[96:99], v[56:59]\n"
        "v_mfma_f32_16x16x32_bf16 v[60:63], v[228:231], v[100:103], v[60:63]\n"
        "v_mfma_f32_16x16x32_bf16 v[64:67], v[232:235], v[100:103], v[64:67]\n"
        "v_mfma_f32_16x16x32_bf16 v[68:71], v[236:239], v[100:103], v[68:71]\n"
        "v_mfma_f32_16x16x32_bf16 v[72:75], v[240:243], v[100:103], v[72:75]\n"
        "v_mfma_f32_16x16x32_bf16 v[76:79], v[244:247], v[100:103], v[76:79]\n"
        "v_mfma_f32_16x16x32_bf16 v[80:83], v[248:251], v[100:103], v[80:83]\n"
        "v_mfma_f32_16x16x32_bf16 v[84:87], v[252:255], v[100:103], v[84:87]\n"
        "ds_read_b128 v[96:99], v22 offset:2048\n"
        "v_cvt_pk_bf16_f32 v32, v32, v33\n"
        "v_cvt_pk_bf16_f32 v33, v34, v35\n"
        "v_cvt_pk_bf16_f32 v34, v36, v37\n"
        "v_cvt_pk_bf16_f32 v35, v38, v39\n"
        "buffer_store_dwordx4 v[32:35], v16, s[40:43], s50 offen offset:0 sc1\n"
        "v_cvt_pk_bf16_f32 v40, v40, v41\n"
        "v_cvt_pk_bf16_f32 v41, v42, v43\n"
        "v_cvt_pk_bf16_f32 v42, v44, v45\n"
        "v_cvt_pk_bf16_f32 v43, v46, v47\n"
        "buffer_store_dwordx4 v[40:43], v16, s[40:43], s50 offen offset:1024 sc1\n"
        "v_cvt_pk_bf16_f32 v48, v48, v49\n"
        "v_cvt_pk_bf16_f32 v49, v50, v51\n"
        "v_cvt_pk_bf16_f32 v50, v52, v53\n"
        "v_cvt_pk_bf16_f32 v51, v54, v55\n"
        "buffer_store_dwordx4 v[48:51], v16, s[40:43], s50 offen offset:2048 sc1\n"
        "v_cvt_pk_bf16_f32 v28, v56, v57\n"
        "v_cvt_pk_bf16_f32 v29, v58, v59\n"
        "v_cvt_pk_bf16_f32 v30, v84, v85\n"
        "v_cvt_pk_bf16_f32 v31, v86, v87\n"
        "buffer_store_dwordx4 v[28:31], v16, s[40:43], s51 offen sc1\n"
        "v_mov_b32 v56, v28\n"
        "v_mov_b32 v57, v29\n"
        "v_mov_b32 v54, 0\n"
        "v_mov_b32 v55, 0\n"
        "v_mov_b32 v58, 0\n"
        "v_mov_b32 v59, 0\n"
        "s_waitcnt lgkmcnt(0)\n"
        "v_mfma_f32_16x16x32_bf16 v[36:39], v[32:35], v[88:91], 0\n"
        "v_mfma_f32_16x16x32_bf16 v[36:39], v[40:43], v[92:95], v[36:39]\n"
        "v_mfma_f32_16x16x32_bf16 v[36:39], v[48:51], v[96:99], v[36:39]\n"
        "v_mfma_f32_16x16x32_bf16 v[36:39], v[54:57], v[116:119], v[36:39]\n"
        "v_mfma_f32_16x16x32_bf16 v[44:47], v[32:35], v[104:107], 0\n"
        "v_mfma_f32_16x16x32_bf16 v[44:47], v[40:43], v[108:111], v[44:47]\n"
        "v_mfma_f32_16x16x32_bf16 v[44:47], v[48:51], v[112:115], v[44:47]\n"
        "v_mfma_f32_16x16x32_bf16 v[44:47], v[56:59], v[116:119], v[44:47]\n"
        "v_cvt_pk_bf16_f32 v60, v60, v61\n"
        "v_cvt_pk_bf16_f32 v61, v62, v63\n"
        "v_cvt_pk_bf16_f32 v62, v64, v65\n"
        "v_cvt_pk_bf16_f32 v63, v66, v67\n"
        "buffer_store_dwordx4 v[60:63], v16, s[40:43], s52 offen offset:0 sc1\n"
        "v_cvt_pk_bf16_f32 v68, v68, v69\n"
        "v_cvt_pk_bf16_f32 v69, v70, v71\n"
        "v_cvt_pk_bf16_f32 v70, v72, v73\n"
        "v_cvt_pk_bf16_f32 v71, v74, v75\n"
        "buffer_store_dwordx4 v[68:71], v16, s[40:43], s52 offen offset:1024 sc1\n"
        "v_cvt_pk_bf16_f32 v76, v76, v77\n"
        "v_cvt_pk_bf16_f32 v77, v78, v79\n"
        "v_cvt_pk_bf16_f32 v78, v80, v81\n"
        "v_cvt_pk_bf16_f32 v79, v82, v83\n"
        "buffer_store_dwordx4 v[76:79], v16, s[40:43], s52 offen offset:2048 sc1\n"
        "v_mov_b32 v84, v30\n"
        "v_mov_b32 v85, v31\n"
        "v_mov_b32 v86, 0\n"
        "v_mov_b32 v87, 0\n"
        "v_mfma_f32_16x16x32_bf16 v[52:55], v[60:63], v[104:107], 0\n"
        "v_mfma_f32_16x16x32_bf16 v[52:55], v[68:71], v[108:111], v[52:55]\n"
        "v_mfma_f32_16x16x32_bf16 v[52:55], v[76:79], v[112:115], v[52:55]\n"
        "v_mfma_f32_16x16x32_bf16 v[52:55], v[84:87], v[116:119], v[52:55]\n"
        "ds_write_b128 v24, v[36:39] offset:0\n"
        "ds_write_b128 v24, v[44:47] offset:1024\n"
        "s_nop 5\n"
        "ds_write_b128 v24, v[52:55] offset:2048\n"
        "s_waitcnt lgkmcnt(0)\n"
        "s_barrier\n"
        "s_cmp_eq_u32 %6, 3\n"
        "s_cbranch_scc1 .Lw0_sk_%=\n"
        "ds_read_b128 v[88:91], v26 offset:0\n"
        "ds_read_b128 v[92:95], v26 offset:3072\n"
        "ds_read_b128 v[96:99], v26 offset:6144\n"
        "ds_read_b128 v[100:103], v26 offset:9216\n"
        "s_waitcnt lgkmcnt(2)\n"
        "v_pk_add_f32 v[88:89], v[88:89], v[92:93]\n"
        "v_pk_add_f32 v[90:91], v[90:91], v[94:95]\n"
        "s_waitcnt lgkmcnt(0)\n"
        "v_pk_add_f32 v[96:97], v[96:97], v[100:101]\n"
        "v_pk_add_f32 v[98:99], v[98:99], v[102:103]\n"
        "v_pk_add_f32 v[88:89], v[88:89], v[96:97]\n"
        "v_pk_add_f32 v[90:91], v[90:91], v[98:99]\n"
        "buffer_store_dwordx4 v[88:91], v25, s[40:43], s55 offen sc1\n"
        ".Lw0_sk_%=:\n"
        "v_xor_b32 v21, 0x7000, v21\n"
        "v_mov_b32 v104, 1\n"
        "s_mov_b32 s44, s47\n"
        "s_waitcnt vmcnt(0)\n"
        "s_barrier\n"
        "buffer_store_dword v104, v18, s[40:43], s54 offen sc1\n"
        "s_cmp_lt_u32 s44, 64\n"
        "s_cbranch_scc1 .Lw0_loop_%=\n"
        "s_waitcnt vmcnt(0)\n"
            :
            : "s"(lo), "s"(hi), "s"(WS_BYTES), "s"(rep), "s"(nrep), "v"(tab), "s"(wv), "s"(OFF_WC + (14 * set + pb) * 28672u),
              "s"(OFF_WC + (14 * set + ph) * 28672u + half * 64), "s"(OFF_KQF + set * 14336u + pb * 1024), "s"(OFF_KQF + set * 14336u + ph * 1024 + half * 229376u),
              "s"(pb * 1024), "s"(ph * 1024 + half * 8), "s"(OFF_SXX + set * 3072u),
              "v"(vL), "v"(g * 512 + (n >> 2) * 128 + (n & 3) * 16), "v"(vF), "v"(oblk(q0 >> 1) + (q0 & 1) * 4096 + set * 256 + n * 16),
              "v"(q1 < 28 ? oblk(q1 >> 1) + (q1 & 1) * 4096 + set * 256 + n * 16 : OOR),
              "v"(tid < 80 ? q0 * BLK + set * 256 + n * 16 : (wv != 2 || lane >= 40 ? OOR : (lane < 32 ? B_MEMH + set * 512 + lane * 16 : B_TRG + set * 128 + n * 16)))
            : "memory", "vcc", "scc", "s40", "s41", "s42", "s43", "s44", "s45", "s46", "s47", "s48", "s49", "s50", "s51", "s52", "s53", "s54", "s55", "s56", "s57", "s58", "s59", "s60", "s61", "s62", "s63", "s64", "s65", "s66", "s67", "s68", "s69", "s70", "s71", "s72", "s73", "s74", "s75", "s76", "s77", "s78", "s79", "v16", "v17", "v18", "v19", "v20", "v21", "v22", "v23", "v24", "v25", "v26", "v27", "v28", "v29", "v30", "v31", "v32", "v33", "v34", "v35", "v36", "v37", "v38", "v39", "v40", "v41", "v42", "v43", "v44", "v45", "v46", "v47", "v48", "v49", "v50", "v51", "v52", "v53", "v54", "v55", "v56", "v57", "v58", "v59", "v60", "v61", "v62", "v63", "v64", "v65", "v66", "v67", "v68", "v69", "v70", "v71", "v72", "v73", "v74", "v75", "v76", "v77", "v78", "v79", "v80", "v81", "v82", "v83", "v84", "v85", "v86", "v87", "v88", "v89", "v90", "v91", "v92", "v93", "v94", "v95", "v96", "v97", "v98", "v99", "v100", "v101", "v102", "v103", "v104", "v105", "v106", "v107", "v108", "v109", "v110", "v111", "v112", "v113", "v114", "v115", "v116", "v117", "v118", "v119", "v120", "v121", "v122", "v123", "v124", "v125", "v126", "v127", "v128", "v129", "v130", "v131", "v132", "v133", "v134", "v135", "v136", "v137", "v138", "v139", "v140", "v141", "v142", "v143", "v144", "v145", "v146", "v147", "v148", "v149", "v150", "v151", "v152", "v153", "v154", "v155", "v156", "v157", "v158", "v159", "v160", "v161", "v162", "v163", "v164", "v165", "v166", "v167", "v168", "v169", "v170", "v171", "v172", "v173", "v174", "v175", "v176", "v177", "v178", "v179", "v180", "v181", "v182", "v183", "v184", "v185", "v186", "v187", "v188", "v189", "v190", "v191", "v192", "v193", "v194", "v195", "v196", "v197", "v198", "v199", "v200", "v201", "v202", "v203", "v204", "v205", "v206", "v207", "v208", "v209", "v210", "v211", "v212", "v213", "v214", "v215", "v216", "v217", "v218", "v219", "v220", "v221", "v222", "v223", "v224", "v225", "v226", "v227", "v228", "v229", "v230", "v231", "v232", "v233", "v234", "v235", "v236", "v237", "v238", "v239", "v240", "v241", "v242", "v243", "v244", "v245", "v246", "v247", "v248", "v249", "v250", "v251", "v252", "v253", "v254", "v255", "a0", "a1", "a2", "a3", "a4", "a5", "a6", "a7", "a8", "a9", "a10", "a11", "a12", "a13", "a14", "a15", "a16", "a17", "a18", "a19", "a20", "a21", "a22", "a23", "a24", "a25", "a26", "a27", "a28", "a29", "a30", "a31", "a32", "a33", "a34", "a35", "a36", "a37", "a38", "a39", "a40", "a41", "a42", "a43", "a44", "a45", "a46", "a47", "a48", "a49", "a50", "a51", "a52", "a53", "a54", "a55", "a56", "a57", "a58", "a59", "a60", "a61", "a62", "a63", "a64", "a65", "a66", "a67", "a68", "a69", "a70", "a71", "a72", "a73", "a74", "a75", "a76", "a77", "a78", "a79", "a80", "a81", "a82", "a83", "a84", "a85", "a86", "a87", "a88", "a89", "a90", "a91", "a92", "a93", "a94", "a95", "a96", "a97", "a98", "a99", "a100", "a101", "a102", "a103", "a104", "a105", "a106", "a107", "a108", "a109", "a110", "a111", "a112", "a113", "a114", "a115", "a116", "a117", "a118", "a119", "a120", "a121", "a122", "a123", "a124", "a125", "a126", "a127", "a128", "a129", "a130", "a131", "a132", "a133", "a134", "a135", "a136", "a137", "a138", "a139", "a140", "a141", "a142", "a143", "a144", "a145", "a146", "a147", "a148", "a149", "a150", "a151", "a152", "a153", "a154", "a155", "a156", "a157", "a158", "a159", "a160", "a161", "a162", "a163", "a164", "a165", "a166", "a167", "a168", "a169", "a170", "a171", "a172", "a173", "a174", "a175", "a176", "a177", "a178", "a179", "a180", "a181", "a182", "a183", "a184", "a185", "a186", "a187", "a188", "a189", "a190", "a191", "a192", "a193", "a194", "a195", "a196", "a197", "a198", "a199", "a200", "a201", "a202", "a203", "a204", "a205", "a206", "a207", "a208", "a209", "a210", "a211", "a212", "a213", "a214", "a215", "a216", "a217", "a218", "a219", "a220", "a221", "a222", "a223", "a224", "a225", "a226", "a227", "a228", "a229", "a230", "a231", "a232", "a233", "a234", "a235", "a236", "a237", "a238", "a239", "a240", "a241", "a242", "a243", "a244", "a245", "a246", "a247", "a248", "a249", "a250", "a251", "a252", "a253", "a254", "a255");
    }
    if (ROLE == 1) {
        asm volatile(
        "s_mov_b32 s40, %0\n"
        "s_and_b32 s41, %1, 0xffff\n"
        "s_mov_b32 s42, %2\n"
        "s_mov_b32 s43, 0x00020000\n"
        "s_mov_b32 s44, %3\n"
        "s_mul_i32 s45, %6, 7168\n"
        "s_add_u32 s46, s45, 97779712\n"
        "s_mov_b32 s74, 0xbfb8aa3b\n"
        "s_mov_b32 s75, 0xbfb8aa3b\n"
        "v_mov_b32 v16, %14\n"
        "v_mov_b32 v17, %15\n"
        "v_mov_b32 v18, %16\n"
        "v_mov_b32 v19, %17\n"
        "v_mov_b32 v20, %18\n"
        "v_mov_b32 v21, v16\n"
        "s_mul_i32 s57, s44, 28672\n"
        "s_add_u32 s57, s57, s46\n"
        "s_add_u32 s58, s57, 4096\n"
        "s_mov_b32 m0, s45\n"
        "s_nop 0\n"
        "buffer_load_dwordx4 v16, s[40:43], s57 offen offset:0 lds\n"
        "buffer_load_dwordx4 v16, s[40:43], s57 offen offset:1024 lds\n"
        "buffer_load_dwordx4 v16, s[40:43], s57 offen offset:2048 lds\n"
        "buffer_load_dwordx4 v16, s[40:43], s57 offen offset:3072 lds\n"
        "s_add_u32 m0, s45, 4096\n"
        "s_nop 0\n"
        "buffer_load_dwordx4 v16, s[40:43], s58 offen offset:0 lds\n"
        "buffer_load_dwordx4 v16, s[40:43], s58 offen offset:1024 lds\n"
        "buffer_load_dwordx4 v16, s[40:43], s58 offen offset:2048 lds\n"
        "s_mov_b32 s60, %7\n"
        "s_add_u32 s61, s60, 28672\n"
        "s_add_u32 s62, s60, 57344\n"
        "s_mov_b32 s63, %8\n"
        "buffer_load_dwordx4 a[0:3], v17, s[40:43], s60 offen offset:0\n"
        "buffer_load_dwordx4 a[4:7], v17, s[40:43], s60 offen offset:256\n"
        "buffer_load_dwordx4 a[8:11], v17, s[40:43], s61 offen offset:0\n"
        "buffer_load_dwordx4 a[12:15], v17, s[40:43], s61 offen offset:256\n"
        "buffer_load_dwordx4 a[16:19], v17, s[40:43], s62 offen offset:0\n"
        "buffer_load_dwordx4 a[20:23], v17, s[40:43], s62 offen offset:256\n"
        "buffer_load_dwordx4 a[24:27], v17, s[40:43], s63 offen offset:0\n"
        "buffer_load_dwordx4 a[28:31], v17, s[40:43], s60 offen offset:2048\n"
        "buffer_load_dwordx4 a[32:35], v17, s[40:43], s60 offen offset:2304\n"
        "buffer_load_dwordx4 a[36:39], v17, s[40:43], s61 offen offset:2048\n"
        "buffer_load_dwordx4 a[40:43], v17, s[40:43], s61 offen offset:2304\n"
        "buffer_load_dwordx4 a[44:47], v17, s[40:43], s62 offen offset:2048\n"
        "buffer_load_dwordx4 a[48:51], v17, s[40:43], s62 offen offset:2304\n"
        "buffer_load_dwordx4 a[52:55], v17, s[40:43], s63 offen offset:2048\n"
        "s_add_u32 s60, s60, 4096\n"
        "s_add_u32 s61, s61, 4096\n"
        "s_add_u32 s62, s62, 4096\n"
        "s_add_u32 s63, s63, 4096\n"
        "buffer_load_dwordx4 a[56:59], v17, s[40:43], s60 offen offset:0\n"
        "buffer_load_dwordx4 a[60:63], v17, s[40:43], s60 offen offset:256\n"
        "buffer_load_dwordx4 a[64:67], v17, s[40:43], s61 offen offset:0\n"
        "buffer_load_dwordx4 a[68:71], v17, s[40:43], s61 offen offset:256\n"
        "buffer_load_dwordx4 a[72:75], v17, s[40:43], s62 offen offset:0\n"
        "buffer_load_dwordx4 a[76:79], v17, s[40:43], s62 offen offset:256\n"
        "buffer_load_dwordx4 a[80:83], v17, s[40:43], s63 offen offset:0\n"
        "buffer_load_dwordx4 a[84:87], v17, s[40:43], s60 offen offset:2048\n"
        "buffer_load_dwordx4 a[88:91], v17, s[40:43], s60 offen offset:2304\n"
        "buffer_load_dwordx4 a[92:95], v17, s[40:43], s61 offen offset:2048\n"
        "buffer_load_dwordx4 a[96:99], v17, s[40:43], s61 offen offset:2304\n"
        "buffer_load_dwordx4 a[100:103], v17, s[40:43], s62 offen offset:2048\n"
        "buffer_load_dwordx4 a[104:107], v17, s[40:43], s62 offen offset:2304\n"
        "buffer_load_dwordx4 a[108:111], v17, s[40:43], s63 offen offset:2048\n"
        "s_add_u32 s60, s60, 4096\n"
        "s_add_u32 s61, s61, 4096\n"
        "s_add_u32 s62, s62, 4096\n"
        "s_add_u32 s63, s63, 4096\n"
        "buffer_load_dwordx4 a[112:115], v17, s[40:43], s60 offen offset:0\n"
        "buffer_load_dwordx4 a[116:119], v17, s[40:43], s60 offen offset:256\n"
        "buffer_load_dwordx4 a[120:123], v17, s[40:43], s61 offen offset:0\n"
        "buffer_load_dwordx4 a[124:127], v17, s[40:43], s61 offen offset:256\n"
        "buffer_load_dwordx4 a[128:131], v17, s[40:43], s62 offen offset:0\n"
        "buffer_load_dwordx4 a[132:135], v17, s[40:43], s62 offen offset:256\n"
        "buffer_load_dwordx4 a[136:139], v17, s[40:43], s63 offen offset:0\n"
        "buffer_load_dwordx4 a[140:143], v17, s[40:43], s60 offen offset:2048\n"
        "buffer_load_dwordx4 a[144:147], v17, s[40:43], s60 offen offset:2304\n"
        "buffer_load_dwordx4 a[148:151], v17, s[40:43], s61 offen offset:2048\n"
        "buffer_load_dwordx4 a[152:155], v17, s[40:43], s61 offen offset:2304\n"
        "buffer_load_dwordx4 a[156:159], v17, s[40:43], s62 offen offset:2048\n"
        "buffer_load_dwordx4 a[160:163], v17, s[40:43], s62 offen offset:2304\n"
        "buffer_load_dwordx4 a[164:167], v17, s[40:43], s63 offen offset:2048\n"
        "s_add_u32 s60, s60, 4096\n"
        "s_add_u32 s61, s61, 4096\n"
        "s_add_u32 s62, s62, 4096\n"
        "s_add_u32 s63, s63, 4096\n"
        "buffer_load_dwordx4 a[168:171], v17, s[40:43], s60 offen offset:0\n"
        "buffer_load_dwordx4 a[172:175], v17, s[40:43], s60 offen offset:256\n"
        "buffer_load_dwordx4 a[176:179], v17, s[40:43], s61 offen offset:0\n"
        "buffer_load_dwordx4 a[180:183], v17, s[40:43], s61 offen offset:256\n"
        "buffer_load_dwordx4 a[184:187], v17, s[40:43], s62 offen offset:0\n"
        "buffer_load_dwordx4 a[188:191], v17, s[40:43], s62 offen offset:256\n"
        "buffer_load_dwordx4 a[192:195], v17, s[40:43], s63 offen offset:0\n"
        "buffer_load_dwordx4 a[196:199], v17, s[40:43], s60 offen offset:2048\n"
        "buffer_load_dwordx4 a[200:203], v17, s[40:43], s60 offen offset:2304\n"
        "buffer_load_dwordx4 a[204:207], v17, s[40:43], s61 offen offset:2048\n"
        "buffer_load_dwordx4 a[208:211], v17, s[40:43], s61 offen offset:2304\n"
        "buffer_load_dwordx4 a[212:215], v17, s[40:43], s62 offen offset:2048\n"
        "buffer_load_dwordx4 a[216:219], v17, s[40:43], s62 offen offset:2304\n"
        "buffer_load_dwordx4 a[220:223], v17, s[40:43], s63 offen offset:2048\n"
        "s_add_u32 s60, s60, 4096\n"
        "s_add_u32 s61, s61, 4096\n"
        "s_add_u32 s62, s62, 4096\n"
        "s_add_u32 s63, s63, 4096\n"
        "buffer_load_dwordx4 a[224:227], v17, s[40:43], s60 offen offset:0\n"
        "buffer_load_dwordx4 a[228:231], v17, s[40:43], s60 offen offset:256\n"
        "buffer_load_dwordx4 a[232:235], v17, s[40:43], s61 offen offset:0\n"
        "buffer_load_dwordx4 a[236:239], v17, s[40:43], s61 offen offset:256\n"
        "buffer_load_dwordx4 a[240:243], v17, s[40:43], s62 offen offset:0\n"
        "buffer_load_dwordx4 a[244:247], v17, s[40:43], s62 offen offset:256\n"
        "buffer_load_dwordx4 a[248:251], v17, s[40:43], s63 offen offset:0\n"
        "buffer_load_dwordx4 a[252:255], v17, s[40:43], s60 offen offset:2048\n"
        "buffer_load_dwordx4 v[120:123], v17, s[40:43], s60 offen offset:2304\n"
        "buffer_load_dwordx4 v[124:127], v17, s[40:43], s61 offen offset:2048\n"
        "buffer_load_dwordx4 v[128:131], v17, s[40:43], s61 offen offset:2304\n"
        "buffer_load_dwordx4 v[132:135], v17, s[40:43], s62 offen offset:2048\n"
        "buffer_load_dwordx4 v[136:139], v17, s[40:43], s62 offen offset:2304\n"
        "buffer_load_dwordx4 v[140:143], v17, s[40:43], s63 offen offset:2048\n"
        "s_add_u32 s60, s60, 4096\n"
        "s_add_u32 s61, s61, 4096\n"
        "s_add_u32 s62, s62, 4096\n"
        "s_add_u32 s63, s63, 4096\n"
        "buffer_load_dwordx4 v[144:147], v17, s[40:43], s60 offen offset:0\n"
        "buffer_load_dwordx4 v[148:151], v17, s[40:43], s60 offen offset:256\n"
        "buffer_load_dwordx4 v[152:155], v17, s[40:43], s61 offen offset:0\n"
        "buffer_load_dwordx4 v[156:159], v17, s[40:43], s61 offen offset:256\n"
        "buffer_load_dwordx4 v[160:163], v17, s[40:43], s62 offen offset:0\n"
        "buffer_load_dwordx4 v[164:167], v17, s[40:43], s62 offen offset:256\n"
        "buffer_load_dwordx4 v[168:171], v17, s[40:43], s63 offen offset:0\n"
        "buffer_load_dwordx4 v[172:175], v17, s[40:43], s60 offen offset:2048\n"
        "buffer_load_dwordx4 v[176:179], v17, s[40:43], s60 offen offset:2304\n"
        "buffer_load_dwordx4 v[180:183], v17, s[40:43], s61 offen offset:2048\n"
        "buffer_load_dwordx4 v[184:187], v17, s[40:43], s61 offen offset:2304\n"
        "buffer_load_dwordx4 v[188:191], v17, s[40:43], s62 offen offset:2048\n"
        "buffer_load_dwordx4 v[192:195], v17, s[40:43], s62 offen offset:2304\n"
        "buffer_load_dwordx4 v[196:199], v17, s[40:43], s63 offen offset:2048\n"
        "s_add_u32 s60, s60, 4096\n"
        "s_add_u32 s61, s61, 4096\n"
        "s_add_u32 s62, s62, 4096\n"
        "s_add_u32 s63, s63, 4096\n"
        "buffer_load_dwordx4 v[200:203], v17, s[40:43], s60 offen offset:0\n"
        "buffer_load_dwordx4 v[204:207], v17, s[40:43], s60 offen offset:256\n"
        "buffer_load_dwordx4 v[208:211], v17, s[40:43], s61 offen offset:0\n"
        "buffer_load_dwordx4 v[212:215], v17, s[40:43], s61 offen offset:256\n"
        "buffer_load_dwordx4 v[216:219], v17, s[40:43], s62 offen offset:0\n"
        "buffer_load_dwordx4 v[220:223], v17, s[40:43], s62 offen offset:256\n"
        "buffer_load_dwordx4 v[224:227], v17, s[40:43], s63 offen offset:0\n"
        "buffer_load_dwordx4 v[228:231], v17, s[40:43], s60 offen offset:2048\n"
        "buffer_load_dwordx4 v[232:235], v17, s[40:43], s60 offen offset:2304\n"
        "buffer_load_dwordx4 v[236:239], v17, s[40:43], s61 offen offset:2048\n"
        "buffer_load_dwordx4 v[240:243], v17, s[40:43], s61 offen offset:2304\n"
        "buffer_load_dwordx4 v[244:247], v17, s[40:43], s62 offen offset:2048\n"
        "buffer_load_dwordx4 v[248:251], v17, s[40:43], s62 offen offset:2304\n"
        "buffer_load_dwordx4 v[252:255], v17, s[40:43], s63 offen offset:2048\n"
        "s_add_u32 s60, s60, 4096\n"
        "s_add_u32 s61, s61, 4096\n"
        "s_add_u32 s62, s62, 4096\n"
        "s_add_u32 s63, s63, 4096\n"
        "s_waitcnt vmcnt(63)\n"
        "s_mov_b32 s59, 28672\n"
        "s_barrier\n"
        ".Lw1_loop_%=:\n"
        "ds_read_b128 v[88:91], v21 offset:0\n"
        "ds_read_b128 v[92:95], v21 offset:14336\n"
        "s_add_u32 s45, s45, s59\n"
        "s_sub_u32 s59, 0, s59\n"
        "s_add_u32 s47, s44, %4\n"
        "s_min_u32 s48, s47, 63\n"
        "s_mul_i32 s57, s48, 28672\n"
        "s_add_u32 s57, s57, s46\n"
        "s_add_u32 s58, s57, 4096\n"
        "s_mov_b32 m0, s45\n"
        "s_nop 0\n"
        "buffer_load_dwordx4 v16, s[40:43], s57 offen offset:0 lds\n"
        "buffer_load_dwordx4 v16, s[40:43], s57 offen offset:1024 lds\n"
        "buffer_load_dwordx4 v16, s[40:43], s57 offen offset:2048 lds\n"
        "buffer_load_dwordx4 v16, s[40:43], s57 offen offset:3072 lds\n"
        "s_add_u32 m0, s45, 4096\n"
        "s_nop 0\n"
        "buffer_load_dwordx4 v16, s[40:43], s58 offen offset:0 lds\n"
        "buffer_load_dwordx4 v16, s[40:43], s58 offen offset:1024 lds\n"
        "buffer_load_dwordx4 v16, s[40:43], s58 offen offset:2048 lds\n"
        "s_mul_i32 s57, s44, 458752\n"
        "s_add_u32 s50, s57, %9\n"
        "s_add_u32 s51, s57, %10\n"
        "s_add_u32 s52, s50, 4096\n"
        "s_add_u32 s53, s51, 229376\n"
        "s_lshl_b32 s54, s44, 9\n"
        "ds_read_b128 v[96:99], v21 offset:1024\n"
        "ds_read_b128 v[100:103], v21 offset:15360\n"
        "s_waitcnt lgkmcnt(2)\n"
        "v_mfma_f32_16x16x32_bf16 v[32:35], v[88:91], a[0:3], 0\n"
        "v_mfma_f32_16x16x32_bf16 v[36:39], v[88:91], a[4:7], 0\n"
        "v_mfma_f32_16x16x32_bf16 v[40:43], v[88:91], a[8:11], 0\n"
        "v_mfma_f32_16x16x32_bf16 v[44:47], v[88:91], a[12:15], 0\n"
        "v_mfma_f32_16x16x32_bf16 v[48:51], v[88:91], a[16:19], 0\n"
        "v_mfma_f32_16x16x32_bf16 v[52:55], v[88:91], a[20:23], 0\n"
        "v_mfma_f32_16x16x32_bf16 v[56:59], v[88:91], a[24:27], 0\n"
        "v_mfma_f32_16x16x32_bf16 v[60:63], v[92:95], a[0:3], 0\n"
        "v_mfma_f32_16x16x32_bf16 v[64:67], v[92:95], a[4:7], 0\n"
        "v_mfma_f32_16x16x32_bf16 v[68:71], v[92:95], a[8:11], 0\n"
        "v_mfma_f32_16x16x32_bf16 v[72:75], v[92:95], a[12:15], 0\n"
        "v_mfma_f32_16x16x32_bf16 v[76:79], v[92:95], a[16:19], 0\n"
        "v_mfma_f32_16x16x32_bf16 v[80:83], v[92:95], a[20:23], 0\n"
        "v_mfma_f32_16x16x32_bf16 v[84:87], v[92:95], a[24:27], 0\n"
        "ds_read_b128 v[88:91], v21 offset:2048\n"
        "ds_read_b128 v[92:95], v21 offset:16384\n"
        "s_waitcnt lgkmcnt(2)\n"
        "v_mfma_f32_16x16x32_bf16 v[32:35], v[96:99], a[28:31], v[32:35]\n"
        "v_mfma_f32_16x16x32_bf16 v[36:39], v[96:99], a[32:35], v[36:39]\n"
        "v_mfma_f32_16x16x32_bf16 v[40:43], v[96:99], a[36:39], v[40:43]\n"
        "v_mfma_f32_16x16x32_bf16 v[44:47], v[96:99], a[40:43], v[44:47]\n"
        "v_mfma_f32_16x16x32_bf16 v[48:51], v[96:99], a[44:47], v[48:51]\n"
        "v_mfma_f32_16x16x32_bf16 v[52:55], v[96:99], a[48:51], v[52:55]\n"
        "v_mfma_f32_16x16x32_bf16 v[56:59], v[96:99], a[52:55], v[56:59]\n"
        "v_mfma_f32_16x16x32_bf16 v[60:63], v[100:103], a[28:31], v[60:63]\n"
        "v_mfma_f32_16x16x32_bf16 v[64:67], v[100:103], a[32:35], v[64:67]\n"
        "v_mfma_f32_16x16x32_bf16 v[68:71], v[100:103], a[36:39], v[68:71]\n"
        "v_mfma_f32_16x16x32_bf16 v[72:75], v[100:103], a[40:43], v[72:75]\n"
        "v_mfma_f32_16x16x32_bf16 v[76:79], v[100:103], a[44:47], v[76:79]\n"
        "v_mfma_f32_16x16x32_bf16 v[80:83], v[100:103], a[48:51], v[80:83]\n"
        "v_mfma_f32_16x16x32_bf16 v[84:87], v[100:103], a[52:55], v[84:87]\n"
        "ds_read_b128 v[96:99], v21 offset:3072\n"
        "ds_read_b128 v[100:103], v21 offset:17408\n"
        "s_waitcnt lgkmcnt(2)\n"
        "v_mfma_f32_16x16x32_bf16 v[32:35], v[88:91], a[56:59], v[32:35]\n"
        "v_mfma_f32_16x16x32_bf16 v[36:39], v[88:91], a[60:63], v[36:39]\n"
        "v_mfma_f32_16x16x32_bf16 v[40:43], v[88:91], a[64:67], v[40:43]\n"
        "v_mfma_f32_16x16x32_bf16 v[44:47], v[88:91], a[68:71], v[44:47]\n"
        "v_mfma_f32_16x16x32_bf16 v[48:51], v[88:91], a[72:75], v[48:51]\n"
        "v_mfma_f32_16x16x32_bf16 v[52:55], v[88:91], a[76:79], v[52:55]\n"
        "v_mfma_f32_16x16x32_bf16 v[56:59], v[88:91], a[80:83], v[56:59]\n"
        "v_mfma_f32_16x16x32_bf16 v[60:63], v[92:95], a[56:59], v[60:63]\n"
        "v_mfma_f32_16x16x32_bf16 v[64:67], v[92:95], a[60:63], v[64:67]\n"
        "v_mfma_f32_16x16x32_bf16 v[68:71], v[92:95], a[64:67], v[68:71]\n"
        "v_mfma_f32_16x16x32_bf16 v[72:75], v[92:95], a[68:71], v[72:75]\n"
        "v_mfma_f32_16x16x32_bf16 v[76:79], v[92:95], a[72:75], v[76:79]\n"
        "v_mfma_f32_16x16x32_bf16 v[80:83], v[92:95], a[76:79], v[80:83]\n"
        "v_mfma_f32_16x16x32_bf16 v[84:87], v[92:95], a[80:83], v[84:87]\n"
        "ds_read_b128 v[88:91], v21 offset:4096\n"
        "ds_read_b128 v[92:95], v21 offset:18432\n"
        "s_waitcnt lgkmcnt(2)\n"
        "v_mfma_f32_16x16x32_bf16 v[32:35], v[96:99], a[84:87], v[32:35]\n"
        "v_mfma_f32_16x16x32_bf16 v[36:39], v[96:99], a[88:91], v[36:39]\n"
        "v_mfma_f32_16x16x32_bf16 v[40:43], v[96:99], a[92:95], v[40:43]\n"
        "v_mfma_f32_16x16x32_bf16 v[44:47], v[96:99], a[96:99], v[44:47]\n"
        "v_mfma_f32_16x16x32_bf16 v[48:51], v[96:99], a[100:103], v[48:51]\n"
        "v_mfma_f32_16x16x32_bf16 v[52:55], v[96:99], a[104:107], v[52:55]\n"
        "v_mfma_f32_16x16x32_bf16 v[56:59], v[96:99], a[108:111], v[56:59]\n"
        "v_mfma_f32_16x16x32_bf16 v[60:63], v[100:103], a[84:87], v[60:63]\n"
        "v_mfma_f32_16x16x32_bf16 v[64:67], v[100:103], a[88:91], v[64:67]\n"
        "v_mfma_f32_16x16x32_bf16 v[68:71], v[100:103], a[92:95], v[68:71]\n"
        "v_mfma_f32_16x16x32_bf16 v[72:75], v[100:103], a[96:99], v[72:75]\n"
        "v_mfma_f32_16x16x32_bf16 v[76:79], v[100:103], a[100:103], v[76:79]\n"
        "v_mfma_f32_16x16x32_bf16 v[80:83], v[100:103], a[104:107], v[80:83]\n"
        "v_mfma_f32_16x16x32_bf16 v[84:87], v[100:103], a[108:111], v[84:87]\n"
        "ds_read_b128 v[96:99], v21 offset:5120\n"
        "ds_read_b128 v[100:103], v21 offset:19456\n"
        "s_waitcnt lgkmcnt(2)\n"
        "v_mfma_f32_16x16x32_bf16 v[32:35], v[88:91], a[112:115], v[32:35]\n"
        "v_mfma_f32_16x16x32_bf16 v[36:39], v[88:91], a[116:119], v[36:39]\n"
        "v_mfma_f32_16x16x32_bf16 v[40:43], v[88:91], a[120:123], v[40:43]\n"
        "v_mfma_f32_16x16x32_bf16 v[44:47], v[88:91], a[124:127], v[44:47]\n"
        "v_mfma_f32_16x16x32_bf16 v[48:51], v[88:91], a[128:131], v[48:51]\n"
        "v_mfma_f32_16x16x32_bf16 v[52:55], v[88:91], a[132:135], v[52:55]\n"
        "v_mfma_f32_16x16x32_bf16 v[56:59], v[88:91], a[136:139], v[56:59]\n"
        "v_mfma_f32_16x16x32_bf16 v[60:63], v[92:95], a[112:115], v[60:63]\n"
        "v_mfma_f32_16x16x32_bf16 v[64:67], v[92:95], a[116:119], v[64:67]\n"
        "v_mfma_f32_16x16x32_bf16 v[68:71], v[92:95], a[120:123], v[68:71]\n"
        "v_mfma_f32_16x16x32_bf16 v[72:75], v[92:95], a[124:127], v[72:75]\n"
        "v_mfma_f32_16x16x32_bf16 v[76:79], v[92:95], a[128:131], v[76:79]\n"
        "v_mfma_f32_16x16x32_bf16 v[80:83], v[92:95], a[132:135], v[80:83]\n"
        "v_mfma_f32_16x16x32_bf16 v[84:87], v[92:95], a[136:139], v[84:87]\n"
        "ds_read_b128 v[88:91], v21 offset:6144\n"
        "ds_read_b128 v[92:95], v21 offset:20480\n"
        "s_waitcnt lgkmcnt(2)\n"
        "v_mfma_f32_16x16x32_bf16 v[32:35], v[96:99], a[140:143], v[32:35]\n"
        "v_mfma_f32_16x16x32_bf16 v[36:39], v[96:99], a[144:147], v[36:39]\n"
        "v_mfma_f32_16x16x32_bf16 v[40:43], v[96:99], a[148:151], v[40:43]\n"
        "v_mfma_f32_16x16x32_bf16 v[44:47], v[96:99], a[152:155], v[44:47]\n"
        "v_mfma_f32_16x16x32_bf16 v[48:51], v[96:99], a[156:159], v[48:51]\n"
        "v_mfma_f32_16x16x32_bf16 v[52:55], v[96:99], a[160:163], v[52:55]\n"
        "v_mfma_f32_16x16x32_bf16 v[56:59], v[96:99], a[164:167], v[56:59]\n"
        "v_mfma_f32_16x16x32_bf16 v[60:63], v[100:103], a[140:143], v[60:63]\n"
        "v_mfma_f32_16x16x32_bf16 v[64:67], v[100:103], a[144:147], v[64:67]\n"
        "v_mfma_f32_16x16x32_bf16 v[68:71], v[100:103], a[148:151], v[68:71]\n"
        "v_mfma_f32_16x16x32_bf16 v[72:75], v[100:103], a[152:155], v[72:75]\n"
        "v_mfma_f32_16x16x32_bf16 v[76:79], v[100:103], a[156:159], v[76:79]\n"
        "v_mfma_f32_16x16x32_bf16 v[80:83], v[100:103], a[160:163], v[80:83]\n"
        "v_mfma_f32_16x16x32_bf16 v[84:87], v[100:103], a[164:167], v[84:87]\n"
        "ds_read_b128 v[96:99], v21 offset:7168\n"
        "ds_read_b128 v[100:103], v21 offset:21504\n"
        "s_waitcnt vmcnt(56) lgkmcnt(2)\n"
        "v_mfma_f32_16x16x32_bf16 v[32:35], v[88:91], a[168:171], v[32:35]\n"
        "v_mfma_f32_16x16x32_bf16 v[36:39], v[88:91], a[172:175], v[36:39]\n"
        "v_mfma_f32_16x16x32_bf16 v[40:43], v[88:91], a[176:179], v[40:43]\n"
        "v_mfma_f32_16x16x32_bf16 v[44:47], v[88:91], a[180:183], v[44:47]\n"
        "v_mfma_f32_16x16x32_bf16 v[48:51], v[88:91], a[184:187], v[48:51]\n"
        "v_mfma_f32_16x16x32_bf16 v[52:55], v[88:91], a[188:191], v[52:55]\n"
        "v_mfma_f32_16x16x32_bf16 v[56:59], v[88:91], a[192:195], v[56:59]\n"
        "v_mfma_f32_16x16x32_bf16 v[60:63], v[92:95], a[168:171], v[60:63]\n"
        "v_mfma_f32_16x16x32_bf16 v[64:67], v[92:95], a[172:175], v[64:67]\n"
        "v_mfma_f32_16x16x32_bf16 v[68:71], v[92:95], a[176:179], v[68:71]\n"
        "v_mfma_f32_16x16x32_bf16 v[72:75], v[92:95], a[180:183], v[72:75]\n"
        "v_mfma_f32_16x16x32_bf16 v[76:79], v[92:95], a[184:187], v[76:79]\n"
        "v_mfma_f32_16x16x32_bf16 v[80:83], v[92:95], a[188:191], v[80:83]\n"
        "v_mfma_f32_16x16x32_bf16 v[84:87], v[92:95], a[192:195], v[84:87]\n"
        "ds_read_b128 v[88:91], v21 offset:8192\n"
        "ds_read_b128 v[92:95], v21 offset:22528\n"
        "s_waitcnt vmcnt(49) lgkmcnt(2)\n"
        "v_mfma_f32_16x16x32_bf16 v[32:35], v[96:99], a[196:199], v[32:35]\n"
        "v_mfma_f32_16x16x32_bf16 v[36:39], v[96:99], a[200:203], v[36:39]\n"
        "v_mfma_f32_16x16x32_bf16 v[40:43], v[96:99], a[204:207], v[40:43]\n"
        "v_mfma_f32_16x16x32_bf16 v[44:47], v[96:99], a[208:211], v[44:47]\n"
        "v_mfma_f32_16x16x32_bf16 v[48:51], v[96:99], a[212:215], v[48:51]\n"
        "v_mfma_f32_16x16x32_bf16 v[52:55], v[96:99], a[216:219], v[52:55]\n"
        "v_mfma_f32_16x16x32_bf16 v[56:59], v[96:99], a[220:223], v[56:59]\n"
        "v_mfma_f32_16x16x32_bf16 v[60:63], v[100:103], a[196:199], v[60:63]\n"
        "v_mfma_f32_16x16x32_bf16 v[64:67], v[100:103], a[200:203], v[64:67]\n"
        "v_mfma_f32_16x16x32_bf16 v[68:71], v[100:103], a[204:207], v[68:71]\n"
        "v_mfma_f32_16x16x32_bf16 v[72:75], v[100:103], a[208:211], v[72:75]\n"
        "v_mfma_f32_16x16x32_bf16 v[76:79], v[100:103], a[212:215], v[76:79]\n"
        "v_mfma_f32_16x16x32_bf16 v[80:83], v[100:103], a[216:219], v[80:83]\n"
        "v_mfma_f32_16x16x32_bf16 v[84:87], v[100:103], a[220:223], v[84:87]\n"
        "ds_read_b128 v[96:99], v21 offset:9216\n"
        "ds_read_b128 v[100:103], v21 offset:23552\n"
        "s_waitcnt vmcnt(42) lgkmcnt(2)\n"
        "v_mfma_f32_16x16x32_bf16 v[32:35], v[88:91], a[224:227], v[32:35]\n"
        "v_mfma_f32_16x16x32_bf16 v[36:39], v[88:91], a[228:231], v[36:39]\n"
        "v_mfma_f32_16x16x32_bf16 v[40:43], v[88:91], a[232:235], v[40:43]\n"
        "v_mfma_f32_16x16x32_bf16 v[44:47], v[88:91], a[236:239], v[44:47]\n"
        "v_mfma_f32_16x16x32_bf16 v[48:51], v[88:91], a[240:243], v[48:51]\n"
        "v_mfma_f32_16x16x32_bf16 v[52:55], v[88:91], a[244:247], v[52:55]\n"
        "v_mfma_f32_16x16x32_bf16 v[56:59], v[88:91], a[248:251], v[56:59]\n"
        "v_mfma_f32_16x16x32_bf16 v[60:63], v[92:95], a[224:227], v[60:63]\n"
        "v_mfma_f32_16x16x32_bf16 v[64:67], v[92:95], a[228:231], v[64:67]\n"
        "v_mfma_f32_16x16x32_bf16 v[68:71], v[92:95], a[232:235], v[68:71]\n"
        "v_mfma_f32_16x16x32_bf16 v[72:75], v[92:95], a[236:239], v[72:75]\n"
        "v_mfma_f32_16x16x32_bf16 v[76:79], v[92:95], a[240:243], v[76:79]\n"
        "v_mfma_f32_16x16x32_bf16 v[80:83], v[92:95], a[244:247], v[80:83]\n"
        "v_mfma_f32_16x16x32_bf16 v[84:87], v[92:95], a[248:251], v[84:87]\n"
        "ds_read_b128 v[88:91], v21 offset:10240\n"
        "ds_read_b128 v[92:95], v21 offset:24576\n"
        "s_waitcnt vmcnt(35) lgkmcnt(2)\n"
        "v_mfma_f32_16x16x32_bf16 v[32:35], v[96:99], a[252:255], v[32:35]\n"
        "v_mfma_f32_16x16x32_bf16 v[36:39], v[96:99], v[120:123], v[36:39]\n"
        "v_mfma_f32_16x16x32_bf16 v[40:43], v[96:99], v[124:127], v[40:43]\n"
        "v_mfma_f32_16x16x32_bf16 v[44:47], v[96:99], v[128:131], v[44:47]\n"
        "v_mfma_f32_16x16x32_bf16 v[48:51], v[96:99], v[132:135], v[48:51]\n"
        "v_mfma_f32_16x16x32_bf16 v[52:55], v[96:99], v[136:139], v[52:55]\n"
        "v_mfma_f32_16x16x32_bf16 v[56:59], v[96:99], v[140:143], v[56:59]\n"
        "v_mfma_f32_16x16x32_bf16 v[60:63], v[100:103], a[252:255], v[60:63]\n"
        "v_mfma_f32_16x16x32_bf16 v[64:67], v[100:103], v[120:123], v[64:67]\n"
        "v_mfma_f32_16x16x32_bf16 v[68:71], v[100:103], v[124:127], v[68:71]\n"
        "v_mfma_f32_16x16x32_bf16 v[72:75], v[100:103], v[128:131], v[72:75]\n"
        "v_mfma_f32_16x16x32_bf16 v[76:79], v[100:103], v[132:135], v[76:79]\n"
        "v_mfma_f32_16x16x32_bf16 v[80:83], v[100:103], v[136:139], v[80:83]\n"
        "v_mfma_f32_16x16x32_bf16 v[84:87], v[100:103], v[140:143], v[84:87]\n"
        "ds_read_b128 v[96:99], v21 offset:11264\n"
        "ds_read_b128 v[100:103], v21 offset:25600\n"
        "s_waitcnt vmcnt(28) lgkmcnt(2)\n"
        "v_mfma_f32_16x16x32_bf16 v[32:35], v[88:91], v[144:147], v[32:35]\n"
        "v_mfma_f32_16x16x32_bf16 v[36:39], v[88:91], v[148:151], v[36:39]\n"
        "v_mfma_f32_16x16x32_bf16 v[40:43], v[88:91], v[152:155], v[40:43]\n"
        "v_mfma_f32_16x16x32_bf16 v[44:47], v[88:91], v[156:159], v[44:47]\n"
        "v_mfma_f32_16x16x32_bf16 v[48:51], v[88:91], v[160:163], v[48:51]\n"
        "v_mfma_f32_16x16x32_bf16 v[52:55], v[88:91], v[164:167], v[52:55]\n"
        "v_mfma_f32_16x16x32_bf16 v[56:59], v[88:91], v[168:171], v[56:59]\n"
        "v_mfma_f32_16x16x32_bf16 v[60:63], v[92:95], v[144:147], v[60:63]\n"
        "v_mfma_f32_16x16x32_bf16 v[64:67], v[92:95], v[148:151], v[64:67]\n"
        "v_mfma_f32_16x16x32_bf16 v[68:71], v[92:95], v[152:155], v[68:71]\n"
        "v_mfma_f32_16x16x32_bf16 v[72:75], v[92:95], v[156:159], v[72:75]\n"
        "v_mfma_f32_16x16x32_bf16 v[76:79], v[92:95], v[160:163], v[76:79]\n"
        "v_mfma_f32_16x16x32_bf16 v[80:83], v[92:95], v[164:167], v[80:83]\n"
        "v_mfma_f32_16x16x32_bf16 v[84:87], v[92:95], v[168:171], v[84:87]\n"
        "ds_read_b128 v[88:91], v21 offset:12288\n"
        "ds_read_b128 v[92:95], v21 offset:26624\n"
        "s_waitcnt vmcnt(21) lgkmcnt(2)\n"
        "v_mfma_f32_16x16x32_bf16 v[32:35], v[96:99], v[172:175], v[32:35]\n"
        "v_mfma_f32_16x16x32_bf16 v[36:39], v[96:99], v[176:179], v[36:39]\n"
        "v_mfma_f32_16x16x32_bf16 v[40:43], v[96:99], v[180:183], v[40:43]\n"
        "v_mfma_f32_16x16x32_bf16 v[44:47], v[96:99], v[184:187], v[44:47]\n"
        "v_mfma_f32_16x16x32_bf16 v[48:51], v[96:99], v[188:191], v[48:51]\n"
        "v_mfma_f32_16x16x32_bf16 v[52:55], v[96:99], v[192:195], v[52:55]\n"
        "v_mfma_f32_16x16x32_bf16 v[56:59], v[96:99], v[196:199], v[56:59]\n"
        "v_mfma_f32_16x16x32_bf16 v[60:63], v[100:103], v[172:175], v[60:63]\n"
        "v_mfma_f32_16x16x32_bf16 v[64:67], v[100:103], v[176:179], v[64:67]\n"
        "v_mfma_f32_16x16x32_bf16 v[68:71], v[100:103], v[180:183], v[68:71]\n"
        "v_mfma_f32_16x16x32_bf16 v[72:75], v[100:103], v[184:187], v[72:75]\n"
        "v_mfma_f32_16x16x32_bf16 v[76:79], v[100:103], v[188:191], v[76:79]\n"
        "v_mfma_f32_16x16x32_bf16 v[80:83], v[100:103], v[192:195], v[80:83]\n"
        "v_mfma_f32_16x16x32_bf16 v[84:87], v[100:103], v[196:199], v[84:87]\n"
        "ds_read_b128 v[96:99], v21 offset:13312\n"
        "ds_read_b128 v[100:103], v21 offset:27648\n"
        "s_waitcnt vmcnt(14) lgkmcnt(2)\n"
        "v_mfma_f32_16x16x32_bf16 v[32:35], v[88:91], v[200:203], v[32:35]\n"
        "v_mfma_f32_16x16x32_bf16 v[36:39], v[88:91], v[204:207], v[36:39]\n"
        "v_mfma_f32_16x16x32_bf16 v[40:43], v[88:91], v[208:211], v[40:43]\n"
        "v_mfma_f32_16x16x32_bf16 v[44:47], v[88:91], v[212:215], v[44:47]\n"
        "v_mfma_f32_16x16x32_bf16 v[48:51], v[88:91], v[216:219], v[48:51]\n"
        "v_mfma_f32_16x16x32_bf16 v[52:55], v[88:91], v[220:223], v[52:55]\n"
        "v_mfma_f32_16x16x32_bf16 v[56:59], v[88:91], v[224:227], v[56:59]\n"
        "v_mfma_f32_16x16x32_bf16 v[60:63], v[92:95], v[200:203], v[60:63]\n"
        "v_mfma_f32_16x16x32_bf16 v[64:67], v[92:95], v[204:207], v[64:67]\n"
        "v_mfma_f32_16x16x32_bf16 v[68:71], v[92:95], v[208:211], v[68:71]\n"
        "v_mfma_f32_16x16x32_bf16 v[72:75], v[92:95], v[212:215], v[72:75]\n"
        "v_mfma_f32_16x16x32_bf16 v[76:79], v[92:95], v[216:219], v[76:79]\n"
        "v_mfma_f32_16x16x32_bf16 v[80:83], v[92:95], v[220:223], v[80:83]\n"
        "v_mfma_f32_16x16x32_bf16 v[84:87], v[92:95], v[224:227], v[84:87]\n"
        "s_waitcnt vmcnt(7) lgkmcnt(0)\n"
        "v_mfma_f32_16x16x32_bf16 v[32:35], v[96:99], v[228:231], v[32:35]\n"
        "v_mfma_f32_16x16x32_bf16 v[36:39], v[96:99], v[232:235], v[36:39]\n"
        "v_mfma_f32_16x16x32_bf16 v[40:43], v[96:99], v[236:239], v[40:43]\n"
        "v_mfma_f32_16x16x32_bf16 v[44:47], v[96:99], v[240:243], v[44:47]\n"
        "v_mfma_f32_16x16x32_bf16 v[48:51], v[96:99], v[244:247], v[48:51]\n"
        "v_mfma_f32_16x16x32_bf16 v[52:55], v[96:99], v[248:251], v[52:55]\n"
        "v_mfma_f32_16x16x32_bf16 v[56:59], v[96:99], v[252:255], v[56:59]\n"
        "v_mfma_f32_16x16x32_bf16 v[60:63], v[100:103], v[228:231], v[60:63]\n"
        "v_mfma_f32_16x16x32_bf16 v[64:67], v[100:103], v[232:235], v[64:67]\n"
        "v_mfma_f32_16x16x32_bf16 v[68:71], v[100:103], v[236:239], v[68:71]\n"
        "v_mfma_f32_16x16x32_bf16 v[72:75], v[100:103], v[240:243], v[72:75]\n"
        "v_mfma_f32_16x16x32_bf16 v[76:79], v[100:103], v[244:247], v[76:79]\n"
        "v_mfma_f32_16x16x32_bf16 v[80:83], v[100:103], v[248:251], v[80:83]\n"
        "v_mfma_f32_16x16x32_bf16 v[84:87], v[100:103], v[252:255], v[84:87]\n"
        "v_cvt_pk_bf16_f32 v32, v32, v33\n"
        "v_cvt_pk_bf16_f32 v33, v34, v35\n"
        "v_cvt_pk_bf16_f32 v34, v60, v61\n"
        "v_cvt_pk_bf16_f32 v35, v62, v63\n"
        "buffer_store_dwordx4 v[32:35], v16, s[40:43], s50 offen offset:0 sc1\n"
        "v_cvt_pk_bf16_f32 v36, v36, v37\n"
        "v_cvt_pk_bf16_f32 v37, v38, v39\n"
        "v_cvt_pk_bf16_f32 v38, v64, v65\n"
        "v_cvt_pk_bf16_f32 v39, v66, v67\n"
        "buffer_store_dwordx4 v[36:39], v16, s[40:43], s50 offen offset:1024 sc1\n"
        "v_cvt_pk_bf16_f32 v40, v40, v41\n"
        "v_cvt_pk_bf16_f32 v41, v42, v43\n"
        "v_cvt_pk_bf16_f32 v42, v68, v69\n"
        "v_cvt_pk_bf16_f32 v43, v70, v71\n"
        "buffer_store_dwordx4 v[40:43], v16, s[40:43], s50 offen offset:2048 sc1\n"
        "v_cvt_pk_bf16_f32 v44, v44, v45\n"
        "v_cvt_pk_bf16_f32 v45, v46, v47\n"
        "v_cvt_pk_bf16_f32 v46, v72, v73\n"
        "v_cvt_pk_bf16_f32 v47, v74, v75\n"
        "buffer_store_dwordx4 v[44:47], v16, s[40:43], s50 offen offset:3072 sc1\n"
        "v_cvt_pk_bf16_f32 v48, v48, v49\n"
        "v_cvt_pk_bf16_f32 v49, v50, v51\n"
        "v_cvt_pk_bf16_f32 v50, v76, v77\n"
        "v_cvt_pk_bf16_f32 v51, v78, v79\n"
        "buffer_store_dwordx4 v[48:51], v16, s[40:43], s52 offen offset:0 sc1\n"
        "v_cvt_pk_bf16_f32 v52, v52, v53\n"
        "v_cvt_pk_bf16_f32 v53, v54, v55\n"
        "v_cvt_pk_bf16_f32 v54, v80, v81\n"
        "v_cvt_pk_bf16_f32 v55, v82, v83\n"
        "buffer_store_dwordx4 v[52:55], v16, s[40:43], s52 offen offset:1024 sc1\n"
        "v_cvt_pk_bf16_f32 v56, v56, v57\n"
        "v_cvt_pk_bf16_f32 v57, v58, v59\n"
        "v_cvt_pk_bf16_f32 v58, v84, v85\n"
        "v_cvt_pk_bf16_f32 v59, v86, v87\n"
        "buffer_store_dwordx4 v[56:59], v16, s[40:43], s51 offen offset:0 sc1\n"
        "v_xor_b32 v21, 0x7000, v21\n"
        "v_mov_b32 v104, 1\n"
        "s_mov_b32 s44, s47\n"
        "s_waitcnt vmcnt(0)\n"
        "s_barrier\n"
        "buffer_store_dword v104, v18, s[40:43], s54 offen sc1\n"
        "s_cmp_lt_u32 s44, 64\n"
        "s_cbranch_scc1 .Lw1_loop_%=\n"
        "s_waitcnt vmcnt(0)\n"
            :
            : "s"(lo), "s"(hi), "s"(WS_BYTES), "s"(rep), "s"(nrep), "s"(0), "s"(wv), "s"(OFF_WC + (224 + 14 * set + pb) * 28672u),
              "s"(OFF_WC + (224 + 14 * set + ph) * 28672u + half * 256), "s"(OFF_VOT + (28 * set + 2 * pb) * 1024u), "s"(OFF_VOT + (28 * set + 2 * ph + half) * 1024u),
              "s"(0), "s"(0), "s"(0),
              "v"(vL), "v"(g * 512 + n * 16), "v"(vF), "v"(vL), "v"(vL)
            : "memory", "vcc", "scc", "s40", "s41", "s42", "s43", "s44", "s45", "s46", "s47", "s48", "s49", "s50", "s51", "s52", "s53", "s54", "s55", "s56", "s57", "s58", "s59", "s60", "s61", "s62", "s63", "s64", "s65", "s66", "s67", "s68", "s69", "s70", "s71", "s72", "s73", "s74", "s75", "s76", "s77", "s78", "s79", "v16", "v17", "v18", "v19", "v20", "v21", "v22", "v23", "v24", "v25", "v26", "v27", "v28", "v29", "v30", "v31", "v32", "v33", "v34", "v35", "v36", "v37", "v38", "v39", "v40", "v41", "v42", "v43", "v44", "v45", "v46", "v47", "v48", "v49", "v50", "v51", "v52", "v53", "v54", "v55", "v56", "v57", "v58", "v59", "v60", "v61", "v62", "v63", "v64", "v65", "v66", "v67", "v68", "v69", "v70", "v71", "v72", "v73", "v74", "v75", "v76", "v77", "v78", "v79", "v80", "v81", "v82", "v83", "v84", "v85", "v86", "v87", "v88", "v89", "v90", "v91", "v92", "v93", "v94", "v95", "v96", "v97", "v98", "v99", "v100", "v101", "v102", "v103", "v104", "v105", "v106", "v107", "v108", "v109", "v110", "v111", "v112", "v113", "v114", "v115", "v116", "v117", "v118", "v119", "v120", "v121", "v122", "v123", "v124", "v125", "v126", "v127", "v128", "v129", "v130", "v131", "v132", "v133", "v134", "v135", "v136", "v137", "v138", "v139", "v140", "v141", "v142", "v143", "v144", "v145", "v146", "v147", "v148", "v149", "v150", "v151", "v152", "v153", "v154", "v155", "v156", "v157", "v158", "v159", "v160", "v161", "v162", "v163", "v164", "v165", "v166", "v167", "v168", "v169", "v170", "v171", "v172", "v173", "v174", "v175", "v176", "v177", "v178", "v179", "v180", "v181", "v182", "v183", "v184", "v185", "v186", "v187", "v188", "v189", "v190", "v191", "v192", "v193", "v194", "v195", "v196", "v197", "v198", "v199", "v200", "v201", "v202", "v203", "v204", "v205", "v206", "v207", "v208", "v209", "v210", "v211", "v212", "v213", "v214", "v215", "v216", "v217", "v218", "v219", "v220", "v221", "v222", "v223", "v224", "v225", "v226", "v227", "v228", "v229", "v230", "v231", "v232", "v233", "v234", "v235", "v236", "v237", "v238", "v239", "v240", "v241", "v242", "v243", "v244", "v245", "v246", "v247", "v248", "v249", "v250", "v251", "v252", "v253", "v254", "v255", "a0", "a1", "a2", "a3", "a4", "a5", "a6", "a7", "a8", "a9", "a10", "a11", "a12", "a13", "a14", "a15", "a16", "a17", "a18", "a19", "a20", "a21", "a22", "a23", "a24", "a25", "a26", "a27", "a28", "a29", "a30", "a31", "a32", "a33", "a34", "a35", "a36", "a37", "a38", "a39", "a40", "a41", "a42", "a43", "a44", "a45", "a46", "a47", "a48", "a49", "a50", "a51", "a52", "a53", "a54", "a55", "a56", "a57", "a58", "a59", "a60", "a61", "a62", "a63", "a64", "a65", "a66", "a67", "a68", "a69", "a70", "a71", "a72", "a73", "a74", "a75", "a76", "a77", "a78", "a79", "a80", "a81", "a82", "a83", "a84", "a85", "a86", "a87", "a88", "a89", "a90", "a91", "a92", "a93", "a94", "a95", "a96", "a97", "a98", "a99", "a100", "a101", "a102", "a103", "a104", "a105", "a106", "a107", "a108", "a109", "a110", "a111", "a112", "a113", "a114", "a115", "a116", "a117", "a118", "a119", "a120", "a121", "a122", "a123", "a124", "a125", "a126", "a127", "a128", "a129", "a130", "a131", "a132", "a133", "a134", "a135", "a136", "a137", "a138", "a139", "a140", "a141", "a142", "a143", "a144", "a145", "a146", "a147", "a148", "a149", "a150", "a151", "a152", "a153", "a154", "a155", "a156", "a157", "a158", "a159", "a160", "a161", "a162", "a163", "a164", "a165", "a166", "a167", "a168", "a169", "a170", "a171", "a172", "a173", "a174", "a175", "a176", "a177", "a178", "a179", "a180", "a181", "a182", "a183", "a184", "a185", "a186", "a187", "a188", "a189", "a190", "a191", "a192", "a193", "a194", "a195", "a196", "a197", "a198", "a199", "a200", "a201", "a202", "a203", "a204", "a205", "a206", "a207", "a208", "a209", "a210", "a211", "a212", "a213", "a214", "a215", "a216", "a217", "a218", "a219", "a220", "a221", "a222", "a223", "a224", "a225", "a226", "a227", "a228", "a229", "a230", "a231", "a232", "a233", "a234", "a235", "a236", "a237", "a238", "a239", "a240", "a241", "a242", "a243", "a244", "a245", "a246", "a247", "a248", "a249", "a250", "a251", "a252", "a253", "a254", "a255");
    }
    if (ROLE == 2) {
        asm volatile(
        "s_mov_b32 s40, %0\n"
        "s_and_b32 s41, %1, 0xffff\n"
        "s_mov_b32 s42, %2\n"
        "s_mov_b32 s43, 0x00020000\n"
        "s_mov_b32 s44, %3\n"
        "s_mul_i32 s45, %6, 7168\n"
        "s_add_u32 s46, s45, 97779712\n"
        "s_mov_b32 s74, 0xbfb8aa3b\n"
        "s_mov_b32 s75, 0xbfb8aa3b\n"
        "v_mov_b32 v16, %14\n"
        "v_mov_b32 v17, %15\n"
        "v_mov_b32 v18, %16\n"
        "v_mov_b32 v19, %17\n"
        "v_mov_b32 v20, %18\n"
        "v_mov_b32 v21, v16\n"
        "s_mov_b32 s60, %7\n"
        "s_and_b32 s61, %8, 0xffff\n"
        "s_mov_b32 s62, 1605632\n"
        "s_mov_b32 s63, 0x00020000\n"
        "s_mov_b32 s64, %11\n"
        "s_and_b32 s65, %12, 0xffff\n"
        "s_mov_b32 s66, 1605632\n"
        "s_mov_b32 s67, 0x00020000\n"
        "v_lshrrev_b32 v104, 4, v16\n"
        "v_add_u32 v105, 0, v104\n"
        "v_mul_u32_u24 v106, 9363, v105\n"
        "v_lshrrev_b32 v106, 16, v106\n"
        "v_mul_u32_u24 v106, 3472, v106\n"
        "v_lshl_add_u32 v32, v105, 4, v106\n"
        "v_add_u32 v105, 64, v104\n"
        "v_mul_u32_u24 v106, 9363, v105\n"
        "v_lshrrev_b32 v106, 16, v106\n"
        "v_mul_u32_u24 v106, 3472, v106\n"
        "v_lshl_add_u32 v33, v105, 4, v106\n"
        "v_add_u32 v105, 128, v104\n"
        "v_mul_u32_u24 v106, 9363, v105\n"
        "v_lshrrev_b32 v106, 16, v106\n"
        "v_mul_u32_u24 v106, 3472, v106\n"
        "v_lshl_add_u32 v34, v105, 4, v106\n"
        "v_add_u32 v105, 192, v104\n"
        "v_mul_u32_u24 v106, 9363, v105\n"
        "v_lshrrev_b32 v106, 16, v106\n"
        "v_mul_u32_u24 v106, 3472, v106\n"
        "v_lshl_add_u32 v35, v105, 4, v106\n"
        "v_add_u32 v105, 256, v104\n"
        "v_mul_u32_u24 v106, 9363, v105\n"
        "v_lshrrev_b32 v106, 16, v106\n"
        "v_mul_u32_u24 v106, 3472, v106\n"
        "v_lshl_add_u32 v36, v105, 4, v106\n"
        "v_add_u32 v105, 320, v104\n"
        "v_mul_u32_u24 v106, 9363, v105\n"
        "v_lshrrev_b32 v106, 16, v106\n"
        "v_mul_u32_u24 v106, 3472, v106\n"
        "v_lshl_add_u32 v37, v105, 4, v106\n"
        "v_add_u32 v105, 384, v104\n"
        "v_mul_u32_u24 v106, 9363, v105\n"
        "v_lshrrev_b32 v106, 16, v106\n"
        "v_mul_u32_u24 v106, 3472, v106\n"
        "v_lshl_add_u32 v38, v105, 4, v106\n"
        "s_mul_i32 s70, %6, 28672\n"
        "s_mov_b32 s71, %13\n"
        "s_add_u32 s72, s71, 0\n"
        "s_add_u32 m0, s70, 0\n"
        "s_nop 0\n"
        "buffer_load_dwordx4 v32, s[60:63], s71 offen nt lds\n"
        "s_add_u32 m0, s70, 1024\n"
        "s_nop 0\n"
        "buffer_load_dwordx4 v33, s[60:63], s71 offen nt lds\n"
        "s_add_u32 m0, s70, 2048\n"
        "s_nop 0\n"
        "buffer_load_dwordx4 v34, s[60:63], s71 offen nt lds\n"
        "s_add_u32 m0, s70, 3072\n"
        "s_nop 0\n"
        "buffer_load_dwordx4 v35, s[60:63], s71 offen nt lds\n"
        "s_add_u32 m0, s70, 4096\n"
        "s_nop 0\n"
        "buffer_load_dwordx4 v36, s[60:63], s71 offen nt lds\n"
        "s_add_u32 m0, s70, 5120\n"
        "s_nop 0\n"
        "buffer_load_dwordx4 v37, s[60:63], s71 offen nt lds\n"
        "s_add_u32 m0, s70, 6144\n"
        "s_nop 0\n"
        "buffer_load_dwordx4 v38, s[60:63], s71 offen nt lds\n"
        "s_add_u32 m0, s70, 7168\n"
        "s_nop 0\n"
        "buffer_load_dwordx4 v32, s[64:67], s72 offen nt lds\n"
        "s_add_u32 m0, s70, 8192\n"
        "s_nop 0\n"
        "buffer_load_dwordx4 v33, s[64:67], s72 offen nt lds\n"
        "s_add_u32 m0, s70, 9216\n"
        "s_nop 0\n"
        "buffer_load_dwordx4 v34, s[64:67], s72 offen nt lds\n"
        "s_add_u32 m0, s70, 10240\n"
        "s_nop 0\n"
        "buffer_load_dwordx4 v35, s[64:67], s72 offen nt lds\n"
        "s_add_u32 m0, s70, 11264\n"
        "s_nop 0\n"
        "buffer_load_dwordx4 v36, s[64:67], s72 offen nt lds\n"
        "s_add_u32 m0, s70, 12288\n"
        "s_nop 0\n"
        "buffer_load_dwordx4 v37, s[64:67], s72 offen nt lds\n"
        "s_add_u32 m0, s70, 13312\n"
        "s_nop 0\n"
        "buffer_load_dwordx4 v38, s[64:67], s72 offen nt lds\n"
        "s_add_u32 s71, s71, 229376\n"
        "s_add_u32 s72, s71, 0\n"
        "s_add_u32 m0, s70, 14336\n"
        "s_nop 0\n"
        "buffer_load_dwordx4 v32, s[60:63], s71 offen nt lds\n"
        "s_add_u32 m0, s70, 15360\n"
        "s_nop 0\n"
        "buffer_load_dwordx4 v33, s[60:63], s71 offen nt lds\n"
        "s_add_u32 m0, s70, 16384\n"
        "s_nop 0\n"
        "buffer_load_dwordx4 v34, s[60:63], s71 offen nt lds\n"
        "s_add_u32 m0, s70, 17408\n"
        "s_nop 0\n"
        "buffer_load_dwordx4 v35, s[60:63], s71 offen nt lds\n"
        "s_add_u32 m0, s70, 18432\n"
        "s_nop 0\n"
        "buffer_load_dwordx4 v36, s[60:63], s71 offen nt lds\n"
        "s_add_u32 m0, s70, 19456\n"
        "s_nop 0\n"
        "buffer_load_dwordx4 v37, s[60:63], s71 offen nt lds\n"
        "s_add_u32 m0, s70, 20480\n"
        "s_nop 0\n"
        "buffer_load_dwordx4 v38, s[60:63], s71 offen nt lds\n"
        "s_add_u32 m0, s70, 21504\n"
        "s_nop 0\n"
        "buffer_load_dwordx4 v32, s[64:67], s72 offen nt lds\n"
        "s_add_u32 m0, s70, 22528\n"
        "s_nop 0\n"
        "buffer_load_dwordx4 v33, s[64:67], s72 offen nt lds\n"
        "s_add_u32 m0, s70, 23552\n"
        "s_nop 0\n"
        "buffer_load_dwordx4 v34, s[64:67], s72 offen nt lds\n"
        "s_add_u32 m0, s70, 24576\n"
        "s_nop 0\n"
        "buffer_load_dwordx4 v35, s[64:67], s72 offen nt lds\n"
        "s_add_u32 m0, s70, 25600\n"
        "s_nop 0\n"
        "buffer_load_dwordx4 v36, s[64:67], s72 offen nt lds\n"
        "s_add_u32 m0, s70, 26624\n"
        "s_nop 0\n"
        "buffer_load_dwordx4 v37, s[64:67], s72 offen nt lds\n"
        "s_add_u32 m0, s70, 27648\n"
        "s_nop 0\n"
        "buffer_load_dwordx4 v38, s[64:67], s72 offen nt lds\n"
        "s_add_u32 s71, s71, 229376\n"
        "s_waitcnt vmcnt(14)\n"
        "ds_read_b64_tr_b16 a[0:1], v17 offset:0\n"
        "ds_read_b64_tr_b16 a[2:3], v17 offset:448\n"
        "ds_read_b64_tr_b16 a[4:5], v17 offset:16\n"
        "ds_read_b64_tr_b16 a[6:7], v17 offset:464\n"
        "ds_read_b64_tr_b16 a[8:9], v17 offset:32\n"
        "ds_read_b64_tr_b16 a[10:11], v17 offset:480\n"
        "ds_read_b64_tr_b16 a[12:13], v17 offset:48\n"
        "ds_read_b64_tr_b16 a[14:15], v17 offset:496\n"
        "ds_read_b64_tr_b16 a[16:17], v17 offset:64\n"
        "ds_read_b64_tr_b16 a[18:19], v17 offset:512\n"
        "ds_read_b64_tr_b16 a[20:21], v17 offset:80\n"
        "ds_read_b64_tr_b16 a[22:23], v17 offset:528\n"
        "ds_read_b64_tr_b16 a[24:25], v17 offset:96\n"
        "ds_read_b64_tr_b16 a[26:27], v17 offset:544\n"
        "ds_read_b64_tr_b16 a[28:29], v17 offset:3584\n"
        "ds_read_b64_tr_b16 a[30:31], v17 offset:4032\n"
        "ds_read_b64_tr_b16 a[32:33], v17 offset:3600\n"
        "ds_read_b64_tr_b16 a[34:35], v17 offset:4048\n"
        "ds_read_b64_tr_b16 a[36:37], v17 offset:3616\n"
        "ds_read_b64_tr_b16 a[38:39], v17 offset:4064\n"
        "ds_read_b64_tr_b16 a[40:41], v17 offset:3632\n"
        "ds_read_b64_tr_b16 a[42:43], v17 offset:4080\n"
        "ds_read_b64_tr_b16 a[44:45], v17 offset:3648\n"
        "ds_read_b64_tr_b16 a[46:47], v17 offset:4096\n"
        "ds_read_b64_tr_b16 a[48:49], v17 offset:3664\n"
        "ds_read_b64_tr_b16 a[50:51], v17 offset:4112\n"
        "ds_read_b64_tr_b16 a[52:53], v17 offset:3680\n"
        "ds_read_b64_tr_b16 a[54:55], v17 offset:4128\n"
        "s_waitcnt lgkmcnt(0)\n"
        "s_add_u32 s72, s71, 0\n"
        "s_add_u32 m0, s70, 0\n"
        "s_nop 0\n"
        "buffer_load_dwordx4 v32, s[60:63], s71 offen nt lds\n"
        "s_add_u32 m0, s70, 1024\n"
        "s_nop 0\n"
        "buffer_load_dwordx4 v33, s[60:63], s71 offen nt lds\n"
        "s_add_u32 m0, s70, 2048\n"
        "s_nop 0\n"
        "buffer_load_dwordx4 v34, s[60:63], s71 offen nt lds\n"
        "s_add_u32 m0, s70, 3072\n"
        "s_nop 0\n"
        "buffer_load_dwordx4 v35, s[60:63], s71 offen nt lds\n"
        "s_add_u32 m0, s70, 4096\n"
        "s_nop 0\n"
        "buffer_load_dwordx4 v36, s[60:63], s71 offen nt lds\n"
        "s_add_u32 m0, s70, 5120\n"
        "s_nop 0\n"
        "buffer_load_dwordx4 v37, s[60:63], s71 offen nt lds\n"
        "s_add_u32 m0, s70, 6144\n"
        "s_nop 0\n"
        "buffer_load_dwordx4 v38, s[60:63], s71 offen nt lds\n"
        "s_add_u32 m0, s70, 7168\n"
        "s_nop 0\n"
        "buffer_load_dwordx4 v32, s[64:67], s72 offen nt lds\n"
        "s_add_u32 m0, s70, 8192\n"
        "s_nop 0\n"
        "buffer_load_dwordx4 v33, s[64:67], s72 offen nt lds\n"
        "s_add_u32 m0, s70, 9216\n"
        "s_nop 0\n"
        "buffer_load_dwordx4 v34, s[64:67], s72 offen nt lds\n"
        "s_add_u32 m0, s70, 10240\n"
        "s_nop 0\n"
        "buffer_load_dwordx4 v35, s[64:67], s72 offen nt lds\n"
        "s_add_u32 m0, s70, 11264\n"
        "s_nop 0\n"
        "buffer_load_dwordx4 v36, s[64:67], s72 offen nt lds\n"
        "s_add_u32 m0, s70, 12288\n"
        "s_nop 0\n"
        "buffer_load_dwordx4 v37, s[64:67], s72 offen nt lds\n"
        "s_add_u32 m0, s70, 13312\n"
        "s_nop 0\n"
        "buffer_load_dwordx4 v38, s[64:67], s72 offen nt lds\n"
        "s_add_u32 s71, s71, 229376\n"
        "s_waitcnt vmcnt(14)\n"
        "ds_read_b64_tr_b16 a[56:57], v17 offset:14336\n"
        "ds_read_b64_tr_b16 a[58:59], v17 offset:14784\n"
        "ds_read_b64_tr_b16 a[60:61], v17 offset:14352\n"
        "ds_read_b64_tr_b16 a[62:63], v17 offset:14800\n"
        "ds_read_b64_tr_b16 a[64:65], v17 offset:14368\n"
        "ds_read_b64_tr_b16 a[66:67], v17 offset:14816\n"
        "ds_read_b64_tr_b16 a[68:69], v17 offset:14384\n"
        "ds_read_b64_tr_b16 a[70:71], v17 offset:14832\n"
        "ds_read_b64_tr_b16 a[72:73], v17 offset:14400\n"
        "ds_read_b64_tr_b16 a[74:75], v17 offset:14848\n"
        "ds_read_b64_tr_b16 a[76:77], v17 offset:14416\n"
        "ds_read_b64_tr_b16 a[78:79], v17 offset:14864\n"
        "ds_read_b64_tr_b16 a[80:81], v17 offset:14432\n"
        "ds_read_b64_tr_b16 a[82:83], v17 offset:14880\n"
        "ds_read_b64_tr_b16 a[84:85], v17 offset:17920\n"
        "ds_read_b64_tr_b16 a[86:87], v17 offset:18368\n"
        "ds_read_b64_tr_b16 a[88:89], v17 offset:17936\n"
        "ds_read_b64_tr_b16 a[90:91], v17 offset:18384\n"
        "ds_read_b64_tr_b16 a[92:93], v17 offset:17952\n"
        "ds_read_b64_tr_b16 a[94:95], v17 offset:18400\n"
        "ds_read_b64_tr_b16 a[96:97], v17 offset:17968\n"
        "ds_read_b64_tr_b16 a[98:99], v17 offset:18416\n"
        "ds_read_b64_tr_b16 a[100:101], v17 offset:17984\n"
        "ds_read_b64_tr_b16 a[102:103], v17 offset:18432\n"
        "ds_read_b64_tr_b16 a[104:105], v17 offset:18000\n"
        "ds_read_b64_tr_b16 a[106:107], v17 offset:18448\n"
        "ds_read_b64_tr_b16 a[108:109], v17 offset:18016\n"
        "ds_read_b64_tr_b16 a[110:111], v17 offset:18464\n"
        "s_waitcnt lgkmcnt(0)\n"
        "s_add_u32 s72, s71, 0\n"
        "s_add_u32 m0, s70, 14336\n"
        "s_nop 0\n"
        "buffer_load_dwordx4 v32, s[60:63], s71 offen nt lds\n"
        "s_add_u32 m0, s70, 15360\n"
        "s_nop 0\n"
        "buffer_load_dwordx4 v33, s[60:63], s71 offen nt lds\n"
        "s_add_u32 m0, s70, 16384\n"
        "s_nop 0\n"
        "buffer_load_dwordx4 v34, s[60:63], s71 offen nt lds\n"
        "s_add_u32 m0, s70, 17408\n"
        "s_nop 0\n"
        "buffer_load_dwordx4 v35, s[60:63], s71 offen nt lds\n"
        "s_add_u32 m0, s70, 18432\n"
        "s_nop 0\n"
        "buffer_load_dwordx4 v36, s[60:63], s71 offen nt lds\n"
        "s_add_u32 m0, s70, 19456\n"
        "s_nop 0\n"
        "buffer_load_dwordx4 v37, s[60:63], s71 offen nt lds\n"
        "s_add_u32 m0, s70, 20480\n"
        "s_nop 0\n"
        "buffer_load_dwordx4 v38, s[60:63], s71 offen nt lds\n"
        "s_add_u32 m0, s70, 21504\n"
        "s_nop 0\n"
        "buffer_load_dwordx4 v32, s[64:67], s72 offen nt lds\n"
        "s_add_u32 m0, s70, 22528\n"
        "s_nop 0\n"
        "buffer_load_dwordx4 v33, s[64:67], s72 offen nt lds\n"
        "s_add_u32 m0, s70, 23552\n"
        "s_nop 0\n"
        "buffer_load_dwordx4 v34, s[64:67], s72 offen nt lds\n"
        "s_add_u32 m0, s70, 24576\n"
        "s_nop 0\n"
        "buffer_load_dwordx4 v35, s[64:67], s72 offen nt lds\n"
        "s_add_u32 m0, s70, 25600\n"
        "s_nop 0\n"
        "buffer_load_dwordx4 v36, s[64:67], s72 offen nt lds\n"
        "s_add_u32 m0, s70, 26624\n"
        "s_nop 0\n"
        "buffer_load_dwordx4 v37, s[64:67], s72 offen nt lds\n"
        "s_add_u32 m0, s70, 27648\n"
        "s_nop 0\n"
        "buffer_load_dwordx4 v38, s[64:67], s72 offen nt lds\n"
        "s_add_u32 s71, s71, 229376\n"
        "s_waitcnt vmcnt(14)\n"
        "ds_read_b64_tr_b16 a[112:113], v17 offset:0\n"
        "ds_read_b64_tr_b16 a[114:115], v17 offset:448\n"
        "ds_read_b64_tr_b16 a[116:117], v17 offset:16\n"
        "ds_read_b64_tr_b16 a[118:119], v17 offset:464\n"
        "ds_read_b64_tr_b16 a[120:121], v17 offset:32\n"
        "ds_read_b64_tr_b16 a[122:123], v17 offset:480\n"
        "ds_read_b64_tr_b16 a[124:125], v17 offset:48\n"
        "ds_read_b64_tr_b16 a[126:127], v17 offset:496\n"
        "ds_read_b64_tr_b16 a[128:129], v17 offset:64\n"
        "ds_read_b64_tr_b16 a[130:131], v17 offset:512\n"
        "ds_read_b64_tr_b16 a[132:133], v17 offset:80\n"
        "ds_read_b64_tr_b16 a[134:135], v17 offset:528\n"
        "ds_read_b64_tr_b16 a[136:137], v17 offset:96\n"
        "ds_read_b64_tr_b16 a[138:139], v17 offset:544\n"
        "ds_read_b64_tr_b16 a[140:141], v17 offset:3584\n"
        "ds_read_b64_tr_b16 a[142:143], v17 offset:4032\n"
        "ds_read_b64_tr_b16 a[144:145], v17 offset:3600\n"
        "ds_read_b64_tr_b16 a[146:147], v17 offset:4048\n"
        "ds_read_b64_tr_b16 a[148:149], v17 offset:3616\n"
        "ds_read_b64_tr_b16 a[150:151], v17 offset:4064\n"
        "ds_read_b64_tr_b16 a[152:153], v17 offset:3632\n"
        "ds_read_b64_tr_b16 a[154:155], v17 offset:4080\n"
        "ds_read_b64_tr_b16 a[156:157], v17 offset:3648\n"
        "ds_read_b64_tr_b16 a[158:159], v17 offset:4096\n"
        "ds_read_b64_tr_b16 a[160:161], v17 offset:3664\n"
        "ds_read_b64_tr_b16 a[162:163], v17 offset:4112\n"
        "ds_read_b64_tr_b16 a[164:165], v17 offset:3680\n"
        "ds_read_b64_tr_b16 a[166:167], v17 offset:4128\n"
        "s_waitcnt lgkmcnt(0)\n"
        "s_add_u32 s72, s71, 0\n"
        "s_add_u32 m0, s70, 0\n"
        "s_nop 0\n"
        "buffer_load_dwordx4 v32, s[60:63], s71 offen nt lds\n"
        "s_add_u32 m0, s70, 1024\n"
        "s_nop 0\n"
        "buffer_load_dwordx4 v33, s[60:63], s71 offen nt lds\n"
        "s_add_u32 m0, s70, 2048\n"
        "s_nop 0\n"
        "buffer_load_dwordx4 v34, s[60:63], s71 offen nt lds\n"
        "s_add_u32 m0, s70, 3072\n"
        "s_nop 0\n"
        "buffer_load_dwordx4 v35, s[60:63], s71 offen nt lds\n"
        "s_add_u32 m0, s70, 4096\n"
        "s_nop 0\n"
        "buffer_load_dwordx4 v36, s[60:63], s71 offen nt lds\n"
        "s_add_u32 m0, s70, 5120\n"
        "s_nop 0\n"
        "buffer_load_dwordx4 v37, s[60:63], s71 offen nt lds\n"
        "s_add_u32 m0, s70, 6144\n"
        "s_nop 0\n"
        "buffer_load_dwordx4 v38, s[60:63], s71 offen nt lds\n"
        "s_add_u32 m0, s70, 7168\n"
        "s_nop 0\n"
        "buffer_load_dwordx4 v32, s[64:67], s72 offen nt lds\n"
        "s_add_u32 m0, s70, 8192\n"
        "s_nop 0\n"
        "buffer_load_dwordx4 v33, s[64:67], s72 offen nt lds\n"
        "s_add_u32 m0, s70, 9216\n"
        "s_nop 0\n"
        "buffer_load_dwordx4 v34, s[64:67], s72 offen nt lds\n"
        "s_add_u32 m0, s70, 10240\n"
        "s_nop 0\n"
        "buffer_load_dwordx4 v35, s[64:67], s72 offen nt lds\n"
        "s_add_u32 m0, s70, 11264\n"
        "s_nop 0\n"
        "buffer_load_dwordx4 v36, s[64:67], s72 offen nt lds\n"
        "s_add_u32 m0, s70, 12288\n"
        "s_nop 0\n"
        "buffer_load_dwordx4 v37, s[64:67], s72 offen nt lds\n"
        "s_add_u32 m0, s70, 13312\n"
        "s_nop 0\n"
        "buffer_load_dwordx4 v38, s[64:67], s72 offen nt lds\n"
        "s_add_u32 s71, s71, 229376\n"
        "s_waitcnt vmcnt(14)\n"
        "ds_read_b64_tr_b16 a[168:169], v17 offset:14336\n"
        "ds_read_b64_tr_b16 a[170:171], v17 offset:14784\n"
        "ds_read_b64_tr_b16 a[172:173], v17 offset:14352\n"
        "ds_read_b64_tr_b16 a[174:175], v17 offset:14800\n"
        "ds_read_b64_tr_b16 a[176:177], v17 offset:14368\n"
        "ds_read_b64_tr_b16 a[178:179], v17 offset:14816\n"
        "ds_read_b64_tr_b16 a[180:181], v17 offset:14384\n"
        "ds_read_b64_tr_b16 a[182:183], v17 offset:14832\n"
        "ds_read_b64_tr_b16 a[184:185], v17 offset:14400\n"
        "ds_read_b64_tr_b16 a[186:187], v17 offset:14848\n"
        "ds_read_b64_tr_b16 a[188:189], v17 offset:14416\n"
        "ds_read_b64_tr_b16 a[190:191], v17 offset:14864\n"
        "ds_read_b64_tr_b16 a[192:193], v17 offset:14432\n"
        "ds_read_b64_tr_b16 a[194:195], v17 offset:14880\n"
        "ds_read_b64_tr_b16 a[196:197], v17 offset:17920\n"
        "ds_read_b64_tr_b16 a[198:199], v17 offset:18368\n"
        "ds_read_b64_tr_b16 a[200:201], v17 offset:17936\n"
        "ds_read_b64_tr_b16 a[202:203], v17 offset:18384\n"
        "ds_read_b64_tr_b16 a[204:205], v17 offset:17952\n"
        "ds_read_b64_tr_b16 a[206:207], v17 offset:18400\n"
        "ds_read_b64_tr_b16 a[208:209], v17 offset:17968\n"
        "ds_read_b64_tr_b16 a[210:211], v17 offset:18416\n"
        "ds_read_b64_tr_b16 a[212:213], v17 offset:17984\n"
        "ds_read_b64_tr_b16 a[214:215], v17 offset:18432\n"
        "ds_read_b64_tr_b16 a[216:217], v17 offset:18000\n"
        "ds_read_b64_tr_b16 a[218:219], v17 offset:18448\n"
        "ds_read_b64_tr_b16 a[220:221], v17 offset:18016\n"
        "ds_read_b64_tr_b16 a[222:223], v17 offset:18464\n"
        "s_waitcnt lgkmcnt(0)\n"
        "s_add_u32 s72, s71, 0\n"
        "s_add_u32 m0, s70, 14336\n"
        "s_nop 0\n"
        "buffer_load_dwordx4 v32, s[60:63], s71 offen nt lds\n"
        "s_add_u32 m0, s70, 15360\n"
        "s_nop 0\n"
        "buffer_load_dwordx4 v33, s[60:63], s71 offen nt lds\n"
        "s_add_u32 m0, s70, 16384\n"
        "s_nop 0\n"
        "buffer_load_dwordx4 v34, s[60:63], s71 offen nt lds\n"
        "s_add_u32 m0, s70, 17408\n"
        "s_nop 0\n"
        "buffer_load_dwordx4 v35, s[60:63], s71 offen nt lds\n"
        "s_add_u32 m0, s70, 18432\n"
        "s_nop 0\n"
        "buffer_load_dwordx4 v36, s[60:63], s71 offen nt lds\n"
        "s_add_u32 m0, s70, 19456\n"
        "s_nop 0\n"
        "buffer_load_dwordx4 v37, s[60:63], s71 offen nt lds\n"
        "s_add_u32 m0, s70, 20480\n"
        "s_nop 0\n"
        "buffer_load_dwordx4 v38, s[60:63], s71 offen nt lds\n"
        "s_add_u32 m0, s70, 21504\n"
        "s_nop 0\n"
        "buffer_load_dwordx4 v32, s[64:67], s72 offen nt lds\n"
        "s_add_u32 m0, s70, 22528\n"
        "s_nop 0\n"
        "buffer_load_dwordx4 v33, s[64:67], s72 offen nt lds\n"
        "s_add_u32 m0, s70, 23552\n"
        "s_nop 0\n"
        "buffer_load_dwordx4 v34, s[64:67], s72 offen nt lds\n"
        "s_add_u32 m0, s70, 24576\n"
        "s_nop 0\n"
        "buffer_load_dwordx4 v35, s[64:67], s72 offen nt lds\n"
        "s_add_u32 m0, s70, 25600\n"
        "s_nop 0\n"
        "buffer_load_dwordx4 v36, s[64:67], s72 offen nt lds\n"
        "s_add_u32 m0, s70, 26624\n"
        "s_nop 0\n"
        "buffer_load_dwordx4 v37, s[64:67], s72 offen nt lds\n"
        "s_add_u32 m0, s70, 27648\n"
        "s_nop 0\n"
        "buffer_load_dwordx4 v38, s[64:67], s72 offen nt lds\n"
        "s_add_u32 s71, s71, 229376\n"
        "s_waitcnt vmcnt(14)\n"
        "ds_read_b64_tr_b16 a[224:225], v17 offset:0\n"
        "ds_read_b64_tr_b16 a[226:227], v17 offset:448\n"
        "ds_read_b64_tr_b16 a[228:229], v17 offset:16\n"
        "ds_read_b64_tr_b16 a[230:231], v17 offset:464\n"
        "ds_read_b64_tr_b16 a[232:233], v17 offset:32\n"
        "ds_read_b64_tr_b16 a[234:235], v17 offset:480\n"
        "ds_read_b64_tr_b16 a[236:237], v17 offset:48\n"
        "ds_read_b64_tr_b16 a[238:239], v17 offset:496\n"
        "ds_read_b64_tr_b16 a[240:241], v17 offset:64\n"
        "ds_read_b64_tr_b16 a[242:243], v17 offset:512\n"
        "ds_read_b64_tr_b16 a[244:245], v17 offset:80\n"
        "ds_read_b64_tr_b16 a[246:247], v17 offset:528\n"
        "ds_read_b64_tr_b16 a[248:249], v17 offset:96\n"
        "ds_read_b64_tr_b16 a[250:251], v17 offset:544\n"
        "ds_read_b64_tr_b16 a[252:253], v17 offset:3584\n"
        "ds_read_b64_tr_b16 a[254:255], v17 offset:4032\n"
        "ds_read_b64_tr_b16 v[120:121], v17 offset:3600\n"
        "ds_read_b64_tr_b16 v[122:123], v17 offset:4048\n"
        "ds_read_b64_tr_b16 v[124:125], v17 offset:3616\n"
        "ds_read_b64_tr_b16 v[126:127], v17 offset:4064\n"
        "ds_read_b64_tr_b16 v[128:129], v17 offset:3632\n"
        "ds_read_b64_tr_b16 v[130:131], v17 offset:4080\n"
        "ds_read_b64_tr_b16 v[132:133], v17 offset:3648\n"
        "ds_read_b64_tr_b16 v[134:135], v17 offset:4096\n"
        "ds_read_b64_tr_b16 v[136:137], v17 offset:3664\n"
        "ds_read_b64_tr_b16 v[138:139], v17 offset:4112\n"
        "ds_read_b64_tr_b16 v[140:141], v17 offset:3680\n"
        "ds_read_b64_tr_b16 v[142:143], v17 offset:4128\n"
        "s_waitcnt lgkmcnt(0)\n"
        "s_add_u32 s72, s71, 0\n"
        "s_add_u32 m0, s70, 0\n"
        "s_nop 0\n"
        "buffer_load_dwordx4 v32, s[60:63], s71 offen nt lds\n"
        "s_add_u32 m0, s70, 1024\n"
        "s_nop 0\n"
        "buffer_load_dwordx4 v33, s[60:63], s71 offen nt lds\n"
        "s_add_u32 m0, s70, 2048\n"
        "s_nop 0\n"
        "buffer_load_dwordx4 v34, s[60:63], s71 offen nt lds\n"
        "s_add_u32 m0, s70, 3072\n"
        "s_nop 0\n"
        "buffer_load_dwordx4 v35, s[60:63], s71 offen nt lds\n"
        "s_add_u32 m0, s70, 4096\n"
        "s_nop 0\n"
        "buffer_load_dwordx4 v36, s[60:63], s71 offen nt lds\n"
        "s_add_u32 m0, s70, 5120\n"
        "s_nop 0\n"
        "buffer_load_dwordx4 v37, s[60:63], s71 offen nt lds\n"
        "s_add_u32 m0, s70, 6144\n"
        "s_nop 0\n"
        "buffer_load_dwordx4 v38, s[60:63], s71 offen nt lds\n"
        "s_add_u32 m0, s70, 7168\n"
        "s_nop 0\n"
        "buffer_load_dwordx4 v32, s[64:67], s72 offen nt lds\n"
        "s_add_u32 m0, s70, 8192\n"
        "s_nop 0\n"
        "buffer_load_dwordx4 v33, s[64:67], s72 offen nt lds\n"
        "s_add_u32 m0, s70, 9216\n"
        "s_nop 0\n"
        "buffer_load_dwordx4 v34, s[64:67], s72 offen nt lds\n"
        "s_add_u32 m0, s70, 10240\n"
        "s_nop 0\n"
        "buffer_load_dwordx4 v35, s[64:67], s72 offen nt lds\n"
        "s_add_u32 m0, s70, 11264\n"
        "s_nop 0\n"
        "buffer_load_dwordx4 v36, s[64:67], s72 offen nt lds\n"
        "s_add_u32 m0, s70, 12288\n"
        "s_nop 0\n"
        "buffer_load_dwordx4 v37, s[64:67], s72 offen nt lds\n"
        "s_add_u32 m0, s70, 13312\n"
        "s_nop 0\n"
        "buffer_load_dwordx4 v38, s[64:67], s72 offen nt lds\n"
        "s_add_u32 s71, s71, 229376\n"
        "s_waitcnt vmcnt(14)\n"
        "ds_read_b64_tr_b16 v[144:145], v17 offset:14336\n"
        "ds_read_b64_tr_b16 v[146:147], v17 offset:14784\n"
        "ds_read_b64_tr_b16 v[148:149], v17 offset:14352\n"
        "ds_read_b64_tr_b16 v[150:151], v17 offset:14800\n"
        "ds_read_b64_tr_b16 v[152:153], v17 offset:14368\n"
        "ds_read_b64_tr_b16 v[154:155], v17 offset:14816\n"
        "ds_read_b64_tr_b16 v[156:157], v17 offset:14384\n"
        "ds_read_b64_tr_b16 v[158:159], v17 offset:14832\n"
        "ds_read_b64_tr_b16 v[160:161], v17 offset:14400\n"
        "ds_read_b64_tr_b16 v[162:163], v17 offset:14848\n"
        "ds_read_b64_tr_b16 v[164:165], v17 offset:14416\n"
        "ds_read_b64_tr_b16 v[166:167], v17 offset:14864\n"
        "ds_read_b64_tr_b16 v[168:169], v17 offset:14432\n"
        "ds_read_b64_tr_b16 v[170:171], v17 offset:14880\n"
        "ds_read_b64_tr_b16 v[172:173], v17 offset:17920\n"
        "ds_read_b64_tr_b16 v[174:175], v17 offset:18368\n"
        "ds_read_b64_tr_b16 v[176:177], v17 offset:17936\n"
        "ds_read_b64_tr_b16 v[178:179], v17 offset:18384\n"
        "ds_read_b64_tr_b16 v[180:181], v17 offset:17952\n"
        "ds_read_b64_tr_b16 v[182:183], v17 offset:18400\n"
        "ds_read_b64_tr_b16 v[184:185], v17 offset:17968\n"
        "ds_read_b64_tr_b16 v[186:187], v17 offset:18416\n"
        "ds_read_b64_tr_b16 v[188:189], v17 offset:17984\n"
        "ds_read_b64_tr_b16 v[190:191], v17 offset:18432\n"
        "ds_read_b64_tr_b16 v[192:193], v17 offset:18000\n"
        "ds_read_b64_tr_b16 v[194:195], v17 offset:18448\n"
        "ds_read_b64_tr_b16 v[196:197], v17 offset:18016\n"
        "ds_read_b64_tr_b16 v[198:199], v17 offset:18464\n"
        "s_waitcnt lgkmcnt(0)\n"
        "s_waitcnt vmcnt(0)\n"
        "ds_read_b64_tr_b16 v[200:201], v17 offset:0\n"
        "ds_read_b64_tr_b16 v[202:203], v17 offset:448\n"
        "ds_read_b64_tr_b16 v[204:205], v17 offset:16\n"
        "ds_read_b64_tr_b16 v[206:207], v17 offset:464\n"
        "ds_read_b64_tr_b16 v[208:209], v17 offset:32\n"
        "ds_read_b64_tr_b16 v[210:211], v17 offset:480\n"
        "ds_read_b64_tr_b16 v[212:213], v17 offset:48\n"
        "ds_read_b64_tr_b16 v[214:215], v17 offset:496\n"
        "ds_read_b64_tr_b16 v[216:217], v17 offset:64\n"
        "ds_read_b64_tr_b16 v[218:219], v17 offset:512\n"
        "ds_read_b64_tr_b16 v[220:221], v17 offset:80\n"
        "ds_read_b64_tr_b16 v[222:223], v17 offset:528\n"
        "ds_read_b64_tr_b16 v[224:225], v17 offset:96\n"
        "ds_read_b64_tr_b16 v[226:227], v17 offset:544\n"
        "ds_read_b64_tr_b16 v[228:229], v17 offset:3584\n"
        "ds_read_b64_tr_b16 v[230:231], v17 offset:4032\n"
        "ds_read_b64_tr_b16 v[232:233], v17 offset:3600\n"
        "ds_read_b64_tr_b16 v[234:235], v17 offset:4048\n"
        "ds_read_b64_tr_b16 v[236:237], v17 offset:3616\n"
        "ds_read_b64_tr_b16 v[238:239], v17 offset:4064\n"
        "ds_read_b64_tr_b16 v[240:241], v17 offset:3632\n"
        "ds_read_b64_tr_b16 v[242:243], v17 offset:4080\n"
        "ds_read_b64_tr_b16 v[244:245], v17 offset:3648\n"
        "ds_read_b64_tr_b16 v[246:247], v17 offset:4096\n"
        "ds_read_b64_tr_b16 v[248:249], v17 offset:3664\n"
        "ds_read_b64_tr_b16 v[250:251], v17 offset:4112\n"
        "ds_read_b64_tr_b16 v[252:253], v17 offset:3680\n"
        "ds_read_b64_tr_b16 v[254:255], v17 offset:4128\n"
        "s_waitcnt lgkmcnt(0)\n"
        "s_barrier\n"
        "s_mul_i32 s57, s44, 28672\n"
        "s_add_u32 s57, s57, s46\n"
        "s_add_u32 s58, s57, 4096\n"
        "s_mov_b32 m0, s45\n"
        "s_nop 0\n"
        "buffer_load_dwordx4 v16, s[40:43], s57 offen offset:0 lds\n"
        "buffer_load_dwordx4 v16, s[40:43], s57 offen offset:1024 lds\n"
        "buffer_load_dwordx4 v16, s[40:43], s57 offen offset:2048 lds\n"
        "buffer_load_dwordx4 v16, s[40:43], s57 offen offset:3072 lds\n"
        "s_add_u32 m0, s45, 4096\n"
        "s_nop 0\n"
        "buffer_load_dwordx4 v16, s[40:43], s58 offen offset:0 lds\n"
        "buffer_load_dwordx4 v16, s[40:43], s58 offen offset:1024 lds\n"
        "buffer_load_dwordx4 v16, s[40:43], s58 offen offset:2048 lds\n"
        "s_waitcnt vmcnt(0)\n"
        "s_mov_b32 s59, 28672\n"
        "s_barrier\n"
        ".Lw2_loop_%=:\n"
        "ds_read_b128 v[88:91], v21 offset:0\n"
        "ds_read_b128 v[92:95], v21 offset:14336\n"
        "s_add_u32 s45, s45, s59\n"
        "s_sub_u32 s59, 0, s59\n"
        "s_add_u32 s47, s44, %4\n"
        "s_min_u32 s48, s47, 63\n"
        "s_mul_i32 s57, s48, 28672\n"
        "s_add_u32 s57, s57, s46\n"
        "s_add_u32 s58, s57, 4096\n"
        "s_mov_b32 m0, s45\n"
        "s_nop 0\n"
        "buffer_load_dwordx4 v16, s[40:43], s57 offen offset:0 lds\n"
        "buffer_load_dwordx4 v16, s[40:43], s57 offen offset:1024 lds\n"
        "buffer_load_dwordx4 v16, s[40:43], s57 offen offset:2048 lds\n"
        "buffer_load_dwordx4 v16, s[40:43], s57 offen offset:3072 lds\n"
        "s_add_u32 m0, s45, 4096\n"
        "s_nop 0\n"
        "buffer_load_dwordx4 v16, s[40:43], s58 offen offset:0 lds\n"
        "buffer_load_dwordx4 v16, s[40:43], s58 offen offset:1024 lds\n"
        "buffer_load_dwordx4 v16, s[40:43], s58 offen offset:2048 lds\n"
        "s_mul_i32 s57, s44, 114688\n"
        "s_add_u32 s50, s57, %9\n"
        "s_add_u32 s51, s57, %10\n"
        "s_add_u32 s52, s50, 4096\n"
        "s_add_u32 s53, s51, 229376\n"
        "s_lshl_b32 s54, s44, 9\n"
        "ds_read_b128 v[96:99], v21 offset:1024\n"
        "ds_read_b128 v[100:103], v21 offset:15360\n"
        "s_waitcnt lgkmcnt(2)\n"
        "v_mfma_f32_16x16x32_bf16 v[32:35], a[0:3], v[88:91], 0\n"
        "v_mfma_f32_16x16x32_bf16 v[36:39], a[4:7], v[88:91], 0\n"
        "v_mfma_f32_16x16x32_bf16 v[40:43], a[8:11], v[88:91], 0\n"
        "v_mfma_f32_16x16x32_bf16 v[44:47], a[12:15], v[88:91], 0\n"
        "v_mfma_f32_16x16x32_bf16 v[48:51], a[16:19], v[88:91], 0\n"
        "v_mfma_f32_16x16x32_bf16 v[52:55], a[20:23], v[88:91], 0\n"
        "v_mfma_f32_16x16x32_bf16 v[56:59], a[24:27], v[88:91], 0\n"
        "v_mfma_f32_16x16x32_bf16 v[60:63], a[0:3], v[92:95], 0\n"
        "v_mfma_f32_16x16x32_bf16 v[64:67], a[4:7], v[92:95], 0\n"
        "v_mfma_f32_16x16x32_bf16 v[68:71], a[8:11], v[92:95], 0\n"
        "v_mfma_f32_16x16x32_bf16 v[72:75], a[12:15], v[92:95], 0\n"
        "v_mfma_f32_16x16x32_bf16 v[76:79], a[16:19], v[92:95], 0\n"
        "v_mfma_f32_16x16x32_bf16 v[80:83], a[20:23], v[92:95], 0\n"
        "v_mfma_f32_16x16x32_bf16 v[84:87], a[24:27], v[92:95], 0\n"
        "ds_read_b128 v[88:91], v21 offset:2048\n"
        "ds_read_b128 v[92:95], v21 offset:16384\n"
        "s_waitcnt lgkmcnt(2)\n"
        "v_mfma_f32_16x16x32_bf16 v[32:35], a[28:31], v[96:99], v[32:35]\n"
        "v_mfma_f32_16x16x32_bf16 v[36:39], a[32:35], v[96:99], v[36:39]\n"
        "v_mfma_f32_16x16x32_bf16 v[40:43], a[36:39], v[96:99], v[40:43]\n"
        "v_mfma_f32_16x16x32_bf16 v[44:47], a[40:43], v[96:99], v[44:47]\n"
        "v_mfma_f32_16x16x32_bf16 v[48:51], a[44:47], v[96:99], v[48:51]\n"
        "v_mfma_f32_16x16x32_bf16 v[52:55], a[48:51], v[96:99], v[52:55]\n"
        "v_mfma_f32_16x16x32_bf16 v[56:59], a[52:55], v[96:99], v[56:59]\n"
        "v_mfma_f32_16x16x32_bf16 v[60:63], a[28:31], v[100:103], v[60:63]\n"
        "v_mfma_f32_16x16x32_bf16 v[64:67], a[32:35], v[100:103], v[64:67]\n"
        "v_mfma_f32_16x16x32_bf16 v[68:71], a[36:39], v[100:103], v[68:71]\n"
        "v_mfma_f32_16x16x32_bf16 v[72:75], a[40:43], v[100:103], v[72:75]\n"
        "v_mfma_f32_16x16x32_bf16 v[76:79], a[44:47], v[100:103], v[76:79]\n"
        "v_mfma_f32_16x16x32_bf16 v[80:83], a[48:51], v[100:103], v[80:83]\n"
        "v_mfma_f32_16x16x32_bf16 v[84:87], a[52:55], v[100:103], v[84:87]\n"
        "ds_read_b128 v[96:99], v21 offset:3072\n"
        "ds_read_b128 v[100:103], v21 offset:17408\n"
        "s_waitcnt lgkmcnt(2)\n"
        "v_mfma_f32_16x16x32_bf16 v[32:35], a[56:59], v[88:91], v[32:35]\n"
        "v_mfma_f32_16x16x32_bf16 v[36:39], a[60:63], v[88:91], v[36:39]\n"
        "v_mfma_f32_16x16x32_bf16 v[40:43], a[64:67], v[88:91], v[40:43]\n"
        "v_mfma_f32_16x16x32_bf16 v[44:47], a[68:71], v[88:91], v[44:47]\n"
        "v_mfma_f32_16x16x32_bf16 v[48:51], a[72:75], v[88:91], v[48:51]\n"
        "v_mfma_f32_16x16x32_bf16 v[52:55], a[76:79], v[88:91], v[52:55]\n"
        "v_mfma_f32_16x16x32_bf16 v[56:59], a[80:83], v[88:91], v[56:59]\n"
        "v_mfma_f32_16x16x32_bf16 v[60:63], a[56:59], v[92:95], v[60:63]\n"
        "v_mfma_f32_16x16x32_bf16 v[64:67], a[60:63], v[92:95], v[64:67]\n"
        "v_mfma_f32_16x16x32_bf16 v[68:71], a[64:67], v[92:95], v[68:71]\n"
        "v_mfma_f32_16x16x32_bf16 v[72:75], a[68:71], v[92:95], v[72:75]\n"
        "v_mfma_f32_16x16x32_bf16 v[76:79], a[72:75], v[92:95], v[76:79]\n"
        "v_mfma_f32_16x16x32_bf16 v[80:83], a[76:79], v[92:95], v[80:83]\n"
        "v_mfma_f32_16x16x32_bf16 v[84:87], a[80:83], v[92:95], v[84:87]\n"
        "ds_read_b128 v[88:91], v21 offset:4096\n"
        "ds_read_b128 v[92:95], v21 offset:18432\n"
        "s_waitcnt lgkmcnt(2)\n"
        "v_mfma_f32_16x16x32_bf16 v[32:35], a[84:87], v[96:99], v[32:35]\n"
        "v_mfma_f32_16x16x32_bf16 v[36:39], a[88:91], v[96:99], v[36:39]\n"
        "v_mfma_f32_16x16x32_bf16 v[40:43], a[92:95], v[96:99], v[40:43]\n"
        "v_mfma_f32_16x16x32_bf16 v[44:47], a[96:99], v[96:99], v[44:47]\n"
        "v_mfma_f32_16x16x32_bf16 v[48:51], a[100:103], v[96:99], v[48:51]\n"
        "v_mfma_f32_16x16x32_bf16 v[52:55], a[104:107], v[96:99], v[52:55]\n"
        "v_mfma_f32_16x16x32_bf16 v[56:59], a[108:111], v[96:99], v[56:59]\n"
        "v_mfma_f32_16x16x32_bf16 v[60:63], a[84:87], v[100:103], v[60:63]\n"
        "v_mfma_f32_16x16x32_bf16 v[64:67], a[88:91], v[100:103], v[64:67]\n"
        "v_mfma_f32_16x16x32_bf16 v[68:71], a[92:95], v[100:103], v[68:71]\n"
        "v_mfma_f32_16x16x32_bf16 v[72:75], a[96:99], v[100:103], v[72:75]\n"
        "v_mfma_f32_16x16x32_bf16 v[76:79], a[100:103], v[100:103], v[76:79]\n"
        "v_mfma_f32_16x16x32_bf16 v[80:83], a[104:107], v[100:103], v[80:83]\n"
        "v_mfma_f32_16x16x32_bf16 v[84:87], a[108:111], v[100:103], v[84:87]\n"
        "ds_read_b128 v[96:99], v21 offset:5120\n"
        "ds_read_b128 v[100:103], v21 offset:19456\n"
        "s_waitcnt lgkmcnt(2)\n"
        "v_mfma_f32_16x16x32_bf16 v[32:35], a[112:115], v[88:91], v[32:35]\n"
        "v_mfma_f32_16x16x32_bf16 v[36:39], a[116:119], v[88:91], v[36:39]\n"
        "v_mfma_f32_16x16x32_bf16 v[40:43], a[120:123], v[88:91], v[40:43]\n"
        "v_mfma_f32_16x16x32_bf16 v[44:47], a[124:127], v[88:91], v[44:47]\n"
        "v_mfma_f32_16x16x32_bf16 v[48:51], a[128:131], v[88:91], v[48:51]\n"
        "v_mfma_f32_16x16x32_bf16 v[52:55], a[132:135], v[88:91], v[52:55]\n"
        "v_mfma_f32_16x16x32_bf16 v[56:59], a[136:139], v[88:91], v[56:59]\n"
        "v_mfma_f32_16x16x32_bf16 v[60:63], a[112:115], v[92:95], v[60:63]\n"
        "v_mfma_f32_16x16x32_bf16 v[64:67], a[116:119], v[92:95], v[64:67]\n"
        "v_mfma_f32_16x16x32_bf16 v[68:71], a[120:123], v[92:95], v[68:71]\n"
        "v_mfma_f32_16x16x32_bf16 v[72:75], a[124:127], v[92:95], v[72:75]\n"
        "v_mfma_f32_16x16x32_bf16 v[76:79], a[128:131], v[92:95], v[76:79]\n"
        "v_mfma_f32_16x16x32_bf16 v[80:83], a[132:135], v[92:95], v[80:83]\n"
        "v_mfma_f32_16x16x32_bf16 v[84:87], a[136:139], v[92:95], v[84:87]\n"
        "ds_read_b128 v[88:91], v21 offset:6144\n"
        "ds_read_b128 v[92:95], v21 offset:20480\n"
        "s_waitcnt lgkmcnt(2)\n"
        "v_mfma_f32_16x16x32_bf16 v[32:35], a[140:143], v[96:99], v[32:35]\n"
        "v_mfma_f32_16x16x32_bf16 v[36:39], a[144:147], v[96:99], v[36:39]\n"
        "v_mfma_f32_16x16x32_bf16 v[40:43], a[148:151], v[96:99], v[40:43]\n"
        "v_mfma_f32_16x16x32_bf16 v[44:47], a[152:155], v[96:99], v[44:47]\n"
        "v_mfma_f32_16x16x32_bf16 v[48:51], a[156:159], v[96:99], v[48:51]\n"
        "v_mfma_f32_16x16x32_bf16 v[52:55], a[160:163], v[96:99], v[52:55]\n"
        "v_mfma_f32_16x16x32_bf16 v[56:59], a[164:167], v[96:99], v[56:59]\n"
        "v_mfma_f32_16x16x32_bf16 v[60:63], a[140:143], v[100:103], v[60:63]\n"
        "v_mfma_f32_16x16x32_bf16 v[64:67], a[144:147], v[100:103], v[64:67]\n"
        "v_mfma_f32_16x16x32_bf16 v[68:71], a[148:151], v[100:103], v[68:71]\n"
        "v_mfma_f32_16x16x32_bf16 v[72:75], a[152:155], v[100:103], v[72:75]\n"
        "v_mfma_f32_16x16x32_bf16 v[76:79], a[156:159], v[100:103], v[76:79]\n"
        "v_mfma_f32_16x16x32_bf16 v[80:83], a[160:163], v[100:103], v[80:83]\n"
        "v_mfma_f32_16x16x32_bf16 v[84:87], a[164:167], v[100:103], v[84:87]\n"
        "ds_read_b128 v[96:99], v21 offset:7168\n"
        "ds_read_b128 v[100:103], v21 offset:21504\n"
        "s_waitcnt lgkmcnt(2)\n"
        "v_mfma_f32_16x16x32_bf16 v[32:35], a[168:171], v[88:91], v[32:35]\n"
        "v_mfma_f32_16x16x32_bf16 v[36:39], a[172:175], v[88:91], v[36:39]\n"
        "v_mfma_f32_16x16x32_bf16 v[40:43], a[176:179], v[88:91], v[40:43]\n"
        "v_mfma_f32_16x16x32_bf16 v[44:47], a[180:183], v[88:91], v[44:47]\n"
        "v_mfma_f32_16x16x32_bf16 v[48:51], a[184:187], v[88:91], v[48:51]\n"
        "v_mfma_f32_16x16x32_bf16 v[52:55], a[188:191], v[88:91], v[52:55]\n"
        "v_mfma_f32_16x16x32_bf16 v[56:59], a[192:195], v[88:91], v[56:59]\n"
        "v_mfma_f32_16x16x32_bf16 v[60:63], a[168:171], v[92:95], v[60:63]\n"
        "v_mfma_f32_16x16x32_bf16 v[64:67], a[172:175], v[92:95], v[64:67]\n"
        "v_mfma_f32_16x16x32_bf16 v[68:71], a[176:179], v[92:95], v[68:71]\n"
        "v_mfma_f32_16x16x32_bf16 v[72:75], a[180:183], v[92:95], v[72:75]\n"
        "v_mfma_f32_16x16x32_bf16 v[76:79], a[184:187], v[92:95], v[76:79]\n"
        "v_mfma_f32_16x16x32_bf16 v[80:83], a[188:191], v[92:95], v[80:83]\n"
        "v_mfma_f32_16x16x32_bf16 v[84:87], a[192:195], v[92:95], v[84:87]\n"
        "ds_read_b128 v[88:91], v21 offset:8192\n"
        "ds_read_b128 v[92:95], v21 offset:22528\n"
        "s_waitcnt lgkmcnt(2)\n"
        "v_mfma_f32_16x16x32_bf16 v[32:35], a[196:199], v[96:99], v[32:35]\n"
        "v_mfma_f32_16x16x32_bf16 v[36:39], a[200:203], v[96:99], v[36:39]\n"
        "v_mfma_f32_16x16x32_bf16 v[40:43], a[204:207], v[96:99], v[40:43]\n"
        "v_mfma_f32_16x16x32_bf16 v[44:47], a[208:211], v[96:99], v[44:47]\n"
        "v_mfma_f32_16x16x32_bf16 v[48:51], a[212:215], v[96:99], v[48:51]\n"
        "v_mfma_f32_16x16x32_bf16 v[52:55], a[216:219], v[96:99], v[52:55]\n"
        "v_mfma_f32_16x16x32_bf16 v[56:59], a[220:223], v[96:99], v[56:59]\n"
        "v_mfma_f32_16x16x32_bf16 v[60:63], a[196:199], v[100:103], v[60:63]\n"
        "v_mfma_f32_16x16x32_bf16 v[64:67], a[200:203], v[100:103], v[64:67]\n"
        "v_mfma_f32_16x16x32_bf16 v[68:71], a[204:207], v[100:103], v[68:71]\n"
        "v_mfma_f32_16x16x32_bf16 v[72:75], a[208:211], v[100:103], v[72:75]\n"
        "v_mfma_f32_16x16x32_bf16 v[76:79], a[212:215], v[100:103], v[76:79]\n"
        "v_mfma_f32_16x16x32_bf16 v[80:83], a[216:219], v[100:103], v[80:83]\n"
        "v_mfma_f32_16x16x32_bf16 v[84:87], a[220:223], v[100:103], v[84:87]\n"
        "ds_read_b128 v[96:99], v21 offset:9216\n"
        "ds_read_b128 v[100:103], v21 offset:23552\n"
        "s_waitcnt lgkmcnt(2)\n"
        "v_mfma_f32_16x16x32_bf16 v[32:35], a[224:227], v[88:91], v[32:35]\n"
        "v_mfma_f32_16x16x32_bf16 v[36:39], a[228:231], v[88:91], v[36:39]\n"
        "v_mfma_f32_16x16x32_bf16 v[40:43], a[232:235], v[88:91], v[40:43]\n"
        "v_mfma_f32_16x16x32_bf16 v[44:47], a[236:239], v[88:91], v[44:47]\n"
        "v_mfma_f32_16x16x32_bf16 v[48:51], a[240:243], v[88:91], v[48:51]\n"
        "v_mfma_f32_16x16x32_bf16 v[52:55], a[244:247], v[88:91], v[52:55]\n"
        "v_mfma_f32_16x16x32_bf16 v[56:59], a[248:251], v[88:91], v[56:59]\n"
        "v_mfma_f32_16x16x32_bf16 v[60:63], a[224:227], v[92:95], v[60:63]\n"
        "v_mfma_f32_16x16x32_bf16 v[64:67], a[228:231], v[92:95], v[64:67]\n"
        "v_mfma_f32_16x16x32_bf16 v[68:71], a[232:235], v[92:95], v[68:71]\n"
        "v_mfma_f32_16x16x32_bf16 v[72:75], a[236:239], v[92:95], v[72:75]\n"
        "v_mfma_f32_16x16x32_bf16 v[76:79], a[240:243], v[92:95], v[76:79]\n"
        "v_mfma_f32_16x16x32_bf16 v[80:83], a[244:247], v[92:95], v[80:83]\n"
        "v_mfma_f32_16x16x32_bf16 v[84:87], a[248:251], v[92:95], v[84:87]\n"
        "ds_read_b128 v[88:91], v21 offset:10240\n"
        "ds_read_b128 v[92:95], v21 offset:24576\n"
        "s_waitcnt lgkmcnt(2)\n"
        "v_mfma_f32_16x16x32_bf16 v[32:35], a[252:255], v[96:99], v[32:35]\n"
        "v_mfma_f32_16x16x32_bf16 v[36:39], v[120:123], v[96:99], v[36:39]\n"
        "v_mfma_f32_16x16x32_bf16 v[40:43], v[124:127], v[96:99], v[40:43]\n"
        "v_mfma_f32_16x16x32_bf16 v[44:47], v[128:131], v[96:99], v[44:47]\n"
        "v_mfma_f32_16x16x32_bf16 v[48:51], v[132:135], v[96:99], v[48:51]\n"
        "v_mfma_f32_16x16x32_bf16 v[52:55], v[136:139], v[96:99], v[52:55]\n"
        "v_mfma_f32_16x16x32_bf16 v[56:59], v[140:143], v[96:99], v[56:59]\n"
        "v_mfma_f32_16x16x32_bf16 v[60:63], a[252:255], v[100:103], v[60:63]\n"
        "v_mfma_f32_16x16x32_bf16 v[64:67], v[120:123], v[100:103], v[64:67]\n"
        "v_mfma_f32_16x16x32_bf16 v[68:71], v[124:127], v[100:103], v[68:71]\n"
        "v_mfma_f32_16x16x32_bf16 v[72:75], v[128:131], v[100:103], v[72:75]\n"
        "v_mfma_f32_16x16x32_bf16 v[76:79], v[132:135], v[100:103], v[76:79]\n"
        "v_mfma_f32_16x16x32_bf16 v[80:83], v[136:139], v[100:103], v[80:83]\n"
        "v_mfma_f32_16x16x32_bf16 v[84:87], v[140:143], v[100:103], v[84:87]\n"
        "ds_read_b128 v[96:99], v21 offset:11264\n"
        "ds_read_b128 v[100:103], v21 offset:25600\n"
        "s_waitcnt lgkmcnt(2)\n"
        "v_mfma_f32_16x16x32_bf16 v[32:35], v[144:147], v[88:91], v[32:35]\n"
        "v_mfma_f32_16x16x32_bf16 v[36:39], v[148:151], v[88:91], v[36:39]\n"
        "v_mfma_f32_16x16x32_bf16 v[40:43], v[152:155], v[88:91], v[40:43]\n"
        "v_mfma_f32_16x16x32_bf16 v[44:47], v[156:159], v[88:91], v[44:47]\n"
        "v_mfma_f32_16x16x32_bf16 v[48:51], v[160:163], v[88:91], v[48:51]\n"
        "v_mfma_f32_16x16x32_bf16 v[52:55], v[164:167], v[88:91], v[52:55]\n"
        "v_mfma_f32_16x16x32_bf16 v[56:59], v[168:171], v[88:91], v[56:59]\n"
        "v_mfma_f32_16x16x32_bf16 v[60:63], v[144:147], v[92:95], v[60:63]\n"
        "v_mfma_f32_16x16x32_bf16 v[64:67], v[148:151], v[92:95], v[64:67]\n"
        "v_mfma_f32_16x16x32_bf16 v[68:71], v[152:155], v[92:95], v[68:71]\n"
        "v_mfma_f32_16x16x32_bf16 v[72:75], v[156:159], v[92:95], v[72:75]\n"
        "v_mfma_f32_16x16x32_bf16 v[76:79], v[160:163], v[92:95], v[76:79]\n"
        "v_mfma_f32_16x16x32_bf16 v[80:83], v[164:167], v[92:95], v[80:83]\n"
        "v_mfma_f32_16x16x32_bf16 v[84:87], v[168:171], v[92:95], v[84:87]\n"
        "ds_read_b128 v[88:91], v21 offset:12288\n"
        "ds_read_b128 v[92:95], v21 offset:26624\n"
        "s_waitcnt lgkmcnt(2)\n"
        "v_mfma_f32_16x16x32_bf16 v[32:35], v[172:175], v[96:99], v[32:35]\n"
        "v_mfma_f32_16x16x32_bf16 v[36:39], v[176:179], v[96:99], v[36:39]\n"
        "v_mfma_f32_16x16x32_bf16 v[40:43], v[180:183], v[96:99], v[40:43]\n"
        "v_mfma_f32_16x16x32_bf16 v[44:47], v[184:187], v[96:99], v[44:47]\n"
        "v_mfma_f32_16x16x32_bf16 v[48:51], v[188:191], v[96:99], v[48:51]\n"
        "v_mfma_f32_16x16x32_bf16 v[52:55], v[192:195], v[96:99], v[52:55]\n"
        "v_mfma_f32_16x16x32_bf16 v[56:59], v[196:199], v[96:99], v[56:59]\n"
        "v_mfma_f32_16x16x32_bf16 v[60:63], v[172:175], v[100:103], v[60:63]\n"
        "v_mfma_f32_16x16x32_bf16 v[64:67], v[176:179], v[100:103], v[64:67]\n"
        "v_mfma_f32_16x16x32_bf16 v[68:71], v[180:183], v[100:103], v[68:71]\n"
        "v_mfma_f32_16x16x32_bf16 v[72:75], v[184:187], v[100:103], v[72:75]\n"
        "v_mfma_f32_16x16x32_bf16 v[76:79], v[188:191], v[100:103], v[76:79]\n"
        "v_mfma_f32_16x16x32_bf16 v[80:83], v[192:195], v[100:103], v[80:83]\n"
        "v_mfma_f32_16x16x32_bf16 v[84:87], v[196:199], v[100:103], v[84:87]\n"
        "ds_read_b128 v[96:99], v21 offset:13312\n"
        "ds_read_b128 v[100:103], v21 offset:27648\n"
        "s_waitcnt lgkmcnt(2)\n"
        "v_mfma_f32_16x16x32_bf16 v[32:35], v[200:203], v[88:91], v[32:35]\n"
        "v_mfma_f32_16x16x32_bf16 v[36:39], v[204:207], v[88:91], v[36:39]\n"
        "v_mfma_f32_16x16x32_bf16 v[40:43], v[208:211], v[88:91], v[40:43]\n"
        "v_mfma_f32_16x16x32_bf16 v[44:47], v[212:215], v[88:91], v[44:47]\n"
        "v_mfma_f32_16x16x32_bf16 v[48:51], v[216:219], v[88:91], v[48:51]\n"
        "v_mfma_f32_16x16x32_bf16 v[52:55], v[220:223], v[88:91], v[52:55]\n"
        "v_mfma_f32_16x16x32_bf16 v[56:59], v[224:227], v[88:91], v[56:59]\n"
        "v_mfma_f32_16x16x32_bf16 v[60:63], v[200:203], v[92:95], v[60:63]\n"
        "v_mfma_f32_16x16x32_bf16 v[64:67], v[204:207], v[92:95], v[64:67]\n"
        "v_mfma_f32_16x16x32_bf16 v[68:71], v[208:211], v[92:95], v[68:71]\n"
        "v_mfma_f32_16x16x32_bf16 v[72:75], v[212:215], v[92:95], v[72:75]\n"
        "v_mfma_f32_16x16x32_bf16 v[76:79], v[216:219], v[92:95], v[76:79]\n"
        "v_mfma_f32_16x16x32_bf16 v[80:83], v[220:223], v[92:95], v[80:83]\n"
        "v_mfma_f32_16x16x32_bf16 v[84:87], v[224:227], v[92:95], v[84:87]\n"
        "s_waitcnt lgkmcnt(0)\n"
        "v_mfma_f32_16x16x32_bf16 v[32:35], v[228:231], v[96:99], v[32:35]\n"
        "v_mfma_f32_16x16x32_bf16 v[36:39], v[232:235], v[96:99], v[36:39]\n"
        "v_mfma_f32_16x16x32_bf16 v[40:43], v[236:239], v[96:99], v[40:43]\n"
        "v_mfma_f32_16x16x32_bf16 v[44:47], v[240:243], v[96:99], v[44:47]\n"
        "v_mfma_f32_16x16x32_bf16 v[48:51], v[244:247], v[96:99], v[48:51]\n"
        "v_mfma_f32_16x16x32_bf16 v[52:55], v[248:251], v[96:99], v[52:55]\n"
        "v_mfma_f32_16x16x32_bf16 v[56:59], v[252:255], v[96:99], v[56:59]\n"
        "v_mfma_f32_16x16x32_bf16 v[60:63], v[228:231], v[100:103], v[60:63]\n"
        "v_mfma_f32_16x16x32_bf16 v[64:67], v[232:235], v[100:103], v[64:67]\n"
        "v_mfma_f32_16x16x32_bf16 v[68:71], v[236:239], v[100:103], v[68:71]\n"
        "v_mfma_f32_16x16x32_bf16 v[72:75], v[240:243], v[100:103], v[72:75]\n"
        "v_mfma_f32_16x16x32_bf16 v[76:79], v[244:247], v[100:103], v[76:79]\n"
        "v_mfma_f32_16x16x32_bf16 v[80:83], v[248:251], v[100:103], v[80:83]\n"
        "v_mfma_f32_16x16x32_bf16 v[84:87], v[252:255], v[100:103], v[84:87]\n"
        "v_permlane16_swap_b32 v32, v60\n"
        "v_permlane16_swap_b32 v33, v61\n"
        "v_permlane16_swap_b32 v34, v62\n"
        "v_permlane16_swap_b32 v35, v63\n"
        "v_permlane16_swap_b32 v36, v64\n"
        "v_permlane16_swap_b32 v37, v65\n"
        "v_permlane16_swap_b32 v38, v66\n"
        "v_permlane16_swap_b32 v39, v67\n"
        "v_permlane16_swap_b32 v40, v68\n"
        "v_permlane16_swap_b32 v41, v69\n"
        "v_permlane16_swap_b32 v42, v70\n"
        "v_permlane16_swap_b32 v43, v71\n"
        "v_permlane16_swap_b32 v44, v72\n"
        "v_permlane16_swap_b32 v45, v73\n"
        "v_permlane16_swap_b32 v46, v74\n"
        "v_permlane16_swap_b32 v47, v75\n"
        "v_permlane16_swap_b32 v48, v76\n"
        "v_permlane16_swap_b32 v49, v77\n"
        "v_permlane16_swap_b32 v50, v78\n"
        "v_permlane16_swap_b32 v51, v79\n"
        "v_permlane16_swap_b32 v52, v80\n"
        "v_permlane16_swap_b32 v53, v81\n"
        "v_permlane16_swap_b32 v54, v82\n"
        "v_permlane16_swap_b32 v55, v83\n"
        "v_permlane16_swap_b32 v56, v84\n"
        "v_permlane16_swap_b32 v57, v85\n"
        "v_permlane16_swap_b32 v58, v86\n"
        "v_permlane16_swap_b32 v59, v87\n"
        "v_pk_mul_f32 v[88:89], v[32:33], s[74:75]\n"
        "v_pk_mul_f32 v[90:91], v[34:35], s[74:75]\n"
        "v_pk_mul_f32 v[92:93], v[36:37], s[74:75]\n"
        "v_pk_mul_f32 v[94:95], v[38:39], s[74:75]\n"
        "v_pk_mul_f32 v[96:97], v[40:41], s[74:75]\n"
        "v_pk_mul_f32 v[98:99], v[42:43], s[74:75]\n"
        "v_pk_mul_f32 v[100:101], v[44:45], s[74:75]\n"
        "v_pk_mul_f32 v[102:103], v[46:47], s[74:75]\n"
        "v_exp_f32 v88, v88\n"
        "v_exp_f32 v89, v89\n"
        "v_exp_f32 v90, v90\n"
        "v_exp_f32 v91, v91\n"
        "v_exp_f32 v92, v92\n"
        "v_exp_f32 v93, v93\n"
        "v_exp_f32 v94, v94\n"
        "v_exp_f32 v95, v95\n"
        "v_exp_f32 v96, v96\n"
        "v_exp_f32 v97, v97\n"
        "v_exp_f32 v98, v98\n"
        "v_exp_f32 v99, v99\n"
        "v_exp_f32 v100, v100\n"
        "v_exp_f32 v101, v101\n"
        "v_exp_f32 v102, v102\n"
        "v_exp_f32 v103, v103\n"
        "v_pk_add_f32 v[88:89], v[88:89], 1.0 op_sel_hi:[1,0]\n"
        "v_pk_add_f32 v[90:91], v[90:91], 1.0 op_sel_hi:[1,0]\n"
        "v_pk_add_f32 v[92:93], v[92:93], 1.0 op_sel_hi:[1,0]\n"
        "v_pk_add_f32 v[94:95], v[94:95], 1.0 op_sel_hi:[1,0]\n"
        "v_pk_add_f32 v[96:97], v[96:97], 1.0 op_sel_hi:[1,0]\n"
        "v_pk_add_f32 v[98:99], v[98:99], 1.0 op_sel_hi:[1,0]\n"
        "v_pk_add_f32 v[100:101], v[100:101], 1.0 op_sel_hi:[1,0]\n"
        "v_pk_add_f32 v[102:103], v[102:103], 1.0 op_sel_hi:[1,0]\n"
        "v_rcp_f32 v88, v88\n"
        "v_rcp_f32 v89, v89\n"
        "v_rcp_f32 v90, v90\n"
        "v_rcp_f32 v91, v91\n"
        "v_rcp_f32 v92, v92\n"
        "v_rcp_f32 v93, v93\n"
        "v_rcp_f32 v94, v94\n"
        "v_rcp_f32 v95, v95\n"
        "v_rcp_f32 v96, v96\n"
        "v_rcp_f32 v97, v97\n"
        "v_rcp_f32 v98, v98\n"
        "v_rcp_f32 v99, v99\n"
        "v_rcp_f32 v100, v100\n"
        "v_rcp_f32 v101, v101\n"
        "v_rcp_f32 v102, v102\n"
        "v_rcp_f32 v103, v103\n"
        "v_pk_mul_f32 v[32:33], v[32:33], v[60:61]\n"
        "v_pk_mul_f32 v[34:35], v[34:35], v[62:63]\n"
        "v_pk_mul_f32 v[36:37], v[36:37], v[64:65]\n"
        "v_pk_mul_f32 v[38:39], v[38:39], v[66:67]\n"
        "v_pk_mul_f32 v[40:41], v[40:41], v[68:69]\n"
        "v_pk_mul_f32 v[42:43], v[42:43], v[70:71]\n"
        "v_pk_mul_f32 v[44:45], v[44:45], v[72:73]\n"
        "v_pk_mul_f32 v[46:47], v[46:47], v[74:75]\n"
        "v_pk_mul_f32 v[32:33], v[32:33], v[88:89]\n"
        "v_pk_mul_f32 v[34:35], v[34:35], v[90:91]\n"
        "v_pk_mul_f32 v[36:37], v[36:37], v[92:93]\n"
        "v_pk_mul_f32 v[38:39], v[38:39], v[94:95]\n"
        "v_pk_mul_f32 v[40:41], v[40:41], v[96:97]\n"
        "v_pk_mul_f32 v[42:43], v[42:43], v[98:99]\n"
        "v_pk_mul_f32 v[44:45], v[44:45], v[100:101]\n"
        "v_pk_mul_f32 v[46:47], v[46:47], v[102:103]\n"
        "v_cvt_pk_bf16_f32 v60, v32, v33\n"
        "v_cvt_pk_bf16_f32 v61, v34, v35\n"
        "v_cvt_pk_bf16_f32 v62, v36, v37\n"
        "v_cvt_pk_bf16_f32 v63, v38, v39\n"
        "v_cvt_pk_bf16_f32 v68, v40, v41\n"
        "v_cvt_pk_bf16_f32 v69, v42, v43\n"
        "v_cvt_pk_bf16_f32 v70, v44, v45\n"
        "v_cvt_pk_bf16_f32 v71, v46, v47\n"
        "v_permlane32_swap_b32 v60, v62\n"
        "v_permlane32_swap_b32 v61, v63\n"
        "buffer_store_dwordx4 v[60:63], v20, s[40:43], s50 offen offset:0 sc1\n"
        "v_permlane32_swap_b32 v68, v70\n"
        "v_permlane32_swap_b32 v69, v71\n"
        "buffer_store_dwordx4 v[68:71], v20, s[40:43], s50 offen offset:512 sc1\n"
        "v_pk_mul_f32 v[88:89], v[48:49], s[74:75]\n"
        "v_pk_mul_f32 v[90:91], v[50:51], s[74:75]\n"
        "v_pk_mul_f32 v[92:93], v[52:53], s[74:75]\n"
        "v_pk_mul_f32 v[94:95], v[54:55], s[74:75]\n"
        "v_pk_mul_f32 v[96:97], v[56:57], s[74:75]\n"
        "v_pk_mul_f32 v[98:99], v[58:59], s[74:75]\n"
        "v_exp_f32 v88, v88\n"
        "v_exp_f32 v89, v89\n"
        "v_exp_f32 v90, v90\n"
        "v_exp_f32 v91, v91\n"
        "v_exp_f32 v92, v92\n"
        "v_exp_f32 v93, v93\n"
        "v_exp_f32 v94, v94\n"
        "v_exp_f32 v95, v95\n"
        "v_exp_f32 v96, v96\n"
        "v_exp_f32 v97, v97\n"
        "v_exp_f32 v98, v98\n"
        "v_exp_f32 v99, v99\n"
        "v_pk_add_f32 v[88:89], v[88:89], 1.0 op_sel_hi:[1,0]\n"
        "v_pk_add_f32 v[90:91], v[90:91], 1.0 op_sel_hi:[1,0]\n"
        "v_pk_add_f32 v[92:93], v[92:93], 1.0 op_sel_hi:[1,0]\n"
        "v_pk_add_f32 v[94:95], v[94:95], 1.0 op_sel_hi:[1,0]\n"
        "v_pk_add_f32 v[96:97], v[96:97], 1.0 op_sel_hi:[1,0]\n"
        "v_pk_add_f32 v[98:99], v[98:99], 1.0 op_sel_hi:[1,0]\n"
        "v_rcp_f32 v88, v88\n"
        "v_rcp_f32 v89, v89\n"
        "v_rcp_f32 v90, v90\n"
        "v_rcp_f32 v91, v91\n"
        "v_rcp_f32 v92, v92\n"
        "v_rcp_f32 v93, v93\n"
        "v_rcp_f32 v94, v94\n"
        "v_rcp_f32 v95, v95\n"
        "v_rcp_f32 v96, v96\n"
        "v_rcp_f32 v97, v97\n"
        "v_rcp_f32 v98, v98\n"
        "v_rcp_f32 v99, v99\n"
        "v_pk_mul_f32 v[48:49], v[48:49], v[76:77]\n"
        "v_pk_mul_f32 v[50:51], v[50:51], v[78:79]\n"
        "v_pk_mul_f32 v[52:53], v[52:53], v[80:81]\n"
        "v_pk_mul_f32 v[54:55], v[54:55], v[82:83]\n"
        "v_pk_mul_f32 v[56:57], v[56:57], v[84:85]\n"
        "v_pk_mul_f32 v[58:59], v[58:59], v[86:87]\n"
        "v_pk_mul_f32 v[48:49], v[48:49], v[88:89]\n"
        "v_pk_mul_f32 v[50:51], v[50:51], v[90:91]\n"
        "v_pk_mul_f32 v[52:53], v[52:53], v[92:93]\n"
        "v_pk_mul_f32 v[54:55], v[54:55], v[94:95]\n"
        "v_pk_mul_f32 v[56:57], v[56:57], v[96:97]\n"
        "v_pk_mul_f32 v[58:59], v[58:59], v[98:99]\n"
        "v_cvt_pk_bf16_f32 v76, v48, v49\n"
        "v_cvt_pk_bf16_f32 v77, v50, v51\n"
        "v_cvt_pk_bf16_f32 v78, v52, v53\n"
        "v_cvt_pk_bf16_f32 v79, v54, v55\n"
        "v_cvt_pk_bf16_f32 v84, v56, v57\n"
        "v_cvt_pk_bf16_f32 v85, v58, v59\n"
        "v_permlane32_swap_b32 v76, v78\n"
        "v_permlane32_swap_b32 v77, v79\n"
        "buffer_store_dwordx4 v[76:79], v20, s[40:43], s50 offen offset:1024 sc1\n"
        "buffer_store_dwordx2 v[84:85], v19, s[40:43], s50 offen offset:1536 sc1\n"
        "v_xor_b32 v21, 0x7000, v21\n"
        "v_mov_b32 v104, 1\n"
        "s_mov_b32 s44, s47\n"
        "s_waitcnt vmcnt(0)\n"
        "s_barrier\n"
        "buffer_store_dword v104, v18, s[40:43], s54 offen sc1\n"
        "s_cmp_lt_u32 s44, 64\n"
        "s_cbranch_scc1 .Lw2_loop_%=\n"
        "s_waitcnt vmcnt(0)\n"
            :
            : "s"(lo), "s"(hi), "s"(WS_BYTES), "s"(rep), "s"(nrep), "s"(0), "s"(wv), "s"((unsigned)(uintptr_t)w1), "s"((unsigned)((uintptr_t)w1 >> 32)),
              "s"(OFF_HF + (28 * set + 7 * wv) * 256u), "s"(0), "s"((unsigned)(uintptr_t)w3), "s"((unsigned)((uintptr_t)w3 >> 32)), "s"((28 * set + 7 * wv) * 16u),
              "v"(vL), "v"(wv * 28672 + (8 * g + (n >> 2)) * 112 + (n & 1) * 7168 + (n & 2) * 4), "v"(vF), "v"((g & 1) * 57344 + n * 16 + (g >> 1) * 8), "v"((g & 1) * 57344 + n * 16 + (g >> 1) * 256)
            : "memory", "vcc", "scc", "s40", "s41", "s42", "s43", "s44", "s45", "s46", "s47", "s48", "s49", "s50", "s51", "s52", "s53", "s54", "s55", "s56", "s57", "s58", "s59", "s60", "s61", "s62", "s63", "s64", "s65", "s66", "s67", "s68", "s69", "s70", "s71", "s72", "s73", "s74", "s75", "s76", "s77", "s78", "s79", "v16", "v17", "v18", "v19", "v20", "v21", "v22", "v23", "v24", "v25", "v26", "v27", "v28", "v29", "v30", "v31", "v32", "v33", "v34", "v35", "v36", "v37", "v38", "v39", "v40", "v41", "v42", "v43", "v44", "v45", "v46", "v47", "v48", "v49", "v50", "v51", "v52", "v53", "v54", "v55", "v56", "v57", "v58", "v59", "v60", "v61", "v62", "v63", "v64", "v65", "v66", "v67", "v68", "v69", "v70", "v71", "v72", "v73", "v74", "v75", "v76", "v77", "v78", "v79", "v80", "v81", "v82", "v83", "v84", "v85", "v86", "v87", "v88", "v89", "v90", "v91", "v92", "v93", "v94", "v95", "v96", "v97", "v98", "v99", "v100", "v101", "v102", "v103", "v104", "v105", "v106", "v107", "v108", "v109", "v110", "v111", "v112", "v113", "v114", "v115", "v116", "v117", "v118", "v119", "v120", "v121", "v122", "v123", "v124", "v125", "v126", "v127", "v128", "v129", "v130", "v131", "v132", "v133", "v134", "v135", "v136", "v137", "v138", "v139", "v140", "v141", "v142", "v143", "v144", "v145", "v146", "v147", "v148", "v149", "v150", "v151", "v152", "v153", "v154", "v155", "v156", "v157", "v158", "v159", "v160", "v161", "v162", "v163", "v164", "v165", "v166", "v167", "v168", "v169", "v170", "v171", "v172", "v173", "v174", "v175", "v176", "v177", "v178", "v179", "v180", "v181", "v182", "v183", "v184", "v185", "v186", "v187", "v188", "v189", "v190", "v191", "v192", "v193", "v194", "v195", "v196", "v197", "v198", "v199", "v200", "v201", "v202", "v203", "v204", "v205", "v206", "v207", "v208", "v209", "v210", "v211", "v212", "v213", "v214", "v215", "v216", "v217", "v218", "v219", "v220", "v221", "v222", "v223", "v224", "v225", "v226", "v227", "v228", "v229", "v230", "v231", "v232", "v233", "v234", "v235", "v236", "v237", "v238", "v239", "v240", "v241", "v242", "v243", "v244", "v245", "v246", "v247", "v248", "v249", "v250", "v251", "v252", "v253", "v254", "v255", "a0", "a1", "a2", "a3", "a4", "a5", "a6", "a7", "a8", "a9", "a10", "a11", "a12", "a13", "a14", "a15", "a16", "a17", "a18", "a19", "a20", "a21", "a22", "a23", "a24", "a25", "a26", "a27", "a28", "a29", "a30", "a31", "a32", "a33", "a34", "a35", "a36", "a37", "a38", "a39", "a40", "a41", "a42", "a43", "a44", "a45", "a46", "a47", "a48", "a49", "a50", "a51", "a52", "a53", "a54", "a55", "a56", "a57", "a58", "a59", "a60", "a61", "a62", "a63", "a64", "a65", "a66", "a67", "a68", "a69", "a70", "a71", "a72", "a73", "a74", "a75", "a76", "a77", "a78", "a79", "a80", "a81", "a82", "a83", "a84", "a85", "a86", "a87", "a88", "a89", "a90", "a91", "a92", "a93", "a94", "a95", "a96", "a97", "a98", "a99", "a100", "a101", "a102", "a103", "a104", "a105", "a106", "a107", "a108", "a109", "a110", "a111", "a112", "a113", "a114", "a115", "a116", "a117", "a118", "a119", "a120", "a121", "a122", "a123", "a124", "a125", "a126", "a127", "a128", "a129", "a130", "a131", "a132", "a133", "a134", "a135", "a136", "a137", "a138", "a139", "a140", "a141", "a142", "a143", "a144", "a145", "a146", "a147", "a148", "a149", "a150", "a151", "a152", "a153", "a154", "a155", "a156", "a157", "a158", "a159", "a160", "a161", "a162", "a163", "a164", "a165", "a166", "a167", "a168", "a169", "a170", "a171", "a172", "a173", "a174", "a175", "a176", "a177", "a178", "a179", "a180", "a181", "a182", "a183", "a184", "a185", "a186", "a187", "a188", "a189", "a190", "a191", "a192", "a193", "a194", "a195", "a196", "a197", "a198", "a199", "a200", "a201", "a202", "a203", "a204", "a205", "a206", "a207", "a208", "a209", "a210", "a211", "a212", "a213", "a214", "a215", "a216", "a217", "a218", "a219", "a220", "a221", "a222", "a223", "a224", "a225", "a226", "a227", "a228", "a229", "a230", "a231", "a232", "a233", "a234", "a235", "a236", "a237", "a238", "a239", "a240", "a241", "a242", "a243", "a244", "a245", "a246", "a247", "a248", "a249", "a250", "a251", "a252", "a253", "a254", "a255");
    }
}

DI void stage2(const unsigned cg, const unsigned rep, const __bf16* w2, unsigned char* ws, const unsigned wv) {
    const unsigned lane = lane_id(), tid = wv * 64 + lane;
    const unsigned g = lane >> 4, n = lane & 15;
    const unsigned lo = (unsigned)(uintptr_t)ws, hi = (unsigned)((uintptr_t)ws >> 32);
    asm volatile(
        "s_mov_b32 s40, %0\n"
        "s_and_b32 s41, %1, 0xffff\n"
        "s_mov_b32 s42, %2\n"
        "s_mov_b32 s43, 0x00020000\n"
        "s_mov_b32 s60, %5\n"
        "s_and_b32 s61, %6, 0xffff\n"
        "s_mov_b32 s62, 1605632\n"
        "s_mov_b32 s63, 0x00020000\n"
        "s_mov_b32 s44, %3\n"
        "s_mov_b32 s74, 0xbfb8aa3b\n"
        "s_mov_b32 s75, 0xbfb8aa3b\n"
        "s_mov_b32 s55, 0x8000\n"
        "v_mov_b32 v16, %9\n"
        "v_mov_b32 v17, %10\n"
        "v_mov_b32 v18, %11\n"
        "v_mov_b32 v19, %12\n"
        "s_mul_i32 s57, %4, 14336\n"
        "v_add_u32 v24, s57, v16\n"
        "s_add_u32 s46, s57, 158334976\n"
        "s_lshl_b32 s57, %4, 10\n"
        "v_add_u32 v25, s57, v16\n"
        "v_lshrrev_b32 v104, 4, v16\n"
        "v_add_u32 v105, 0, v104\n"
        "v_mul_u32_u24 v106, 4682, v105\n"
        "v_lshrrev_b32 v106, 16, v106\n"
        "v_mul_u32_u24 v106, 672, v106\n"
        "v_lshl_add_u32 v32, v105, 4, v106\n"
        "v_add_u32 v105, 64, v104\n"
        "v_mul_u32_u24 v106, 4682, v105\n"
        "v_lshrrev_b32 v106, 16, v106\n"
        "v_mul_u32_u24 v106, 672, v106\n"
        "v_lshl_add_u32 v33, v105, 4, v106\n"
        "v_add_u32 v105, 128, v104\n"
        "v_mul_u32_u24 v106, 4682, v105\n"
        "v_lshrrev_b32 v106, 16, v106\n"
        "v_mul_u32_u24 v106, 672, v106\n"
        "v_lshl_add_u32 v34, v105, 4, v106\n"
        "v_add_u32 v105, 192, v104\n"
        "v_mul_u32_u24 v106, 4682, v105\n"
        "v_lshrrev_b32 v106, 16, v106\n"
        "v_mul_u32_u24 v106, 672, v106\n"
        "v_lshl_add_u32 v35, v105, 4, v106\n"
        "v_add_u32 v105, 256, v104\n"
        "v_mul_u32_u24 v106, 4682, v105\n"
        "v_lshrrev_b32 v106, 16, v106\n"
        "v_mul_u32_u24 v106, 672, v106\n"
        "v_lshl_add_u32 v36, v105, 4, v106\n"
        "v_add_u32 v105, 320, v104\n"
        "v_mul_u32_u24 v106, 4682, v105\n"
        "v_lshrrev_b32 v106, 16, v106\n"
        "v_mul_u32_u24 v106, 672, v106\n"
        "v_lshl_add_u32 v37, v105, 4, v106\n"
        "v_add_u32 v105, 384, v104\n"
        "v_mul_u32_u24 v106, 4682, v105\n"
        "v_lshrrev_b32 v106, 16, v106\n"
        "v_mul_u32_u24 v106, 672, v106\n"
        "v_lshl_add_u32 v38, v105, 4, v106\n"
        "s_mul_i32 s70, %4, 28672\n"
        "s_mov_b32 s71, %7\n"
        "s_add_u32 s72, s71, 28672\n"
        "s_add_u32 m0, s70, 0\n"
        "s_nop 0\n"
        "buffer_load_dwordx4 v32, s[60:63], s71 offen nt lds\n"
        "s_add_u32 m0, s70, 1024\n"
        "s_nop 0\n"
        "buffer_load_dwordx4 v33, s[60:63], s71 offen nt lds\n"
        "s_add_u32 m0, s70, 2048\n"
        "s_nop 0\n"
        "buffer_load_dwordx4 v34, s[60:63], s71 offen nt lds\n"
        "s_add_u32 m0, s70, 3072\n"
        "s_nop 0\n"
        "buffer_load_dwordx4 v35, s[60:63], s71 offen nt lds\n"
        "s_add_u32 m0, s70, 4096\n"
        "s_nop 0\n"
        "buffer_load_dwordx4 v36, s[60:63], s71 offen nt lds\n"
        "s_add_u32 m0, s70, 5120\n"
        "s_nop 0\n"
        "buffer_load_dwordx4 v37, s[60:63], s71 offen nt lds\n"
        "s_add_u32 m0, s70, 6144\n"
        "s_nop 0\n"
        "buffer_load_dwordx4 v38, s[60:63], s71 offen nt lds\n"
        "s_add_u32 m0, s70, 7168\n"
        "s_nop 0\n"
        "buffer_load_dwordx4 v32, s[60:63], s72 offen nt lds\n"
        "s_add_u32 m0, s70, 8192\n"
        "s_nop 0\n"
        "buffer_load_dwordx4 v33, s[60:63], s72 offen nt lds\n"
        "s_add_u32 m0, s70, 9216\n"
        "s_nop 0\n"
        "buffer_load_dwordx4 v34, s[60:63], s72 offen nt lds\n"
        "s_add_u32 m0, s70, 10240\n"
        "s_nop 0\n"
        "buffer_load_dwordx4 v35, s[60:63], s72 offen nt lds\n"
        "s_add_u32 m0, s70, 11264\n"
        "s_nop 0\n"
        "buffer_load_dwordx4 v36, s[60:63], s72 offen nt lds\n"
        "s_add_u32 m0, s70, 12288\n"
        "s_nop 0\n"
        "buffer_load_dwordx4 v37, s[60:63], s72 offen nt lds\n"
        "s_add_u32 m0, s70, 13312\n"
        "s_nop 0\n"
        "buffer_load_dwordx4 v38, s[60:63], s72 offen nt lds\n"
        "s_add_u32 s71, s71, 57344\n"
        "s_add_u32 s72, s71, 28672\n"
        "s_add_u32 m0, s70, 14336\n"
        "s_nop 0\n"
        "buffer_load_dwordx4 v32, s[60:63], s71 offen nt lds\n"
        "s_add_u32 m0, s70, 15360\n"
        "s_nop 0\n"
        "buffer_load_dwordx4 v33, s[60:63], s71 offen nt lds\n"
        "s_add_u32 m0, s70, 16384\n"
        "s_nop 0\n"
        "buffer_load_dwordx4 v34, s[60:63], s71 offen nt lds\n"
        "s_add_u32 m0, s70, 17408\n"
        "s_nop 0\n"
        "buffer_load_dwordx4 v35, s[60:63], s71 offen nt lds\n"
        "s_add_u32 m0, s70, 18432\n"
        "s_nop 0\n"
        "buffer_load_dwordx4 v36, s[60:63], s71 offen nt lds\n"
        "s_add_u32 m0, s70, 19456\n"
        "s_nop 0\n"
        "buffer_load_dwordx4 v37, s[60:63], s71 offen nt lds\n"
        "s_add_u32 m0, s70, 20480\n"
        "s_nop 0\n"
        "buffer_load_dwordx4 v38, s[60:63], s71 offen nt lds\n"
        "s_add_u32 m0, s70, 21504\n"
        "s_nop 0\n"
        "buffer_load_dwordx4 v32, s[60:63], s72 offen nt lds\n"
        "s_add_u32 m0, s70, 22528\n"
        "s_nop 0\n"
        "buffer_load_dwordx4 v33, s[60:63], s72 offen nt lds\n"
        "s_add_u32 m0, s70, 23552\n"
        "s_nop 0\n"
        "buffer_load_dwordx4 v34, s[60:63], s72 offen nt lds\n"
        "s_add_u32 m0, s70, 24576\n"
        "s_nop 0\n"
        "buffer_load_dwordx4 v35, s[60:63], s72 offen nt lds\n"
        "s_add_u32 m0, s70, 25600\n"
        "s_nop 0\n"
        "buffer_load_dwordx4 v36, s[60:63], s72 offen nt lds\n"
        "s_add_u32 m0, s70, 26624\n"
        "s_nop 0\n"
        "buffer_load_dwordx4 v37, s[60:63], s72 offen nt lds\n"
        "s_add_u32 m0, s70, 27648\n"
        "s_nop 0\n"
        "buffer_load_dwordx4 v38, s[60:63], s72 offen nt lds\n"
        "s_add_u32 s71, s71, 57344\n"
        "s_waitcnt vmcnt(14)\n"
        "ds_read_b64_tr_b16 a[0:1], v17 offset:0\n"
        "ds_read_b64_tr_b16 a[2:3], v17 offset:896\n"
        "ds_read_b64_tr_b16 a[4:5], v17 offset:32\n"
        "ds_read_b64_tr_b16 a[6:7], v17 offset:928\n"
        "ds_read_b64_tr_b16 a[8:9], v17 offset:64\n"
        "ds_read_b64_tr_b16 a[10:11], v17 offset:960\n"
        "ds_read_b64_tr_b16 a[12:13], v17 offset:96\n"
        "ds_read_b64_tr_b16 a[14:15], v17 offset:992\n"
        "ds_read_b64_tr_b16 a[16:17], v17 offset:128\n"
        "ds_read_b64_tr_b16 a[18:19], v17 offset:1024\n"
        "ds_read_b64_tr_b16 a[20:21], v17 offset:160\n"
        "ds_read_b64_tr_b16 a[22:23], v17 offset:1056\n"
        "ds_read_b64_tr_b16 a[24:25], v17 offset:192\n"
        "ds_read_b64_tr_b16 a[26:27], v17 offset:1088\n"
        "ds_read_b64_tr_b16 a[28:29], v17 offset:7168\n"
        "ds_read_b64_tr_b16 a[30:31], v17 offset:8064\n"
        "ds_read_b64_tr_b16 a[32:33], v17 offset:7200\n"
        "ds_read_b64_tr_b16 a[34:35], v17 offset:8096\n"
        "ds_read_b64_tr_b16 a[36:37], v17 offset:7232\n"
        "ds_read_b64_tr_b16 a[38:39], v17 offset:8128\n"
        "ds_read_b64_tr_b16 a[40:41], v17 offset:7264\n"
        "ds_read_b64_tr_b16 a[42:43], v17 offset:8160\n"
        "ds_read_b64_tr_b16 a[44:45], v17 offset:7296\n"
        "ds_read_b64_tr_b16 a[46:47], v17 offset:8192\n"
        "ds_read_b64_tr_b16 a[48:49], v17 offset:7328\n"
        "ds_read_b64_tr_b16 a[50:51], v17 offset:8224\n"
        "ds_read_b64_tr_b16 a[52:53], v17 offset:7360\n"
        "ds_read_b64_tr_b16 a[54:55], v17 offset:8256\n"
        "s_waitcnt lgkmcnt(0)\n"
        "s_add_u32 s72, s71, 28672\n"
        "s_add_u32 m0, s70, 0\n"
        "s_nop 0\n"
        "buffer_load_dwordx4 v32, s[60:63], s71 offen nt lds\n"
        "s_add_u32 m0, s70, 1024\n"
        "s_nop 0\n"
        "buffer_load_dwordx4 v33, s[60:63], s71 offen nt lds\n"
        "s_add_u32 m0, s70, 2048\n"
        "s_nop 0\n"
        "buffer_load_dwordx4 v34, s[60:63], s71 offen nt lds\n"
        "s_add_u32 m0, s70, 3072\n"
        "s_nop 0\n"
        "buffer_load_dwordx4 v35, s[60:63], s71 offen nt lds\n"
        "s_add_u32 m0, s70, 4096\n"
        "s_nop 0\n"
        "buffer_load_dwordx4 v36, s[60:63], s71 offen nt lds\n"
        "s_add_u32 m0, s70, 5120\n"
        "s_nop 0\n"
        "buffer_load_dwordx4 v37, s[60:63], s71 offen nt lds\n"
        "s_add_u32 m0, s70, 6144\n"
        "s_nop 0\n"
        "buffer_load_dwordx4 v38, s[60:63], s71 offen nt lds\n"
        "s_add_u32 m0, s70, 7168\n"
        "s_nop 0\n"
        "buffer_load_dwordx4 v32, s[60:63], s72 offen nt lds\n"
        "s_add_u32 m0, s70, 8192\n"
        "s_nop 0\n"
        "buffer_load_dwordx4 v33, s[60:63], s72 offen nt lds\n"
        "s_add_u32 m0, s70, 9216\n"
        "s_nop 0\n"
        "buffer_load_dwordx4 v34, s[60:63], s72 offen nt lds\n"
        "s_add_u32 m0, s70, 10240\n"
        "s_nop 0\n"
        "buffer_load_dwordx4 v35, s[60:63], s72 offen nt lds\n"
        "s_add_u32 m0, s70, 11264\n"
        "s_nop 0\n"
        "buffer_load_dwordx4 v36, s[60:63], s72 offen nt lds\n"
        "s_add_u32 m0, s70, 12288\n"
        "s_nop 0\n"
        "buffer_load_dwordx4 v37, s[60:63], s72 offen nt lds\n"
        "s_add_u32 m0, s70, 13312\n"
        "s_nop 0\n"
        "buffer_load_dwordx4 v38, s[60:63], s72 offen nt lds\n"
        "s_add_u32 s71, s71, 57344\n"
        "s_waitcnt vmcnt(14)\n"
        "ds_read_b64_tr_b16 a[56:57], v17 offset:14336\n"
        "ds_read_b64_tr_b16 a[58:59], v17 offset:15232\n"
        "ds_read_b64_tr_b16 a[60:61], v17 offset:14368\n"
        "ds_read_b64_tr_b16 a[62:63], v17 offset:15264\n"
        "ds_read_b64_tr_b16 a[64:65], v17 offset:14400\n"
        "ds_read_b64_tr_b16 a[66:67], v17 offset:15296\n"
        "ds_read_b64_tr_b16 a[68:69], v17 offset:14432\n"
        "ds_read_b64_tr_b16 a[70:71], v17 offset:15328\n"
        "ds_read_b64_tr_b16 a[72:73], v17 offset:14464\n"
        "ds_read_b64_tr_b16 a[74:75], v17 offset:15360\n"
        "ds_read_b64_tr_b16 a[76:77], v17 offset:14496\n"
        "ds_read_b64_tr_b16 a[78:79], v17 offset:15392\n"
        "ds_read_b64_tr_b16 a[80:81], v17 offset:14528\n"
        "ds_read_b64_tr_b16 a[82:83], v17 offset:15424\n"
        "ds_read_b64_tr_b16 a[84:85], v17 offset:21504\n"
        "ds_read_b64_tr_b16 a[86:87], v17 offset:22400\n"
        "ds_read_b64_tr_b16 a[88:89], v17 offset:21536\n"
        "ds_read_b64_tr_b16 a[90:91], v17 offset:22432\n"
        "ds_read_b64_tr_b16 a[92:93], v17 offset:21568\n"
        "ds_read_b64_tr_b16 a[94:95], v17 offset:22464\n"
        "ds_read_b64_tr_b16 a[96:97], v17 offset:21600\n"
        "ds_read_b64_tr_b16 a[98:99], v17 offset:22496\n"
        "ds_read_b64_tr_b16 a[100:101], v17 offset:21632\n"
        "ds_read_b64_tr_b16 a[102:103], v17 offset:22528\n"
        "ds_read_b64_tr_b16 a[104:105], v17 offset:21664\n"
        "ds_read_b64_tr_b16 a[106:107], v17 offset:22560\n"
        "ds_read_b64_tr_b16 a[108:109], v17 offset:21696\n"
        "ds_read_b64_tr_b16 a[110:111], v17 offset:22592\n"
        "s_waitcnt lgkmcnt(0)\n"
        "s_add_u32 s72, s71, 28672\n"
        "s_add_u32 m0, s70, 14336\n"
        "s_nop 0\n"
        "buffer_load_dwordx4 v32, s[60:63], s71 offen nt lds\n"
        "s_add_u32 m0, s70, 15360\n"
        "s_nop 0\n"
        "buffer_load_dwordx4 v33, s[60:63], s71 offen nt lds\n"
        "s_add_u32 m0, s70, 16384\n"
        "s_nop 0\n"
        "buffer_load_dwordx4 v34, s[60:63], s71 offen nt lds\n"
        "s_add_u32 m0, s70, 17408\n"
        "s_nop 0\n"
        "buffer_load_dwordx4 v35, s[60:63], s71 offen nt lds\n"
        "s_add_u32 m0, s70, 18432\n"
        "s_nop 0\n"
        "buffer_load_dwordx4 v36, s[60:63], s71 offen nt lds\n"
        "s_add_u32 m0, s70, 19456\n"
        "s_nop 0\n"
        "buffer_load_dwordx4 v37, s[60:63], s71 offen nt lds\n"
        "s_add_u32 m0, s70, 20480\n"
        "s_nop 0\n"
        "buffer_load_dwordx4 v38, s[60:63], s71 offen nt lds\n"
        "s_add_u32 m0, s70, 21504\n"
        "s_nop 0\n"
        "buffer_load_dwordx4 v32, s[60:63], s72 offen nt lds\n"
        "s_add_u32 m0, s70, 22528\n"
        "s_nop 0\n"
        "buffer_load_dwordx4 v33, s[60:63], s72 offen nt lds\n"
        "s_add_u32 m0, s70, 23552\n"
        "s_nop 0\n"
        "buffer_load_dwordx4 v34, s[60:63], s72 offen nt lds\n"
        "s_add_u32 m0, s70, 24576\n"
        "s_nop 0\n"
        "buffer_load_dwordx4 v35, s[60:63], s72 offen nt lds\n"
        "s_add_u32 m0, s70, 25600\n"
        "s_nop 0\n"
        "buffer_load_dwordx4 v36, s[60:63], s72 offen nt lds\n"
        "s_add_u32 m0, s70, 26624\n"
        "s_nop 0\n"
        "buffer_load_dwordx4 v37, s[60:63], s72 offen nt lds\n"
        "s_add_u32 m0, s70, 27648\n"
        "s_nop 0\n"
        "buffer_load_dwordx4 v38, s[60:63], s72 offen nt lds\n"
        "s_add_u32 s71, s71, 57344\n"
        "s_waitcnt vmcnt(14)\n"
        "ds_read_b64_tr_b16 a[112:113], v17 offset:0\n"
        "ds_read_b64_tr_b16 a[114:115], v17 offset:896\n"
        "ds_read_b64_tr_b16 a[116:117], v17 offset:32\n"
        "ds_read_b64_tr_b16 a[118:119], v17 offset:928\n"
        "ds_read_b64_tr_b16 a[120:121], v17 offset:64\n"
        "ds_read_b64_tr_b16 a[122:123], v17 offset:960\n"
        "ds_read_b64_tr_b16 a[124:125], v17 offset:96\n"
        "ds_read_b64_tr_b16 a[126:127], v17 offset:992\n"
        "ds_read_b64_tr_b16 a[128:129], v17 offset:128\n"
        "ds_read_b64_tr_b16 a[130:131], v17 offset:1024\n"
        "ds_read_b64_tr_b16 a[132:133], v17 offset:160\n"
        "ds_read_b64_tr_b16 a[134:135], v17 offset:1056\n"
        "ds_read_b64_tr_b16 a[136:137], v17 offset:192\n"
        "ds_read_b64_tr_b16 a[138:139], v17 offset:1088\n"
        "ds_read_b64_tr_b16 a[140:141], v17 offset:7168\n"
        "ds_read_b64_tr_b16 a[142:143], v17 offset:8064\n"
        "ds_read_b64_tr_b16 a[144:145], v17 offset:7200\n"
        "ds_read_b64_tr_b16 a[146:147], v17 offset:8096\n"
        "ds_read_b64_tr_b16 a[148:149], v17 offset:7232\n"
        "ds_read_b64_tr_b16 a[150:151], v17 offset:8128\n"
        "ds_read_b64_tr_b16 a[152:153], v17 offset:7264\n"
        "ds_read_b64_tr_b16 a[154:155], v17 offset:8160\n"
        "ds_read_b64_tr_b16 a[156:157], v17 offset:7296\n"
        "ds_read_b64_tr_b16 a[158:159], v17 offset:8192\n"
        "ds_read_b64_tr_b16 a[160:161], v17 offset:7328\n"
        "ds_read_b64_tr_b16 a[162:163], v17 offset:8224\n"
        "ds_read_b64_tr_b16 a[164:165], v17 offset:7360\n"
        "ds_read_b64_tr_b16 a[166:167], v17 offset:8256\n"
        "s_waitcnt lgkmcnt(0)\n"
        "s_add_u32 s72, s71, 28672\n"
        "s_add_u32 m0, s70, 0\n"
        "s_nop 0\n"
        "buffer_load_dwordx4 v32, s[60:63], s71 offen nt lds\n"
        "s_add_u32 m0, s70, 1024\n"
        "s_nop 0\n"
        "buffer_load_dwordx4 v33, s[60:63], s71 offen nt lds\n"
        "s_add_u32 m0, s70, 2048\n"
        "s_nop 0\n"
        "buffer_load_dwordx4 v34, s[60:63], s71 offen nt lds\n"
        "s_add_u32 m0, s70, 3072\n"
        "s_nop 0\n"
        "buffer_load_dwordx4 v35, s[60:63], s71 offen nt lds\n"
        "s_add_u32 m0, s70, 4096\n"
        "s_nop 0\n"
        "buffer_load_dwordx4 v36, s[60:63], s71 offen nt lds\n"
        "s_add_u32 m0, s70, 5120\n"
        "s_nop 0\n"
        "buffer_load_dwordx4 v37, s[60:63], s71 offen nt lds\n"
        "s_add_u32 m0, s70, 6144\n"
        "s_nop 0\n"
        "buffer_load_dwordx4 v38, s[60:63], s71 offen nt lds\n"
        "s_add_u32 m0, s70, 7168\n"
        "s_nop 0\n"
        "buffer_load_dwordx4 v32, s[60:63], s72 offen nt lds\n"
        "s_add_u32 m0, s70, 8192\n"
        "s_nop 0\n"
        "buffer_load_dwordx4 v33, s[60:63], s72 offen nt lds\n"
        "s_add_u32 m0, s70, 9216\n"
        "s_nop 0\n"
        "buffer_load_dwordx4 v34, s[60:63], s72 offen nt lds\n"
        "s_add_u32 m0, s70, 10240\n"
        "s_nop 0\n"
        "buffer_load_dwordx4 v35, s[60:63], s72 offen nt lds\n"
        "s_add_u32 m0, s70, 11264\n"
        "s_nop 0\n"
        "buffer_load_dwordx4 v36, s[60:63], s72 offen nt lds\n"
        "s_add_u32 m0, s70, 12288\n"
        "s_nop 0\n"
        "buffer_load_dwordx4 v37, s[60:63], s72 offen nt lds\n"
        "s_add_u32 m0, s70, 13312\n"
        "s_nop 0\n"
        "buffer_load_dwordx4 v38, s[60:63], s72 offen nt lds\n"
        "s_add_u32 s71, s71, 57344\n"
        "s_waitcnt vmcnt(14)\n"
        "ds_read_b64_tr_b16 a[168:169], v17 offset:14336\n"
        "ds_read_b64_tr_b16 a[170:171], v17 offset:15232\n"
        "ds_read_b64_tr_b16 a[172:173], v17 offset:14368\n"
        "ds_read_b64_tr_b16 a[174:175], v17 offset:15264\n"
        "ds_read_b64_tr_b16 a[176:177], v17 offset:14400\n"
        "ds_read_b64_tr_b16 a[178:179], v17 offset:15296\n"
        "ds_read_b64_tr_b16 a[180:181], v17 offset:14432\n"
        "ds_read_b64_tr_b16 a[182:183], v17 offset:15328\n"
        "ds_read_b64_tr_b16 a[184:185], v17 offset:14464\n"
        "ds_read_b64_tr_b16 a[186:187], v17 offset:15360\n"
        "ds_read_b64_tr_b16 a[188:189], v17 offset:14496\n"
        "ds_read_b64_tr_b16 a[190:191], v17 offset:15392\n"
        "ds_read_b64_tr_b16 a[192:193], v17 offset:14528\n"
        "ds_read_b64_tr_b16 a[194:195], v17 offset:15424\n"
        "ds_read_b64_tr_b16 a[196:197], v17 offset:21504\n"
        "ds_read_b64_tr_b16 a[198:199], v17 offset:22400\n"
        "ds_read_b64_tr_b16 a[200:201], v17 offset:21536\n"
        "ds_read_b64_tr_b16 a[202:203], v17 offset:22432\n"
        "ds_read_b64_tr_b16 a[204:205], v17 offset:21568\n"
        "ds_read_b64_tr_b16 a[206:207], v17 offset:22464\n"
        "ds_read_b64_tr_b16 a[208:209], v17 offset:21600\n"
        "ds_read_b64_tr_b16 a[210:211], v17 offset:22496\n"
        "ds_read_b64_tr_b16 a[212:213], v17 offset:21632\n"
        "ds_read_b64_tr_b16 a[214:215], v17 offset:22528\n"
        "ds_read_b64_tr_b16 a[216:217], v17 offset:21664\n"
        "ds_read_b64_tr_b16 a[218:219], v17 offset:22560\n"
        "ds_read_b64_tr_b16 a[220:221], v17 offset:21696\n"
        "ds_read_b64_tr_b16 a[222:223], v17 offset:22592\n"
        "s_waitcnt lgkmcnt(0)\n"
        "s_add_u32 s72, s71, 28672\n"
        "s_add_u32 m0, s70, 14336\n"
        "s_nop 0\n"
        "buffer_load_dwordx4 v32, s[60:63], s71 offen nt lds\n"
        "s_add_u32 m0, s70, 15360\n"
        "s_nop 0\n"
        "buffer_load_dwordx4 v33, s[60:63], s71 offen nt lds\n"
        "s_add_u32 m0, s70, 16384\n"
        "s_nop 0\n"
        "buffer_load_dwordx4 v34, s[60:63], s71 offen nt lds\n"
        "s_add_u32 m0, s70, 17408\n"
        "s_nop 0\n"
        "buffer_load_dwordx4 v35, s[60:63], s71 offen nt lds\n"
        "s_add_u32 m0, s70, 18432\n"
        "s_nop 0\n"
        "buffer_load_dwordx4 v36, s[60:63], s71 offen nt lds\n"
        "s_add_u32 m0, s70, 19456\n"
        "s_nop 0\n"
        "buffer_load_dwordx4 v37, s[60:63], s71 offen nt lds\n"
        "s_add_u32 m0, s70, 20480\n"
        "s_nop 0\n"
        "buffer_load_dwordx4 v38, s[60:63], s71 offen nt lds\n"
        "s_add_u32 m0, s70, 21504\n"
        "s_nop 0\n"
        "buffer_load_dwordx4 v32, s[60:63], s72 offen nt lds\n"
        "s_add_u32 m0, s70, 22528\n"
        "s_nop 0\n"
        "buffer_load_dwordx4 v33, s[60:63], s72 offen nt lds\n"
        "s_add_u32 m0, s70, 23552\n"
        "s_nop 0\n"
        "buffer_load_dwordx4 v34, s[60:63], s72 offen nt lds\n"
        "s_add_u32 m0, s70, 24576\n"
        "s_nop 0\n"
        "buffer_load_dwordx4 v35, s[60:63], s72 offen nt lds\n"
        "s_add_u32 m0, s70, 25600\n"
        "s_nop 0\n"
        "buffer_load_dwordx4 v36, s[60:63], s72 offen nt lds\n"
        "s_add_u32 m0, s70, 26624\n"
        "s_nop 0\n"
        "buffer_load_dwordx4 v37, s[60:63], s72 offen nt lds\n"
        "s_add_u32 m0, s70, 27648\n"
        "s_nop 0\n"
        "buffer_load_dwordx4 v38, s[60:63], s72 offen nt lds\n"
        "s_add_u32 s71, s71, 57344\n"
        "s_waitcnt vmcnt(14)\n"
        "ds_read_b64_tr_b16 a[224:225], v17 offset:0\n"
        "ds_read_b64_tr_b16 a[226:227], v17 offset:896\n"
        "ds_read_b64_tr_b16 a[228:229], v17 offset:32\n"
        "ds_read_b64_tr_b16 a[230:231], v17 offset:928\n"
        "ds_read_b64_tr_b16 a[232:233], v17 offset:64\n"
        "ds_read_b64_tr_b16 a[234:235], v17 offset:960\n"
        "ds_read_b64_tr_b16 a[236:237], v17 offset:96\n"
        "ds_read_b64_tr_b16 a[238:239], v17 offset:992\n"
        "ds_read_b64_tr_b16 a[240:241], v17 offset:128\n"
        "ds_read_b64_tr_b16 a[242:243], v17 offset:1024\n"
        "ds_read_b64_tr_b16 a[244:245], v17 offset:160\n"
        "ds_read_b64_tr_b16 a[246:247], v17 offset:1056\n"
        "ds_read_b64_tr_b16 a[248:249], v17 offset:192\n"
        "ds_read_b64_tr_b16 a[250:251], v17 offset:1088\n"
        "ds_read_b64_tr_b16 a[252:253], v17 offset:7168\n"
        "ds_read_b64_tr_b16 a[254:255], v17 offset:8064\n"
        "ds_read_b64_tr_b16 v[120:121], v17 offset:7200\n"
        "ds_read_b64_tr_b16 v[122:123], v17 offset:8096\n"
        "ds_read_b64_tr_b16 v[124:125], v17 offset:7232\n"
        "ds_read_b64_tr_b16 v[126:127], v17 offset:8128\n"
        "ds_read_b64_tr_b16 v[128:129], v17 offset:7264\n"
        "ds_read_b64_tr_b16 v[130:131], v17 offset:8160\n"
        "ds_read_b64_tr_b16 v[132:133], v17 offset:7296\n"
        "ds_read_b64_tr_b16 v[134:135], v17 offset:8192\n"
        "ds_read_b64_tr_b16 v[136:137], v17 offset:7328\n"
        "ds_read_b64_tr_b16 v[138:139], v17 offset:8224\n"
        "ds_read_b64_tr_b16 v[140:141], v17 offset:7360\n"
        "ds_read_b64_tr_b16 v[142:143], v17 offset:8256\n"
        "s_waitcnt lgkmcnt(0)\n"
        "s_add_u32 s72, s71, 28672\n"
        "s_add_u32 m0, s70, 0\n"
        "s_nop 0\n"
        "buffer_load_dwordx4 v32, s[60:63], s71 offen nt lds\n"
        "s_add_u32 m0, s70, 1024\n"
        "s_nop 0\n"
        "buffer_load_dwordx4 v33, s[60:63], s71 offen nt lds\n"
        "s_add_u32 m0, s70, 2048\n"
        "s_nop 0\n"
        "buffer_load_dwordx4 v34, s[60:63], s71 offen nt lds\n"
        "s_add_u32 m0, s70, 3072\n"
        "s_nop 0\n"
        "buffer_load_dwordx4 v35, s[60:63], s71 offen nt lds\n"
        "s_add_u32 m0, s70, 4096\n"
        "s_nop 0\n"
        "buffer_load_dwordx4 v36, s[60:63], s71 offen nt lds\n"
        "s_add_u32 m0, s70, 5120\n"
        "s_nop 0\n"
        "buffer_load_dwordx4 v37, s[60:63], s71 offen nt lds\n"
        "s_add_u32 m0, s70, 6144\n"
        "s_nop 0\n"
        "buffer_load_dwordx4 v38, s[60:63], s71 offen nt lds\n"
        "s_add_u32 m0, s70, 7168\n"
        "s_nop 0\n"
        "buffer_load_dwordx4 v32, s[60:63], s72 offen nt lds\n"
        "s_add_u32 m0, s70, 8192\n"
        "s_nop 0\n"
        "buffer_load_dwordx4 v33, s[60:63], s72 offen nt lds\n"
        "s_add_u32 m0, s70, 9216\n"
        "s_nop 0\n"
        "buffer_load_dwordx4 v34, s[60:63], s72 offen nt lds\n"
        "s_add_u32 m0, s70, 10240\n"
        "s_nop 0\n"
        "buffer_load_dwordx4 v35, s[60:63], s72 offen nt lds\n"
        "s_add_u32 m0, s70, 11264\n"
        "s_nop 0\n"
        "buffer_load_dwordx4 v36, s[60:63], s72 offen nt lds\n"
        "s_add_u32 m0, s70, 12288\n"
        "s_nop 0\n"
        "buffer_load_dwordx4 v37, s[60:63], s72 offen nt lds\n"
        "s_add_u32 m0, s70, 13312\n"
        "s_nop 0\n"
        "buffer_load_dwordx4 v38, s[60:63], s72 offen nt lds\n"
        "s_add_u32 s71, s71, 57344\n"
        "s_waitcnt vmcnt(14)\n"
        "ds_read_b64_tr_b16 v[144:145], v17 offset:14336\n"
        "ds_read_b64_tr_b16 v[146:147], v17 offset:15232\n"
        "ds_read_b64_tr_b16 v[148:149], v17 offset:14368\n"
        "ds_read_b64_tr_b16 v[150:151], v17 offset:15264\n"
        "ds_read_b64_tr_b16 v[152:153], v17 offset:14400\n"
        "ds_read_b64_tr_b16 v[154:155], v17 offset:15296\n"
        "ds_read_b64_tr_b16 v[156:157], v17 offset:14432\n"
        "ds_read_b64_tr_b16 v[158:159], v17 offset:15328\n"
        "ds_read_b64_tr_b16 v[160:161], v17 offset:14464\n"
        "ds_read_b64_tr_b16 v[162:163], v17 offset:15360\n"
        "ds_read_b64_tr_b16 v[164:165], v17 offset:14496\n"
        "ds_read_b64_tr_b16 v[166:167], v17 offset:15392\n"
        "ds_read_b64_tr_b16 v[168:169], v17 offset:14528\n"
        "ds_read_b64_tr_b16 v[170:171], v17 offset:15424\n"
        "ds_read_b64_tr_b16 v[172:173], v17 offset:21504\n"
        "ds_read_b64_tr_b16 v[174:175], v17 offset:22400\n"
        "ds_read_b64_tr_b16 v[176:177], v17 offset:21536\n"
        "ds_read_b64_tr_b16 v[178:179], v17 offset:22432\n"
        "ds_read_b64_tr_b16 v[180:181], v17 offset:21568\n"
        "ds_read_b64_tr_b16 v[182:183], v17 offset:22464\n"
        "ds_read_b64_tr_b16 v[184:185], v17 offset:21600\n"
        "ds_read_b64_tr_b16 v[186:187], v17 offset:22496\n"
        "ds_read_b64_tr_b16 v[188:189], v17 offset:21632\n"
        "ds_read_b64_tr_b16 v[190:191], v17 offset:22528\n"
        "ds_read_b64_tr_b16 v[192:193], v17 offset:21664\n"
        "ds_read_b64_tr_b16 v[194:195], v17 offset:22560\n"
        "ds_read_b64_tr_b16 v[196:197], v17 offset:21696\n"
        "ds_read_b64_tr_b16 v[198:199], v17 offset:22592\n"
        "s_waitcnt lgkmcnt(0)\n"
        "s_waitcnt vmcnt(0)\n"
        "ds_read_b64_tr_b16 v[200:201], v17 offset:0\n"
        "ds_read_b64_tr_b16 v[202:203], v17 offset:896\n"
        "ds_read_b64_tr_b16 v[204:205], v17 offset:32\n"
        "ds_read_b64_tr_b16 v[206:207], v17 offset:928\n"
        "ds_read_b64_tr_b16 v[208:209], v17 offset:64\n"
        "ds_read_b64_tr_b16 v[210:211], v17 offset:960\n"
        "ds_read_b64_tr_b16 v[212:213], v17 offset:96\n"
        "ds_read_b64_tr_b16 v[214:215], v17 offset:992\n"
        "ds_read_b64_tr_b16 v[216:217], v17 offset:128\n"
        "ds_read_b64_tr_b16 v[218:219], v17 offset:1024\n"
        "ds_read_b64_tr_b16 v[220:221], v17 offset:160\n"
        "ds_read_b64_tr_b16 v[222:223], v17 offset:1056\n"
        "ds_read_b64_tr_b16 v[224:225], v17 offset:192\n"
        "ds_read_b64_tr_b16 v[226:227], v17 offset:1088\n"
        "ds_read_b64_tr_b16 v[228:229], v17 offset:7168\n"
        "ds_read_b64_tr_b16 v[230:231], v17 offset:8064\n"
        "ds_read_b64_tr_b16 v[232:233], v17 offset:7200\n"
        "ds_read_b64_tr_b16 v[234:235], v17 offset:8096\n"
        "ds_read_b64_tr_b16 v[236:237], v17 offset:7232\n"
        "ds_read_b64_tr_b16 v[238:239], v17 offset:8128\n"
        "ds_read_b64_tr_b16 v[240:241], v17 offset:7264\n"
        "ds_read_b64_tr_b16 v[242:243], v17 offset:8160\n"
        "ds_read_b64_tr_b16 v[244:245], v17 offset:7296\n"
        "ds_read_b64_tr_b16 v[246:247], v17 offset:8192\n"
        "ds_read_b64_tr_b16 v[248:249], v17 offset:7328\n"
        "ds_read_b64_tr_b16 v[250:251], v17 offset:8224\n"
        "ds_read_b64_tr_b16 v[252:253], v17 offset:7360\n"
        "ds_read_b64_tr_b16 v[254:255], v17 offset:8256\n"
        "s_waitcnt lgkmcnt(0)\n"
        "s_barrier\n"
        "s_lshl_b32 s58, s44, 9\n"
        ".Lw3_p0_%=:\n"
        "buffer_load_dword v116, v19, s[40:43], s58 offen sc1\n"
        "s_cmp_eq_u32 s55, 0\n"
        "s_cbranch_scc1 .Lw3_p0k_%=\n"
        "s_sub_u32 s55, s55, 1\n"
        "s_waitcnt vmcnt(0)\n"
        "v_cmp_eq_u32 vcc, 0, v116\n"
        "s_cbranch_vccz .Lw3_p0k_%=\n"
        "s_sleep 16\n"
        "s_branch .Lw3_p0_%=\n"
        ".Lw3_p0k_%=:\n"
        "s_mul_i32 s57, s44, 114688\n"
        "s_add_u32 s61, s57, s46\n"
        "s_add_u32 s60, s61, 0\n"
        "s_add_u32 s62, s61, 8192\n"
        "s_add_u32 s63, s61, 12288\n"
        "s_add_u32 s61, s61, 4096\n"
        "buffer_load_dwordx4 v[60:63], v16, s[40:43], s60 offen offset:0 sc1\n"
        "buffer_load_dwordx4 v[64:67], v16, s[40:43], s60 offen offset:1024 sc1\n"
        "buffer_load_dwordx4 v[68:71], v16, s[40:43], s60 offen offset:2048 sc1\n"
        "buffer_load_dwordx4 v[72:75], v16, s[40:43], s60 offen offset:3072 sc1\n"
        "buffer_load_dwordx4 v[76:79], v16, s[40:43], s61 offen offset:0 sc1\n"
        "buffer_load_dwordx4 v[80:83], v16, s[40:43], s61 offen offset:1024 sc1\n"
        "buffer_load_dwordx4 v[84:87], v16, s[40:43], s61 offen offset:2048 sc1\n"
        "buffer_load_dwordx4 v[88:91], v16, s[40:43], s61 offen offset:3072 sc1\n"
        "buffer_load_dwordx4 v[92:95], v16, s[40:43], s62 offen offset:0 sc1\n"
        "buffer_load_dwordx4 v[96:99], v16, s[40:43], s62 offen offset:1024 sc1\n"
        "buffer_load_dwordx4 v[100:103], v16, s[40:43], s62 offen offset:2048 sc1\n"
        "buffer_load_dwordx4 v[104:107], v16, s[40:43], s62 offen offset:3072 sc1\n"
        "buffer_load_dwordx4 v[108:111], v16, s[40:43], s63 offen offset:0 sc1\n"
        "buffer_load_dwordx4 v[112:115], v16, s[40:43], s63 offen offset:1024 sc1\n"
        ".Lw3_loop_%=:\n"
        "s_add_u32 s47, s44, 8\n"
        "s_min_u32 s48, s47, 63\n"
        "s_lshl_b32 s54, s44, 9\n"
        "s_mul_i32 s50, s44, 57344\n"
        "s_add_u32 s50, s50, %8\n"
        "s_lshl_b32 s58, s48, 9\n"
        "buffer_load_dword v116, v19, s[40:43], s58 offen sc1\n"
        "s_mul_i32 s57, s44, 114688\n"
        "s_add_u32 s61, s57, s46\n"
        "s_add_u32 s60, s61, 57344\n"
        "s_add_u32 s62, s61, 65536\n"
        "s_add_u32 s63, s61, 69632\n"
        "s_add_u32 s61, s61, 61440\n"
        "s_waitcnt vmcnt(13)\n"
        "v_mfma_f32_16x16x32_bf16 v[32:35], a[0:3], v[60:63], 0\n"
        "v_mfma_f32_16x16x32_bf16 v[36:39], a[4:7], v[60:63], 0\n"
        "v_mfma_f32_16x16x32_bf16 v[40:43], a[8:11], v[60:63], 0\n"
        "v_mfma_f32_16x16x32_bf16 v[44:47], a[12:15], v[60:63], 0\n"
        "v_mfma_f32_16x16x32_bf16 v[48:51], a[16:19], v[60:63], 0\n"
        "v_mfma_f32_16x16x32_bf16 v[52:55], a[20:23], v[60:63], 0\n"
        "v_mfma_f32_16x16x32_bf16 v[56:59], a[24:27], v[60:63], 0\n"
        "buffer_load_dwordx4 v[60:63], v16, s[40:43], s60 offen offset:0 sc1\n"
        "s_waitcnt vmcnt(13)\n"
        "v_mfma_f32_16x16x32_bf16 v[32:35], a[28:31], v[64:67], v[32:35]\n"
        "v_mfma_f32_16x16x32_bf16 v[36:39], a[32:35], v[64:67], v[36:39]\n"
        "v_mfma_f32_16x16x32_bf16 v[40:43], a[36:39], v[64:67], v[40:43]\n"
        "v_mfma_f32_16x16x32_bf16 v[44:47], a[40:43], v[64:67], v[44:47]\n"
        "v_mfma_f32_16x16x32_bf16 v[48:51], a[44:47], v[64:67], v[48:51]\n"
        "v_mfma_f32_16x16x32_bf16 v[52:55], a[48:51], v[64:67], v[52:55]\n"
        "v_mfma_f32_16x16x32_bf16 v[56:59], a[52:55], v[64:67], v[56:59]\n"
        "buffer_load_dwordx4 v[64:67], v16, s[40:43], s60 offen offset:1024 sc1\n"
        "s_waitcnt vmcnt(13)\n"
        "v_mfma_f32_16x16x32_bf16 v[32:35], a[56:59], v[68:71], v[32:35]\n"
        "v_mfma_f32_16x16x32_bf16 v[36:39], a[60:63], v[68:71], v[36:39]\n"
        "v_mfma_f32_16x16x32_bf16 v[40:43], a[64:67], v[68:71], v[40:43]\n"
        "v_mfma_f32_16x16x32_bf16 v[44:47], a[68:71], v[68:71], v[44:47]\n"
        "v_mfma_f32_16x16x32_bf16 v[48:51], a[72:75], v[68:71], v[48:51]\n"
        "v_mfma_f32_16x16x32_bf16 v[52:55], a[76:79], v[68:71], v[52:55]\n"
        "v_mfma_f32_16x16x32_bf16 v[56:59], a[80:83], v[68:71], v[56:59]\n"
        "buffer_load_dwordx4 v[68:71], v16, s[40:43], s60 offen offset:2048 sc1\n"
        "s_waitcnt vmcnt(13)\n"
        "v_mfma_f32_16x16x32_bf16 v[32:35], a[84:87], v[72:75], v[32:35]\n"
        "v_mfma_f32_16x16x32_bf16 v[36:39], a[88:91], v[72:75], v[36:39]\n"
        "v_mfma_f32_16x16x32_bf16 v[40:43], a[92:95], v[72:75], v[40:43]\n"
        "v_mfma_f32_16x16x32_bf16 v[44:47], a[96:99], v[72:75], v[44:47]\n"
        "v_mfma_f32_16x16x32_bf16 v[48:51], a[100:103], v[72:75], v[48:51]\n"
        "v_mfma_f32_16x16x32_bf16 v[52:55], a[104:107], v[72:75], v[52:55]\n"
        "v_mfma_f32_16x16x32_bf16 v[56:59], a[108:111], v[72:75], v[56:59]\n"
        "buffer_load_dwordx4 v[72:75], v16, s[40:43], s60 offen offset:3072 sc1\n"
        "s_waitcnt vmcnt(13)\n"
        "v_mfma_f32_16x16x32_bf16 v[32:35], a[112:115], v[76:79], v[32:35]\n"
        "v_mfma_f32_16x16x32_bf16 v[36:39], a[116:119], v[76:79], v[36:39]\n"
        "v_mfma_f32_16x16x32_bf16 v[40:43], a[120:123], v[76:79], v[40:43]\n"
        "v_mfma_f32_16x16x32_bf16 v[44:47], a[124:127], v[76:79], v[44:47]\n"
        "v_mfma_f32_16x16x32_bf16 v[48:51], a[128:131], v[76:79], v[48:51]\n"
        "v_mfma_f32_16x16x32_bf16 v[52:55], a[132:135], v[76:79], v[52:55]\n"
        "v_mfma_f32_16x16x32_bf16 v[56:59], a[136:139], v[76:79], v[56:59]\n"
        "buffer_load_dwordx4 v[76:79], v16, s[40:43], s61 offen offset:0 sc1\n"
        "s_waitcnt vmcnt(13)\n"
        "v_mfma_f32_16x16x32_bf16 v[32:35], a[140:143], v[80:83], v[32:35]\n"
        "v_mfma_f32_16x16x32_bf16 v[36:39], a[144:147], v[80:83], v[36:39]\n"
        "v_mfma_f32_16x16x32_bf16 v[40:43], a[148:151], v[80:83], v[40:43]\n"
        "v_mfma_f32_16x16x32_bf16 v[44:47], a[152:155], v[80:83], v[44:47]\n"
        "v_mfma_f32_16x16x32_bf16 v[48:51], a[156:159], v[80:83], v[48:51]\n"
        "v_mfma_f32_16x16x32_bf16 v[52:55], a[160:163], v[80:83], v[52:55]\n"
        "v_mfma_f32_16x16x32_bf16 v[56:59], a[164:167], v[80:83], v[56:59]\n"
        "buffer_load_dwordx4 v[80:83], v16, s[40:43], s61 offen offset:1024 sc1\n"
        "s_waitcnt vmcnt(13)\n"
        "v_mfma_f32_16x16x32_bf16 v[32:35], a[168:171], v[84:87], v[32:35]\n"
        "v_mfma_f32_16x16x32_bf16 v[36:39], a[172:175], v[84:87], v[36:39]\n"
        "v_mfma_f32_16x16x32_bf16 v[40:43], a[176:179], v[84:87], v[40:43]\n"
        "v_mfma_f32_16x16x32_bf16 v[44:47], a[180:183], v[84:87], v[44:47]\n"
        "v_mfma_f32_16x16x32_bf16 v[48:51], a[184:187], v[84:87], v[48:51]\n"
        "v_mfma_f32_16x16x32_bf16 v[52:55], a[188:191], v[84:87], v[52:55]\n"
        "v_mfma_f32_16x16x32_bf16 v[56:59], a[192:195], v[84:87], v[56:59]\n"
        "buffer_load_dwordx4 v[84:87], v16, s[40:43], s61 offen offset:2048 sc1\n"
        "s_waitcnt vmcnt(13)\n"
        "v_mfma_f32_16x16x32_bf16 v[32:35], a[196:199], v[88:91], v[32:35]\n"
        "v_mfma_f32_16x16x32_bf16 v[36:39], a[200:203], v[88:91], v[36:39]\n"
        "v_mfma_f32_16x16x32_bf16 v[40:43], a[204:207], v[88:91], v[40:43]\n"
        "v_mfma_f32_16x16x32_bf16 v[44:47], a[208:211], v[88:91], v[44:47]\n"
        "v_mfma_f32_16x16x32_bf16 v[48:51], a[212:215], v[88:91], v[48:51]\n"
        "v_mfma_f32_16x16x32_bf16 v[52:55], a[216:219], v[88:91], v[52:55]\n"
        "v_mfma_f32_16x16x32_bf16 v[56:59], a[220:223], v[88:91], v[56:59]\n"
        "buffer_load_dwordx4 v[88:91], v16, s[40:43], s61 offen offset:3072 sc1\n"
        "s_waitcnt vmcnt(13)\n"
        "v_mfma_f32_16x16x32_bf16 v[32:35], a[224:227], v[92:95], v[32:35]\n"
        "v_mfma_f32_16x16x32_bf16 v[36:39], a[228:231], v[92:95], v[36:39]\n"
        "v_mfma_f32_16x16x32_bf16 v[40:43], a[232:235], v[92:95], v[40:43]\n"
        "v_mfma_f32_16x16x32_bf16 v[44:47], a[236:239], v[92:95], v[44:47]\n"
        "v_mfma_f32_16x16x32_bf16 v[48:51], a[240:243], v[92:95], v[48:51]\n"
        "v_mfma_f32_16x16x32_bf16 v[52:55], a[244:247], v[92:95], v[52:55]\n"
        "v_mfma_f32_16x16x32_bf16 v[56:59], a[248:251], v[92:95], v[56:59]\n"
        "buffer_load_dwordx4 v[92:95], v16, s[40:43], s62 offen offset:0 sc1\n"
        "s_waitcnt vmcnt(13)\n"
        "v_mfma_f32_16x16x32_bf16 v[32:35], a[252:255], v[96:99], v[32:35]\n"
        "v_mfma_f32_16x16x32_bf16 v[36:39], v[120:123], v[96:99], v[36:39]\n"
        "v_mfma_f32_16x16x32_bf16 v[40:43], v[124:127], v[96:99], v[40:43]\n"
        "v_mfma_f32_16x16x32_bf16 v[44:47], v[128:131], v[96:99], v[44:47]\n"
        "v_mfma_f32_16x16x32_bf16 v[48:51], v[132:135], v[96:99], v[48:51]\n"
        "v_mfma_f32_16x16x32_bf16 v[52:55], v[136:139], v[96:99], v[52:55]\n"
        "v_mfma_f32_16x16x32_bf16 v[56:59], v[140:143], v[96:99], v[56:59]\n"
        "buffer_load_dwordx4 v[96:99], v16, s[40:43], s62 offen offset:1024 sc1\n"
        "s_waitcnt vmcnt(13)\n"
        "v_mfma_f32_16x16x32_bf16 v[32:35], v[144:147], v[100:103], v[32:35]\n"
        "v_mfma_f32_16x16x32_bf16 v[36:39], v[148:151], v[100:103], v[36:39]\n"
        "v_mfma_f32_16x16x32_bf16 v[40:43], v[152:155], v[100:103], v[40:43]\n"
        "v_mfma_f32_16x16x32_bf16 v[44:47], v[156:159], v[100:103], v[44:47]\n"
        "v_mfma_f32_16x16x32_bf16 v[48:51], v[160:163], v[100:103], v[48:51]\n"
        "v_mfma_f32_16x16x32_bf16 v[52:55], v[164:167], v[100:103], v[52:55]\n"
        "v_mfma_f32_16x16x32_bf16 v[56:59], v[168:171], v[100:103], v[56:59]\n"
        "buffer_load_dwordx4 v[100:103], v16, s[40:43], s62 offen offset:2048 sc1\n"
        "s_waitcnt vmcnt(13)\n"
        "v_mfma_f32_16x16x32_bf16 v[32:35], v[172:175], v[104:107], v[32:35]\n"
        "v_mfma_f32_16x16x32_bf16 v[36:39], v[176:179], v[104:107], v[36:39]\n"
        "v_mfma_f32_16x16x32_bf16 v[40:43], v[180:183], v[104:107], v[40:43]\n"
        "v_mfma_f32_16x16x32_bf16 v[44:47], v[184:187], v[104:107], v[44:47]\n"
        "v_mfma_f32_16x16x32_bf16 v[48:51], v[188:191], v[104:107], v[48:51]\n"
        "v_mfma_f32_16x16x32_bf16 v[52:55], v[192:195], v[104:107], v[52:55]\n"
        "v_mfma_f32_16x16x32_bf16 v[56:59], v[196:199], v[104:107], v[56:59]\n"
        "buffer_load_dwordx4 v[104:107], v16, s[40:43], s62 offen offset:3072 sc1\n"
        "s_waitcnt vmcnt(13)\n"
        "v_mfma_f32_16x16x32_bf16 v[32:35], v[200:203], v[108:111], v[32:35]\n"
        "v_mfma_f32_16x16x32_bf16 v[36:39], v[204:207], v[108:111], v[36:39]\n"
        "v_mfma_f32_16x16x32_bf16 v[40:43], v[208:211], v[108:111], v[40:43]\n"
        "v_mfma_f32_16x16x32_bf16 v[44:47], v[212:215], v[108:111], v[44:47]\n"
        "v_mfma_f32_16x16x32_bf16 v[48:51], v[216:219], v[108:111], v[48:51]\n"
        "v_mfma_f32_16x16x32_bf16 v[52:55], v[220:223], v[108:111], v[52:55]\n"
        "v_mfma_f32_16x16x32_bf16 v[56:59], v[224:227], v[108:111], v[56:59]\n"
        "buffer_load_dwordx4 v[108:111], v16, s[40:43], s63 offen offset:0 sc1\n"
        "s_waitcnt vmcnt(13)\n"
        "v_mfma_f32_16x16x32_bf16 v[32:35], v[228:231], v[112:115], v[32:35]\n"
        "v_mfma_f32_16x16x32_bf16 v[36:39], v[232:235], v[112:115], v[36:39]\n"
        "v_mfma_f32_16x16x32_bf16 v[40:43], v[236:239], v[112:115], v[40:43]\n"
        "v_mfma_f32_16x16x32_bf16 v[44:47], v[240:243], v[112:115], v[44:47]\n"
        "v_mfma_f32_16x16x32_bf16 v[48:51], v[244:247], v[112:115], v[48:51]\n"
        "v_mfma_f32_16x16x32_bf16 v[52:55], v[248:251], v[112:115], v[52:55]\n"
        "v_mfma_f32_16x16x32_bf16 v[56:59], v[252:255], v[112:115], v[56:59]\n"
        "buffer_load_dwordx4 v[112:115], v16, s[40:43], s63 offen offset:1024 sc1\n"
        "ds_write_b128 v24, v[32:35] offset:0\n"
        "ds_write_b128 v24, v[36:39] offset:2048\n"
        "ds_write_b128 v24, v[40:43] offset:4096\n"
        "ds_write_b128 v24, v[44:47] offset:6144\n"
        "ds_write_b128 v24, v[48:51] offset:8192\n"
        "ds_write_b128 v24, v[52:55] offset:10240\n"
        "s_nop 0\n"
        "ds_write_b128 v24, v[56:59] offset:12288\n"
        "s_waitcnt lgkmcnt(0)\n"
        "s_waitcnt vmcnt(14)\n"
        "v_cmp_eq_u32 vcc, 0, v116\n"
        "s_cbranch_vccz .Lw3_p1k_%=\n"
        "s_lshl_b32 s58, s48, 9\n"
        ".Lw3_p1_%=:\n"
        "buffer_load_dword v116, v19, s[40:43], s58 offen sc1\n"
        "s_cmp_eq_u32 s55, 0\n"
        "s_cbranch_scc1 .Lw3_p1k_%=\n"
        "s_sub_u32 s55, s55, 1\n"
        "s_waitcnt vmcnt(0)\n"
        "v_cmp_eq_u32 vcc, 0, v116\n"
        "s_cbranch_vccz .Lw3_p1k_%=\n"
        "s_sleep 16\n"
        "s_branch .Lw3_p1_%=\n"
        ".Lw3_p1k_%=:\n"
        "s_mul_i32 s57, s48, 114688\n"
        "s_add_u32 s61, s57, s46\n"
        "s_add_u32 s60, s61, 0\n"
        "s_add_u32 s62, s61, 8192\n"
        "s_add_u32 s63, s61, 12288\n"
        "s_add_u32 s61, s61, 4096\n"
        "s_waitcnt vmcnt(13)\n"
        "v_mfma_f32_16x16x32_bf16 v[32:35], a[0:3], v[60:63], 0\n"
        "v_mfma_f32_16x16x32_bf16 v[36:39], a[4:7], v[60:63], 0\n"
        "v_mfma_f32_16x16x32_bf16 v[40:43], a[8:11], v[60:63], 0\n"
        "v_mfma_f32_16x16x32_bf16 v[44:47], a[12:15], v[60:63], 0\n"
        "v_mfma_f32_16x16x32_bf16 v[48:51], a[16:19], v[60:63], 0\n"
        "v_mfma_f32_16x16x32_bf16 v[52:55], a[20:23], v[60:63], 0\n"
        "v_mfma_f32_16x16x32_bf16 v[56:59], a[24:27], v[60:63], 0\n"
        "buffer_load_dwordx4 v[60:63], v16, s[40:43], s60 offen offset:0 sc1\n"
        "s_waitcnt vmcnt(13)\n"
        "v_mfma_f32_16x16x32_bf16 v[32:35], a[28:31], v[64:67], v[32:35]\n"
        "v_mfma_f32_16x16x32_bf16 v[36:39], a[32:35], v[64:67], v[36:39]\n"
        "v_mfma_f32_16x16x32_bf16 v[40:43], a[36:39], v[64:67], v[40:43]\n"
        "v_mfma_f32_16x16x32_bf16 v[44:47], a[40:43], v[64:67], v[44:47]\n"
        "v_mfma_f32_16x16x32_bf16 v[48:51], a[44:47], v[64:67], v[48:51]\n"
        "v_mfma_f32_16x16x32_bf16 v[52:55], a[48:51], v[64:67], v[52:55]\n"
        "v_mfma_f32_16x16x32_bf16 v[56:59], a[52:55], v[64:67], v[56:59]\n"
        "buffer_load_dwordx4 v[64:67], v16, s[40:43], s60 offen offset:1024 sc1\n"
        "s_waitcnt vmcnt(13)\n"
        "v_mfma_f32_16x16x32_bf16 v[32:35], a[56:59], v[68:71], v[32:35]\n"
        "v_mfma_f32_16x16x32_bf16 v[36:39], a[60:63], v[68:71], v[36:39]\n"
        "v_mfma_f32_16x16x32_bf16 v[40:43], a[64:67], v[68:71], v[40:43]\n"
        "v_mfma_f32_16x16x32_bf16 v[44:47], a[68:71], v[68:71], v[44:47]\n"
        "v_mfma_f32_16x16x32_bf16 v[48:51], a[72:75], v[68:71], v[48:51]\n"
        "v_mfma_f32_16x16x32_bf16 v[52:55], a[76:79], v[68:71], v[52:55]\n"
        "v_mfma_f32_16x16x32_bf16 v[56:59], a[80:83], v[68:71], v[56:59]\n"
        "buffer_load_dwordx4 v[68:71], v16, s[40:43], s60 offen offset:2048 sc1\n"
        "s_waitcnt vmcnt(13)\n"
        "v_mfma_f32_16x16x32_bf16 v[32:35], a[84:87], v[72:75], v[32:35]\n"
        "v_mfma_f32_16x16x32_bf16 v[36:39], a[88:91], v[72:75], v[36:39]\n"
        "v_mfma_f32_16x16x32_bf16 v[40:43], a[92:95], v[72:75], v[40:43]\n"
        "v_mfma_f32_16x16x32_bf16 v[44:47], a[96:99], v[72:75], v[44:47]\n"
        "v_mfma_f32_16x16x32_bf16 v[48:51], a[100:103], v[72:75], v[48:51]\n"
        "v_mfma_f32_16x16x32_bf16 v[52:55], a[104:107], v[72:75], v[52:55]\n"
        "v_mfma_f32_16x16x32_bf16 v[56:59], a[108:111], v[72:75], v[56:59]\n"
        "buffer_load_dwordx4 v[72:75], v16, s[40:43], s60 offen offset:3072 sc1\n"
        "s_waitcnt vmcnt(13)\n"
        "v_mfma_f32_16x16x32_bf16 v[32:35], a[112:115], v[76:79], v[32:35]\n"
        "v_mfma_f32_16x16x32_bf16 v[36:39], a[116:119], v[76:79], v[36:39]\n"
        "v_mfma_f32_16x16x32_bf16 v[40:43], a[120:123], v[76:79], v[40:43]\n"
        "v_mfma_f32_16x16x32_bf16 v[44:47], a[124:127], v[76:79], v[44:47]\n"
        "v_mfma_f32_16x16x32_bf16 v[48:51], a[128:131], v[76:79], v[48:51]\n"
        "v_mfma_f32_16x16x32_bf16 v[52:55], a[132:135], v[76:79], v[52:55]\n"
        "v_mfma_f32_16x16x32_bf16 v[56:59], a[136:139], v[76:79], v[56:59]\n"
        "buffer_load_dwordx4 v[76:79], v16, s[40:43], s61 offen offset:0 sc1\n"
        "s_waitcnt vmcnt(13)\n"
        "v_mfma_f32_16x16x32_bf16 v[32:35], a[140:143], v[80:83], v[32:35]\n"
        "v_mfma_f32_16x16x32_bf16 v[36:39], a[144:147], v[80:83], v[36:39]\n"
        "v_mfma_f32_16x16x32_bf16 v[40:43], a[148:151], v[80:83], v[40:43]\n"
        "v_mfma_f32_16x16x32_bf16 v[44:47], a[152:155], v[80:83], v[44:47]\n"
        "v_mfma_f32_16x16x32_bf16 v[48:51], a[156:159], v[80:83], v[48:51]\n"
        "v_mfma_f32_16x16x32_bf16 v[52:55], a[160:163], v[80:83], v[52:55]\n"
        "v_mfma_f32_16x16x32_bf16 v[56:59], a[164:167], v[80:83], v[56:59]\n"
        "buffer_load_dwordx4 v[80:83], v16, s[40:43], s61 offen offset:1024 sc1\n"
        "s_waitcnt vmcnt(13)\n"
        "v_mfma_f32_16x16x32_bf16 v[32:35], a[168:171], v[84:87], v[32:35]\n"
        "v_mfma_f32_16x16x32_bf16 v[36:39], a[172:175], v[84:87], v[36:39]\n"
        "v_mfma_f32_16x16x32_bf16 v[40:43], a[176:179], v[84:87], v[40:43]\n"
        "v_mfma_f32_16x16x32_bf16 v[44:47], a[180:183], v[84:87], v[44:47]\n"
        "v_mfma_f32_16x16x32_bf16 v[48:51], a[184:187], v[84:87], v[48:51]\n"
        "v_mfma_f32_16x16x32_bf16 v[52:55], a[188:191], v[84:87], v[52:55]\n"
        "v_mfma_f32_16x16x32_bf16 v[56:59], a[192:195], v[84:87], v[56:59]\n"
        "buffer_load_dwordx4 v[84:87], v16, s[40:43], s61 offen offset:2048 sc1\n"
        "s_waitcnt vmcnt(13)\n"
        "v_mfma_f32_16x16x32_bf16 v[32:35], a[196:199], v[88:91], v[32:35]\n"
        "v_mfma_f32_16x16x32_bf16 v[36:39], a[200:203], v[88:91], v[36:39]\n"
        "v_mfma_f32_16x16x32_bf16 v[40:43], a[204:207], v[88:91], v[40:43]\n"
        "v_mfma_f32_16x16x32_bf16 v[44:47], a[208:211], v[88:91], v[44:47]\n"
        "v_mfma_f32_16x16x32_bf16 v[48:51], a[212:215], v[88:91], v[48:51]\n"
        "v_mfma_f32_16x16x32_bf16 v[52:55], a[216:219], v[88:91], v[52:55]\n"
        "v_mfma_f32_16x16x32_bf16 v[56:59], a[220:223], v[88:91], v[56:59]\n"
        "buffer_load_dwordx4 v[88:91], v16, s[40:43], s61 offen offset:3072 sc1\n"
        "s_waitcnt vmcnt(13)\n"
        "v_mfma_f32_16x16x32_bf16 v[32:35], a[224:227], v[92:95], v[32:35]\n"
        "v_mfma_f32_16x16x32_bf16 v[36:39], a[228:231], v[92:95], v[36:39]\n"
        "v_mfma_f32_16x16x32_bf16 v[40:43], a[232:235], v[92:95], v[40:43]\n"
        "v_mfma_f32_16x16x32_bf16 v[44:47], a[236:239], v[92:95], v[44:47]\n"
        "v_mfma_f32_16x16x32_bf16 v[48:51], a[240:243], v[92:95], v[48:51]\n"
        "v_mfma_f32_16x16x32_bf16 v[52:55], a[244:247], v[92:95], v[52:55]\n"
        "v_mfma_f32_16x16x32_bf16 v[56:59], a[248:251], v[92:95], v[56:59]\n"
        "buffer_load_dwordx4 v[92:95], v16, s[40:43], s62 offen offset:0 sc1\n"
        "s_waitcnt vmcnt(13)\n"
        "v_mfma_f32_16x16x32_bf16 v[32:35], a[252:255], v[96:99], v[32:35]\n"
        "v_mfma_f32_16x16x32_bf16 v[36:39], v[120:123], v[96:99], v[36:39]\n"
        "v_mfma_f32_16x16x32_bf16 v[40:43], v[124:127], v[96:99], v[40:43]\n"
        "v_mfma_f32_16x16x32_bf16 v[44:47], v[128:131], v[96:99], v[44:47]\n"
        "v_mfma_f32_16x16x32_bf16 v[48:51], v[132:135], v[96:99], v[48:51]\n"
        "v_mfma_f32_16x16x32_bf16 v[52:55], v[136:139], v[96:99], v[52:55]\n"
        "v_mfma_f32_16x16x32_bf16 v[56:59], v[140:143], v[96:99], v[56:59]\n"
        "buffer_load_dwordx4 v[96:99], v16, s[40:43], s62 offen offset:1024 sc1\n"
        "s_waitcnt vmcnt(13)\n"
        "v_mfma_f32_16x16x32_bf16 v[32:35], v[144:147], v[100:103], v[32:35]\n"
        "v_mfma_f32_16x16x32_bf16 v[36:39], v[148:151], v[100:103], v[36:39]\n"
        "v_mfma_f32_16x16x32_bf16 v[40:43], v[152:155], v[100:103], v[40:43]\n"
        "v_mfma_f32_16x16x32_bf16 v[44:47], v[156:159], v[100:103], v[44:47]\n"
        "v_mfma_f32_16x16x32_bf16 v[48:51], v[160:163], v[100:103], v[48:51]\n"
        "v_mfma_f32_16x16x32_bf16 v[52:55], v[164:167], v[100:103], v[52:55]\n"
        "v_mfma_f32_16x16x32_bf16 v[56:59], v[168:171], v[100:103], v[56:59]\n"
        "buffer_load_dwordx4 v[100:103], v16, s[40:43], s62 offen offset:2048 sc1\n"
        "s_waitcnt vmcnt(13)\n"
        "v_mfma_f32_16x16x32_bf16 v[32:35], v[172:175], v[104:107], v[32:35]\n"
        "v_mfma_f32_16x16x32_bf16 v[36:39], v[176:179], v[104:107], v[36:39]\n"
        "v_mfma_f32_16x16x32_bf16 v[40:43], v[180:183], v[104:107], v[40:43]\n"
        "v_mfma_f32_16x16x32_bf16 v[44:47], v[184:187], v[104:107], v[44:47]\n"
        "v_mfma_f32_16x16x32_bf16 v[48:51], v[188:191], v[104:107], v[48:51]\n"
        "v_mfma_f32_16x16x32_bf16 v[52:55], v[192:195], v[104:107], v[52:55]\n"
        "v_mfma_f32_16x16x32_bf16 v[56:59], v[196:199], v[104:107], v[56:59]\n"
        "buffer_load_dwordx4 v[104:107], v16, s[40:43], s62 offen offset:3072 sc1\n"
        "s_waitcnt vmcnt(13)\n"
        "v_mfma_f32_16x16x32_bf16 v[32:35], v[200:203], v[108:111], v[32:35]\n"
        "v_mfma_f32_16x16x32_bf16 v[36:39], v[204:207], v[108:111], v[36:39]\n"
        "v_mfma_f32_16x16x32_bf16 v[40:43], v[208:211], v[108:111], v[40:43]\n"
        "v_mfma_f32_16x16x32_bf16 v[44:47], v[212:215], v[108:111], v[44:47]\n"
        "v_mfma_f32_16x16x32_bf16 v[48:51], v[216:219], v[108:111], v[48:51]\n"
        "v_mfma_f32_16x16x32_bf16 v[52:55], v[220:223], v[108:111], v[52:55]\n"
        "v_mfma_f32_16x16x32_bf16 v[56:59], v[224:227], v[108:111], v[56:59]\n"
        "buffer_load_dwordx4 v[108:111], v16, s[40:43], s63 offen offset:0 sc1\n"
        "s_waitcnt vmcnt(13)\n"
        "v_mfma_f32_16x16x32_bf16 v[32:35], v[228:231], v[112:115], v[32:35]\n"
        "v_mfma_f32_16x16x32_bf16 v[36:39], v[232:235], v[112:115], v[36:39]\n"
        "v_mfma_f32_16x16x32_bf16 v[40:43], v[236:239], v[112:115], v[40:43]\n"
        "v_mfma_f32_16x16x32_bf16 v[44:47], v[240:243], v[112:115], v[44:47]\n"
        "v_mfma_f32_16x16x32_bf16 v[48:51], v[244:247], v[112:115], v[48:51]\n"
        "v_mfma_f32_16x16x32_bf16 v[52:55], v[248:251], v[112:115], v[52:55]\n"
        "v_mfma_f32_16x16x32_bf16 v[56:59], v[252:255], v[112:115], v[56:59]\n"
        "buffer_load_dwordx4 v[112:115], v16, s[40:43], s63 offen offset:1024 sc1\n"
        "ds_write_b128 v24, v[32:35] offset:1024\n"
        "ds_write_b128 v24, v[36:39] offset:3072\n"
        "ds_write_b128 v24, v[40:43] offset:5120\n"
        "ds_write_b128 v24, v[44:47] offset:7168\n"
        "ds_write_b128 v24, v[48:51] offset:9216\n"
        "ds_write_b128 v24, v[52:55] offset:11264\n"
        "s_nop 0\n"
        "ds_write_b128 v24, v[56:59] offset:13312\n"
        "s_waitcnt lgkmcnt(0)\n"
        "s_barrier\n"
        "ds_read_b128 v[32:35], v25 offset:0\n"
        "ds_read_b128 v[36:39], v25 offset:14336\n"
        "ds_read_b128 v[40:43], v25 offset:28672\n"
        "ds_read_b128 v[44:47], v25 offset:43008\n"
        "s_waitcnt lgkmcnt(2)\n"
        "v_pk_add_f32 v[32:33], v[32:33], v[36:37]\n"
        "v_pk_add_f32 v[34:35], v[34:35], v[38:39]\n"
        "s_waitcnt lgkmcnt(0)\n"
        "v_pk_add_f32 v[40:41], v[40:41], v[44:45]\n"
        "v_pk_add_f32 v[42:43], v[42:43], v[46:47]\n"
        "v_pk_add_f32 v[32:33], v[32:33], v[40:41]\n"
        "v_pk_add_f32 v[34:35], v[34:35], v[42:43]\n"
        "v_pk_mul_f32 v[48:49], v[32:33], s[74:75]\n"
        "v_pk_mul_f32 v[50:51], v[34:35], s[74:75]\n"
        "v_exp_f32 v48, v48\n"
        "v_exp_f32 v49, v49\n"
        "v_exp_f32 v50, v50\n"
        "v_exp_f32 v51, v51\n"
        "v_pk_add_f32 v[48:49], v[48:49], 1.0 op_sel_hi:[1,0]\n"
        "v_pk_add_f32 v[50:51], v[50:51], 1.0 op_sel_hi:[1,0]\n"
        "v_rcp_f32 v48, v48\n"
        "v_rcp_f32 v49, v49\n"
        "v_rcp_f32 v50, v50\n"
        "v_rcp_f32 v51, v51\n"
        "v_pk_mul_f32 v[48:49], v[48:49], v[32:33]\n"
        "v_pk_mul_f32 v[50:51], v[50:51], v[34:35]\n"
        "buffer_store_dwordx4 v[48:51], v25, s[40:43], s50 offen sc1\n"
        "s_add_u32 s50, s50, 4096\n"
        "ds_read_b128 v[32:35], v25 offset:4096\n"
        "ds_read_b128 v[36:39], v25 offset:18432\n"
        "ds_read_b128 v[40:43], v25 offset:32768\n"
        "ds_read_b128 v[44:47], v25 offset:47104\n"
        "s_waitcnt lgkmcnt(2)\n"
        "v_pk_add_f32 v[32:33], v[32:33], v[36:37]\n"
        "v_pk_add_f32 v[34:35], v[34:35], v[38:39]\n"
        "s_waitcnt lgkmcnt(0)\n"
        "v_pk_add_f32 v[40:41], v[40:41], v[44:45]\n"
        "v_pk_add_f32 v[42:43], v[42:43], v[46:47]\n"
        "v_pk_add_f32 v[32:33], v[32:33], v[40:41]\n"
        "v_pk_add_f32 v[34:35], v[34:35], v[42:43]\n"
        "v_pk_mul_f32 v[52:53], v[32:33], s[74:75]\n"
        "v_pk_mul_f32 v[54:55], v[34:35], s[74:75]\n"
        "v_exp_f32 v52, v52\n"
        "v_exp_f32 v53, v53\n"
        "v_exp_f32 v54, v54\n"
        "v_exp_f32 v55, v55\n"
        "v_pk_add_f32 v[52:53], v[52:53], 1.0 op_sel_hi:[1,0]\n"
        "v_pk_add_f32 v[54:55], v[54:55], 1.0 op_sel_hi:[1,0]\n"
        "v_rcp_f32 v52, v52\n"
        "v_rcp_f32 v53, v53\n"
        "v_rcp_f32 v54, v54\n"
        "v_rcp_f32 v55, v55\n"
        "v_pk_mul_f32 v[52:53], v[52:53], v[32:33]\n"
        "v_pk_mul_f32 v[54:55], v[54:55], v[34:35]\n"
        "buffer_store_dwordx4 v[52:55], v25, s[40:43], s50 offen sc1\n"
        "s_add_u32 s50, s50, 4096\n"
        "ds_read_b128 v[32:35], v25 offset:8192\n"
        "ds_read_b128 v[36:39], v25 offset:22528\n"
        "ds_read_b128 v[40:43], v25 offset:36864\n"
        "ds_read_b128 v[44:47], v25 offset:51200\n"
        "s_waitcnt lgkmcnt(2)\n"
        "v_pk_add_f32 v[32:33], v[32:33], v[36:37]\n"
        "v_pk_add_f32 v[34:35], v[34:35], v[38:39]\n"
        "s_waitcnt lgkmcnt(0)\n"
        "v_pk_add_f32 v[40:41], v[40:41], v[44:45]\n"
        "v_pk_add_f32 v[42:43], v[42:43], v[46:47]\n"
        "v_pk_add_f32 v[32:33], v[32:33], v[40:41]\n"
        "v_pk_add_f32 v[34:35], v[34:35], v[42:43]\n"
        "v_pk_mul_f32 v[48:49], v[32:33], s[74:75]\n"
        "v_pk_mul_f32 v[50:51], v[34:35], s[74:75]\n"
        "v_exp_f32 v48, v48\n"
        "v_exp_f32 v49, v49\n"
        "v_exp_f32 v50, v50\n"
        "v_exp_f32 v51, v51\n"
        "v_pk_add_f32 v[48:49], v[48:49], 1.0 op_sel_hi:[1,0]\n"
        "v_pk_add_f32 v[50:51], v[50:51], 1.0 op_sel_hi:[1,0]\n"
        "v_rcp_f32 v48, v48\n"
        "v_rcp_f32 v49, v49\n"
        "v_rcp_f32 v50, v50\n"
        "v_rcp_f32 v51, v51\n"
        "v_pk_mul_f32 v[48:49], v[48:49], v[32:33]\n"
        "v_pk_mul_f32 v[50:51], v[50:51], v[34:35]\n"
        "buffer_store_dwordx4 v[48:51], v25, s[40:43], s50 offen sc1\n"
        "s_add_u32 s50, s50, 4096\n"
        "s_cmp_ge_u32 %4, 2\n"
        "s_cbranch_scc1 .Lw3_sk_%=\n"
        "ds_read_b128 v[32:35], v25 offset:12288\n"
        "ds_read_b128 v[36:39], v25 offset:26624\n"
        "ds_read_b128 v[40:43], v25 offset:40960\n"
        "ds_read_b128 v[44:47], v25 offset:55296\n"
        "s_waitcnt lgkmcnt(2)\n"
        "v_pk_add_f32 v[32:33], v[32:33], v[36:37]\n"
        "v_pk_add_f32 v[34:35], v[34:35], v[38:39]\n"
        "s_waitcnt lgkmcnt(0)\n"
        "v_pk_add_f32 v[40:41], v[40:41], v[44:45]\n"
        "v_pk_add_f32 v[42:43], v[42:43], v[46:47]\n"
        "v_pk_add_f32 v[32:33], v[32:33], v[40:41]\n"
        "v_pk_add_f32 v[34:35], v[34:35], v[42:43]\n"
        "v_pk_mul_f32 v[52:53], v[32:33], s[74:75]\n"
        "v_pk_mul_f32 v[54:55], v[34:35], s[74:75]\n"
        "v_exp_f32 v52, v52\n"
        "v_exp_f32 v53, v53\n"
        "v_exp_f32 v54, v54\n"
        "v_exp_f32 v55, v55\n"
        "v_pk_add_f32 v[52:53], v[52:53], 1.0 op_sel_hi:[1,0]\n"
        "v_pk_add_f32 v[54:55], v[54:55], 1.0 op_sel_hi:[1,0]\n"
        "v_rcp_f32 v52, v52\n"
        "v_rcp_f32 v53, v53\n"
        "v_rcp_f32 v54, v54\n"
        "v_rcp_f32 v55, v55\n"
        "v_pk_mul_f32 v[52:53], v[52:53], v[32:33]\n"
        "v_pk_mul_f32 v[54:55], v[54:55], v[34:35]\n"
        "buffer_store_dwordx4 v[52:55], v25, s[40:43], s50 offen sc1\n"
        "s_add_u32 s50, s50, 4096\n"
        ".Lw3_sk_%=:\n"
        "v_mov_b32 v117, 1\n"
        "s_mov_b32 s44, s47\n"
        "s_waitcnt vmcnt(0)\n"
        "s_barrier\n"
        "buffer_store_dword v117, v18, s[40:43], s54 offen sc1\n"
        "s_cmp_lt_u32 s44, 64\n"
        "s_cbranch_scc1 .Lw3_loop_%=\n"
        "s_waitcnt vmcnt(0)\n"
        :
        : "s"(lo), "s"(hi), "s"(WS_BYTES), "s"(rep), "s"(wv), "s"((unsigned)(uintptr_t)w2), "s"((unsigned)((uintptr_t)w2 >> 32)),
          "s"(wv * 448u * 896u + cg * 224u), "s"(OFF_G + cg * 14336u),
          "v"(lane * 16), "v"(wv * 28672 + (8 * g + (n >> 2)) * 224 + (n & 3) * 8), "v"(tid == 0 ? OFF_FLAG + 384 + cg * 4 : OOR),
          "v"(OFF_FLAG + 256 + (2 * wv + (lane & 1)) * 4)
        : "memory", "vcc", "scc", "s40", "s41", "s42", "s43", "s44", "s45", "s46", "s47", "s48", "s49", "s50", "s51", "s52", "s53", "s54", "s55", "s56", "s57", "s58", "s59", "s60", "s61", "s62", "s63", "s64", "s65", "s66", "s67", "s68", "s69", "s70", "s71", "s72", "s73", "s74", "s75", "s76", "s77", "s78", "s79", "v16", "v17", "v18", "v19", "v20", "v21", "v22", "v23", "v24", "v25", "v26", "v27", "v28", "v29", "v30", "v31", "v32", "v33", "v34", "v35", "v36", "v37", "v38", "v39", "v40", "v41", "v42", "v43", "v44", "v45", "v46", "v47", "v48", "v49", "v50", "v51", "v52", "v53", "v54", "v55", "v56", "v57", "v58", "v59", "v60", "v61", "v62", "v63", "v64", "v65", "v66", "v67", "v68", "v69", "v70", "v71", "v72", "v73", "v74", "v75", "v76", "v77", "v78", "v79", "v80", "v81", "v82", "v83", "v84", "v85", "v86", "v87", "v88", "v89", "v90", "v91", "v92", "v93", "v94", "v95", "v96", "v97", "v98", "v99", "v100", "v101", "v102", "v103", "v104", "v105", "v106", "v107", "v108", "v109", "v110", "v111", "v112", "v113", "v114", "v115", "v116", "v117", "v118", "v119", "v120", "v121", "v122", "v123", "v124", "v125", "v126", "v127", "v128", "v129", "v130", "v131", "v132", "v133", "v134", "v135", "v136", "v137", "v138", "v139", "v140", "v141", "v142", "v143", "v144", "v145", "v146", "v147", "v148", "v149", "v150", "v151", "v152", "v153", "v154", "v155", "v156", "v157", "v158", "v159", "v160", "v161", "v162", "v163", "v164", "v165", "v166", "v167", "v168", "v169", "v170", "v171", "v172", "v173", "v174", "v175", "v176", "v177", "v178", "v179", "v180", "v181", "v182", "v183", "v184", "v185", "v186", "v187", "v188", "v189", "v190", "v191", "v192", "v193", "v194", "v195", "v196", "v197", "v198", "v199", "v200", "v201", "v202", "v203", "v204", "v205", "v206", "v207", "v208", "v209", "v210", "v211", "v212", "v213", "v214", "v215", "v216", "v217", "v218", "v219", "v220", "v221", "v222", "v223", "v224", "v225", "v226", "v227", "v228", "v229", "v230", "v231", "v232", "v233", "v234", "v235", "v236", "v237", "v238", "v239", "v240", "v241", "v242", "v243", "v244", "v245", "v246", "v247", "v248", "v249", "v250", "v251", "v252", "v253", "v254", "v255", "a0", "a1", "a2", "a3", "a4", "a5", "a6", "a7", "a8", "a9", "a10", "a11", "a12", "a13", "a14", "a15", "a16", "a17", "a18", "a19", "a20", "a21", "a22", "a23", "a24", "a25", "a26", "a27", "a28", "a29", "a30", "a31", "a32", "a33", "a34", "a35", "a36", "a37", "a38", "a39", "a40", "a41", "a42", "a43", "a44", "a45", "a46", "a47", "a48", "a49", "a50", "a51", "a52", "a53", "a54", "a55", "a56", "a57", "a58", "a59", "a60", "a61", "a62", "a63", "a64", "a65", "a66", "a67", "a68", "a69", "a70", "a71", "a72", "a73", "a74", "a75", "a76", "a77", "a78", "a79", "a80", "a81", "a82", "a83", "a84", "a85", "a86", "a87", "a88", "a89", "a90", "a91", "a92", "a93", "a94", "a95", "a96", "a97", "a98", "a99", "a100", "a101", "a102", "a103", "a104", "a105", "a106", "a107", "a108", "a109", "a110", "a111", "a112", "a113", "a114", "a115", "a116", "a117", "a118", "a119", "a120", "a121", "a122", "a123", "a124", "a125", "a126", "a127", "a128", "a129", "a130", "a131", "a132", "a133", "a134", "a135", "a136", "a137", "a138", "a139", "a140", "a141", "a142", "a143", "a144", "a145", "a146", "a147", "a148", "a149", "a150", "a151", "a152", "a153", "a154", "a155", "a156", "a157", "a158", "a159", "a160", "a161", "a162", "a163", "a164", "a165", "a166", "a167", "a168", "a169", "a170", "a171", "a172", "a173", "a174", "a175", "a176", "a177", "a178", "a179", "a180", "a181", "a182", "a183", "a184", "a185", "a186", "a187", "a188", "a189", "a190", "a191", "a192", "a193", "a194", "a195", "a196", "a197", "a198", "a199", "a200", "a201", "a202", "a203", "a204", "a205", "a206", "a207", "a208", "a209", "a210", "a211", "a212", "a213", "a214", "a215", "a216", "a217", "a218", "a219", "a220", "a221", "a222", "a223", "a224", "a225", "a226", "a227", "a228", "a229", "a230", "a231", "a232", "a233", "a234", "a235", "a236", "a237", "a238", "a239", "a240", "a241", "a242", "a243", "a244", "a245", "a246", "a247", "a248", "a249", "a250", "a251", "a252", "a253", "a254", "a255");
}

constexpr unsigned L_XQ = 57344;
DI float xmax16(float v) { const auto r = __builtin_amdgcn_permlane16_swap(fbits(v), fbits(v), false, false); return fmaxf(bitsf(r[0]), bitsf(r[1])); }
DI float xmax32(float v) { const auto r = __builtin_amdgcn_permlane32_swap(fbits(v), fbits(v), false, false); return fmaxf(bitsf(r[0]), bitsf(r[1])); }
DI float xadd16(float v) { const auto r = __builtin_amdgcn_permlane16_swap(fbits(v), fbits(v), false, false); return bitsf(r[0]) + bitsf(r[1]); }
DI float xadd32(float v) { const auto r = __builtin_amdgcn_permlane32_swap(fbits(v), fbits(v), false, false); return bitsf(r[0]) + bitsf(r[1]); }
DI void out_task(const int seg, const int cg, const int tm, const __bf16* __restrict__ x, __bf16* __restrict__ out, const rsrc_t rs, const int wv) {
    const int lane = lane_id(), tid = wv * 64 + lane;
    const int g = lane >> 4, n = lane & 15;
    const f4 z = {0.f, 0.f, 0.f, 0.f};
    {
        u4 xs[7];
#pragma unroll
        for (int i = 0; i < 7; ++i) xs[i] = ld128<0>(rs, OFF_XNF + tid * 16, seg * 28672u + i * 4096);
#pragma unroll
        for (int i = 0; i < 7; ++i) ldsw128(L_XQ + i * 4096 + tid * 16, xs[i]);
    }
    if (wv == 0) wait_flags<16>(rs, OFF_FLAG + seg * 512 + (lane < 32 ? lane * 4 : 384 + cg * 4));
    wg_barrier();
    f4 e[4][3];
#pragma unroll
    for (int hh = 0; hh < 4; ++hh)
#pragma unroll
        for (int b = 0; b < 3; ++b) e[hh][b] = __builtin_bit_cast(f4, ld128<SC1>(rs, OFF_SXX + b * 1024 + lane * 16, (unsigned)(seg * 16 + 4 * wv + hh) * 3072u));
    u4 vo[7][4];
#pragma unroll
    for (int dt = 0; dt < 7; ++dt)
#pragma unroll
        for (int hh = 0; hh < 4; ++hh) vo[dt][hh] = ld128<SC1>(rs, OFF_VOT + (dt & 3) * 1024 + lane * 16, (unsigned)seg * 458752u + (unsigned)((4 * wv + hh) * 28 + 7 * cg + (dt & 4)) * 1024u);
    u4 gate[4]; u2 xv[4];
#pragma unroll
    for (int i = 0; i < 4; ++i) {
        const int it = (i < 3 || wv < 2) ? tid + 256 * i : tid, tl = it >> 6;
        gate[i] = ld128<SC1>(rs, OFF_G + it * 16, (unsigned)(seg * 4 + cg) * 14336u);
        xv[i] = ldnt((const u2*)(x + (size_t)(32 * seg + 16 * (tl & 1) + n) * DM + 112 * cg + 16 * (tl >> 1) + 4 * g));
    }
    constexpr float NEG = -1.0e9f * 1.4426950408889634f;
    float mx[4][2], lx[4][2], qm[4];
#pragma unroll
    for (int hh = 0; hh < 4; ++hh) {
#pragma unroll
        for (int r = 0; r < 4; ++r) {
            e[hh][0][r] = 4 * g + r <= n ? e[hh][0][r] : NEG;
            e[hh][2][r] = 4 * g + r <= n ? e[hh][2][r] : NEG;
        }
        const f4 a = e[hh][0], b = e[hh][1], c = e[hh][2];
        float m0 = fmaxf(fmaxf(a[0], a[1]), fmaxf(a[2], a[3]));
        float m1 = fmaxf(fmaxf(fmaxf(b[0], b[1]), fmaxf(b[2], b[3])), fmaxf(fmaxf(c[0], c[1]), fmaxf(c[2], c[3])));
        m0 = fmaxf(m0, NEG);
        m0 = xmax32(xmax16(m0));
        m1 = xmax32(xmax16(m1));
#pragma unroll
        for (int r = 0; r < 4; ++r) { e[hh][0][r] = ex2(a[r] - m0); e[hh][1][r] = ex2(b[r] - m1); e[hh][2][r] = ex2(c[r] - m1); }
        mx[hh][0] = m0; mx[hh][1] = m1; qm[hh] = ex2(NEG - m0);
        lx[hh][0] = (e[hh][0][0] + e[hh][0][1]) + (e[hh][0][2] + e[hh][0][3]) + 4.0f * qm[hh];
        lx[hh][1] = ((e[hh][1][0] + e[hh][1][1]) + (e[hh][1][2] + e[hh][1][3])) + ((e[hh][2][0] + e[hh][2][1]) + (e[hh][2][2] + e[hh][2][3]));
        asm volatile("" : "+v"(e[hh][0]), "+v"(e[hh][1]), "+v"(e[hh][2]), "+v"(lx[hh][0]), "+v"(lx[hh][1]), "+v"(qm[hh]));
    }
#pragma unroll
    for (int dt = 0; dt < 7; ++dt) asm volatile("" : "+a"(vo[dt][0]), "+a"(vo[dt][1]), "+a"(vo[dt][2]), "+a"(vo[dt][3]));
    asm volatile("" : "+v"(gate[0]), "+v"(gate[1]), "+v"(gate[2]), "+v"(gate[3]), "+v"(xv[0]), "+v"(xv[1]), "+v"(xv[2]), "+v"(xv[3]));
    if (tm == 1) {
#pragma unroll
        for (int hh = 0; hh < 4; ++hh) { mx[hh][1] = mx[hh][0]; lx[hh][1] = lx[hh][0]; e[hh][1] = e[hh][0]; e[hh][2] = f4{qm[hh], qm[hh], qm[hh], qm[hh]}; }
    }
    WTS(8);
    if (wv == 0) wait_flags<OUT_SLEEP>(rs, OFF_RFL + seg * 4096 + (lane & 31) * 128);
    wg_barrier();
    WTS(9);
    u4 ta[8][4], uf[7];
#pragma unroll
    for (int kq = 0; kq < 8; ++kq)
#pragma unroll
        for (int hh = 0; hh < 4; ++hh)
            ta[kq][hh] = ld128<SC1>(rs, OFF_SIDE + 1024 + (kq & 1) * 2048 + lane * 16, seg * 262144u + (unsigned)(2 * (4 * wv + hh) + (kq >> 2)) * 8192u + ((kq >> 1) & 1) * 4096);
#pragma unroll
    for (int dt = 0; dt < 7; ++dt) uf[dt] = ld128<SC1>(rs, OFF_SIDE + lane * 16, seg * 262144u + (unsigned)(7 * cg + dt) * 8192u + wv * 2048);
    __builtin_amdgcn_sched_barrier(0);
    const int idxT = (n & 8) ? 0xEEEE : 0x4444;
#pragma unroll
    for (int tt = 0; tt < 2; ++tt)
        if (tt == 1 || tm == 3) {
            const int tq = tt ? (tm >> 1) : 0;
            f4 sm[4] = {z, z, z, z};
#pragma unroll
            for (int kq = 0; kq < 8; ++kq) {
                const int kh = kq >> 2, kp = kq & 3;
                const int k0 = kp < 3 ? 8 * kh + 2 * kp : 6 + kh, k1 = kp < 3 ? k0 + 1 : k0;
                const u8v xb = cat(ldsr128(L_XQ + (14 * tq + k0) * 1024 + lane * 16), ldsr128(L_XQ + (14 * tq + k1) * 1024 + lane * 16));
#pragma unroll
                for (int hh = 0; hh < 4; ++hh) sm[hh] = smf64(ta[kq][hh], xb, sm[hh], idxT);
            }
            u4 px[4], pm;
#pragma unroll
            for (int hh = 0; hh < 4; ++hh) {
                const f4 sq = sm[hh];
                const auto r0 = __builtin_amdgcn_permlane32_swap(fbits(sq[0]), fbits(sq[2]), false, false);
                const auto r1 = __builtin_amdgcn_permlane32_swap(fbits(sq[1]), fbits(sq[3]), false, false);
                const float m0 = bitsf(r0[0]) + bitsf(r0[1]), m1 = bitsf(r1[0]) + bitsf(r1[1]);
                const float m = fmaxf(xmax32(xmax16(fmaxf(m0, m1))), mx[hh][tt]);
                const float a = ex2(mx[hh][tt] - m), f0 = ex2(m0 - m), f1 = ex2(m1 - m);
                const float l = xadd32(xadd16(a * lx[hh][tt] + (f0 + f1)));
                const float rr = __builtin_amdgcn_rcpf(l), sc = a * rr;
                if (tt == 0) { const float q = qm[hh] * sc; px[hh] = pk8(e[hh][0] * sc, f4{q, q, q, q}); }
                else px[hh] = pk8(e[hh][1] * sc, e[hh][2] * sc);
                pm[hh] = pk(f0 * rr, f1 * rr);
            }
            f4 c[7];
#pragma unroll
            for (int dt = 0; dt < 7; ++dt) c[dt] = mfma(uf[dt], pm, z);
#pragma unroll
            for (int hh = 0; hh < 4; ++hh)
#pragma unroll
                for (int dt = 0; dt < 7; ++dt) c[dt] = mfma(vo[dt][hh], px[hh], c[dt]);
#pragma unroll
            for (int dt = 0; dt < 7; ++dt) ldsw128((wv * 14 + dt * 2 + tq) * 1024 + lane * 16, __builtin_bit_cast(u4, c[dt]));
        }
    WTS(2);
    wg_barrier();
    f4 pr[4][4];
#pragma unroll
    for (int i = 0; i < 4; ++i)
#pragma unroll
        for (int w = 0; w < 4; ++w) pr[i][w] = __builtin_bit_cast(f4, ldsr128(w * 14336 + (tid + 256 * i) * 16));
    __builtin_amdgcn_sched_barrier(0);
#pragma unroll
    for (int i = 0; i < 4; ++i) {
        const int it = tid + 256 * i, tl = it >> 6;
        if ((i < 3 || wv < 2) && ((tm >> (wv & 1)) & 1)) {
            const f4 ss = (pr[i][0] + pr[i][1]) + (pr[i][2] + pr[i][3]);
            const f4 gs = __builtin_bit_cast(f4, gate[i]);
            const float o0 = bflo(xv[i].x) + ss[0] * gs[0], o1 = bfhi(xv[i].x) + ss[1] * gs[1];
            const float o2 = bflo(xv[i].y) + ss[2] * gs[2], o3 = bfhi(xv[i].y) + ss[3] * gs[3];
            *(u2*)(out + (size_t)(32 * seg + 16 * (tl & 1) + n) * DM + 112 * cg + 16 * (tl >> 1) + 4 * g) = u2{pk(o0, o1), pk(o2, o3)};
        }
    }
    WTS(10);
}

constexpr unsigned L_IMG = 4096, L_UL = 11264, L_RED = 15360, L_TBL = 17664;
constexpr unsigned L_REC_BYTES = L_TBL + 23 * 1024;

DI void rec_main(const int P, unsigned char* ws, const unsigned tab) {
    const int tid = threadIdx.x, lane = tid & 63, W = __builtin_amdgcn_readfirstlane(tid >> 6);
    const int g = lane >> 4, n = lane & 15, hA = P >> 1, kh = P & 1, PB = P < 28 ? P : P - 4;
    const int sel = (n >> 1) & 1, row = 2 * (n >> 2) + (n & 1), hd = 4 * W + g;
    unsigned t[23];
    t[0] = tid * 16;
    t[1] = lane * 8;
    t[2] = (n >> 3) * 512 + ((n >> 2) & 1) * 64 + (n & 1) * 32 + sel * 16 + (2 * (g & 1) + (g >> 1)) * 4;
    t[3] = sel ? 0xEEEEu : 0x4444u;
    t[4] = B_S + (hd >> 3) * BLK + (hd & 7) * 512 + row * 32 + sel * 16;
    t[5] = B_ML + hd * 128 + row * 8;
    t[6] = L_UL + W * 1024 + n * 16 + g * 4;
    const unsigned hm = sel == ((n >> 2) & 1) ? ((n & 1) ? 0xFFFF0000u : 0x0000FFFFu) : 0u;
    t[7] = n < 8 ? hm : 0u;
    t[8] = n < 8 ? 0u : hm;
    const unsigned pp = (P >> 4) * BLK + (P & 15) * 256;
    t[9] = W < 2 ? (W ? B_MEMH + P * 256 : pp) + lane * 4
                 : (W == 2 ? (lane < 4 ? ((lane & 2) ? B_S : B_MEMR) + pp + (lane & 1) * 128 : OOR) : ((lane == 0 && kh) ? B_TRG + hA * 128 : OOR));
    t[10] = L_RED + (2 * g + sel) * 272 + (n >> 2) * 32 + (n & 1) * 4;
    t[11] = W < 3 ? L_RED + lane * 32 + (lane >> 3) * 16 : (lane == 0 ? OFF_RFL + 128 * P - 8192 : OOR);
    t[12] = L_IMG + 3072 + ((g & 1) + 2 * kh) * 256 + n * 16;
#pragma unroll
    for (int i = 0; i < 4; ++i) {
        const int c = i < 3 ? 256 * kh + 16 * (4 * i + W) : 192 + 32 * kh + 16 * (W >> 1);
        t[14 + i] = OFF_WC + (unsigned)(14 * hA + (c >> 5)) * 28672u + g * 512 + ((c & 31) + n) * 16;
        t[18 + i] = OFF_WC + (unsigned)(224 + 14 * (4 * W + i) + (PB >> 1)) * 28672u + g * 512 + (16 * (PB & 1) + n) * 16;
    }
    t[13] = tab;
    t[22] = W < 2 ? ((unsigned)lane << 20) + W * 8192 : (W == 2 ? OFF_SIDE + lane * 262144u : OFF_MISC);
#pragma unroll
    for (int i = 0; i < 23; ++i) ldsw32(L_TBL + i * 1024 + tid * 4, t[i]);
    const unsigned lo = (unsigned)(uintptr_t)ws, hi = (unsigned)((uintptr_t)ws >> 32);
    const unsigned ob = oblk(PB >> 1) + (PB & 1) * 4096, sb = OFF_SIDE + P * 8192, var = (W == 0 && kh) ? 4 : W;
    asm volatile(
        "s_mov_b32 s40, %0\n"
        "s_and_b32 s41, %1, 0xffff\n"
        "s_mov_b32 s42, %2\n"
        "s_mov_b32 s43, 0x00020000\n"
        "s_mov_b32 s44, 0\n"
        "s_mov_b32 s56, %4\n"
        "s_mov_b32 s54, %5\n"
        "s_mov_b32 s53, %6\n"
        "s_mov_b32 s55, 0x10000\n"
        "s_mov_b32 s59, 0\n"
        "s_mov_b64 exec, -1\n"
        "s_cmp_eq_u32 %7, 1\n"
        "s_cbranch_scc1 .Lr1_entry_%=\n"
        "s_cmp_eq_u32 %7, 2\n"
        "s_cbranch_scc1 .Lr2_entry_%=\n"
        "s_cmp_eq_u32 %7, 3\n"
        "s_cbranch_scc1 .Lr3_entry_%=\n"
        "s_cmp_eq_u32 %7, 4\n"
        "s_cbranch_scc1 .Lr0b_entry_%=\n"
        ".Lr0a_entry_%=:\n"
        "v_mbcnt_lo_u32_b32 v0, -1, 0\n"
        "v_mbcnt_hi_u32_b32 v0, -1, v0\n"
        "v_lshlrev_b32 v0, 2, v0\n"
        "ds_read_b32 v72, v0 offset:17664\n"
        "ds_read_b32 v73, v0 offset:18688\n"
        "ds_read_b32 v74, v0 offset:19712\n"
        "ds_read_b32 v75, v0 offset:20736\n"
        "ds_read_b32 v76, v0 offset:21760\n"
        "ds_read_b32 v77, v0 offset:22784\n"
        "ds_read_b32 v78, v0 offset:23808\n"
        "ds_read_b32 v79, v0 offset:24832\n"
        "ds_read_b32 v80, v0 offset:25856\n"
        "ds_read_b32 v81, v0 offset:26880\n"
        "ds_read_b32 v82, v0 offset:27904\n"
        "ds_read_b32 v83, v0 offset:28928\n"
        "ds_read_b32 v84, v0 offset:29952\n"
        "ds_read_b32 v85, v0 offset:30976\n"
        "s_waitcnt lgkmcnt(0)\n"
        "ds_read_b32 v1, v0 offset:32000\n"
        "ds_read_b32 v2, v0 offset:33024\n"
        "ds_read_b32 v3, v0 offset:34048\n"
        "ds_read_b32 v4, v0 offset:35072\n"
        "ds_read_b32 v5, v0 offset:36096\n"
        "ds_read_b32 v6, v0 offset:37120\n"
        "ds_read_b32 v7, v0 offset:38144\n"
        "ds_read_b32 v8, v0 offset:39168\n"
        "ds_read_b32 v9, v0 offset:40192\n"
        "s_waitcnt lgkmcnt(0)\n"
        "buffer_load_dword v10, v9, s[40:43], 0 offen\n"
        "v_readlane_b32 s45, v85, 0\n"
        "s_xor_b32 s47, s45, 0x84000\n"
        "s_andn2_b32 s45, s45, 0x1000\n"
        "s_add_u32 s49, s47, 32768\n"
        "s_getpc_b64 s[58:59]\n"
        "v_lshlrev_b32 v12, 5, v0\n"
        "v_min_u32 v12, .Lr0a_exit_%=-.Lr0a_loop_%=-64, v12\n"
        "v_add_u32 v12, .Lr0a_loop_%=-.Lr0a_entry_%=, v12\n"
        "global_load_dword v12, v12, s[58:59]\n"
        "s_mov_b32 s59, 0\n"
        "s_mov_b32 s60, 0\n"
        "s_mov_b32 s61, 4096\n"
        "s_mov_b32 s62, 8192\n"
        "s_mov_b32 s63, 12288\n"
        "s_mov_b32 s64, 16384\n"
        "s_mov_b32 s65, 20480\n"
        "s_mov_b32 s66, 24576\n"
        "buffer_load_dwordx4 v[88:91], v1, s[40:43], s60 offen\n"
        "buffer_load_dwordx4 v[92:95], v1, s[40:43], s60 offen offset:2048\n"
        "buffer_load_dwordx4 v[96:99], v1, s[40:43], s61 offen\n"
        "buffer_load_dwordx4 v[100:103], v1, s[40:43], s61 offen offset:2048\n"
        "buffer_load_dwordx4 v[104:107], v1, s[40:43], s62 offen\n"
        "buffer_load_dwordx4 v[108:111], v1, s[40:43], s62 offen offset:2048\n"
        "buffer_load_dwordx4 v[112:115], v1, s[40:43], s63 offen\n"
        "buffer_load_dwordx4 v[116:119], v1, s[40:43], s63 offen offset:2048\n"
        "buffer_load_dwordx4 v[120:123], v1, s[40:43], s64 offen\n"
        "buffer_load_dwordx4 v[124:127], v1, s[40:43], s64 offen offset:2048\n"
        "buffer_load_dwordx4 v[128:131], v1, s[40:43], s65 offen\n"
        "buffer_load_dwordx4 v[132:135], v1, s[40:43], s65 offen offset:2048\n"
        "buffer_load_dwordx4 v[136:139], v1, s[40:43], s66 offen\n"
        "buffer_load_dwordx4 v[140:143], v1, s[40:43], s66 offen offset:2048\n"
        "buffer_load_dwordx4 v[144:147], v2, s[40:43], s60 offen\n"
        "buffer_load_dwordx4 v[148:151], v2, s[40:43], s60 offen offset:2048\n"
        "buffer_load_dwordx4 v[152:155], v2, s[40:43], s61 offen\n"
        "buffer_load_dwordx4 v[156:159], v2, s[40:43], s61 offen offset:2048\n"
        "buffer_load_dwordx4 v[160:163], v2, s[40:43], s62 offen\n"
        "buffer_load_dwordx4 v[164:167], v2, s[40:43], s62 offen offset:2048\n"
        "buffer_load_dwordx4 v[168:171], v2, s[40:43], s63 offen\n"
        "buffer_load_dwordx4 v[172:175], v2, s[40:43], s63 offen offset:2048\n"
        "buffer_load_dwordx4 v[176:179], v2, s[40:43], s64 offen\n"
        "buffer_load_dwordx4 v[180:183], v2, s[40:43], s64 offen offset:2048\n"
        "buffer_load_dwordx4 v[184:187], v2, s[40:43], s65 offen\n"
        "buffer_load_dwordx4 v[188:191], v2, s[40:43], s65 offen offset:2048\n"
        "buffer_load_dwordx4 v[192:195], v2, s[40:43], s66 offen\n"
        "buffer_load_dwordx4 v[196:199], v2, s[40:43], s66 offen offset:2048\n"
        "buffer_load_dwordx4 v[200:203], v3, s[40:43], s60 offen\n"
        "buffer_load_dwordx4 v[204:207], v3, s[40:43], s60 offen offset:2048\n"
        "buffer_load_dwordx4 v[208:211], v3, s[40:43], s61 offen\n"
        "buffer_load_dwordx4 v[212:215], v3, s[40:43], s61 offen offset:2048\n"
        "buffer_load_dwordx4 v[216:219], v3, s[40:43], s62 offen\n"
        "buffer_load_dwordx4 v[220:223], v3, s[40:43], s62 offen offset:2048\n"
        "buffer_load_dwordx4 v[224:227], v3, s[40:43], s63 offen\n"
        "buffer_load_dwordx4 v[228:231], v3, s[40:43], s63 offen offset:2048\n"
        "buffer_load_dwordx4 v[232:235], v3, s[40:43], s64 offen\n"
        "buffer_load_dwordx4 v[236:239], v3, s[40:43], s64 offen offset:2048\n"
        "buffer_load_dwordx4 v[240:243], v3, s[40:43], s65 offen\n"
        "buffer_load_dwordx4 v[244:247], v3, s[40:43], s65 offen offset:2048\n"
        "buffer_load_dwordx4 v[248:251], v3, s[40:43], s66 offen\n"
        "buffer_load_dwordx4 v[252:255], v3, s[40:43], s66 offen offset:2048\n"
        "buffer_load_dwordx4 a[0:3], v4, s[40:43], s60 offen\n"
        "buffer_load_dwordx4 a[4:7], v4, s[40:43], s60 offen offset:2048\n"
        "buffer_load_dwordx4 a[8:11], v4, s[40:43], s64 offen\n"
        "buffer_load_dwordx4 a[12:15], v4, s[40:43], s64 offen offset:2048\n"
        "buffer_load_dwordx4 a[16:19], v4, s[40:43], s62 offen\n"
        "buffer_load_dwordx4 a[20:23], v4, s[40:43], s62 offen offset:2048\n"
        "buffer_load_dwordx4 a[32:35], v5, s[40:43], s60 offen\n"
        "buffer_load_dwordx4 a[36:39], v5, s[40:43], s60 offen offset:2048\n"
        "buffer_load_dwordx4 a[40:43], v5, s[40:43], s61 offen\n"
        "buffer_load_dwordx4 a[44:47], v5, s[40:43], s61 offen offset:2048\n"
        "buffer_load_dwordx4 a[48:51], v5, s[40:43], s62 offen\n"
        "buffer_load_dwordx4 a[52:55], v5, s[40:43], s62 offen offset:2048\n"
        "buffer_load_dwordx4 a[56:59], v5, s[40:43], s63 offen\n"
        "buffer_load_dwordx4 a[60:63], v5, s[40:43], s63 offen offset:2048\n"
        "buffer_load_dwordx4 a[64:67], v5, s[40:43], s64 offen\n"
        "buffer_load_dwordx4 a[68:71], v5, s[40:43], s64 offen offset:2048\n"
        "buffer_load_dwordx4 a[72:75], v5, s[40:43], s65 offen\n"
        "buffer_load_dwordx4 a[76:79], v5, s[40:43], s65 offen offset:2048\n"
        "buffer_load_dwordx4 a[80:83], v5, s[40:43], s66 offen\n"
        "buffer_load_dwordx4 a[84:87], v5, s[40:43], s66 offen offset:2048\n"
        "buffer_load_dwordx4 a[88:91], v6, s[40:43], s60 offen\n"
        "buffer_load_dwordx4 a[92:95], v6, s[40:43], s60 offen offset:2048\n"
        "buffer_load_dwordx4 a[96:99], v6, s[40:43], s61 offen\n"
        "buffer_load_dwordx4 a[100:103], v6, s[40:43], s61 offen offset:2048\n"
        "buffer_load_dwordx4 a[104:107], v6, s[40:43], s62 offen\n"
        "buffer_load_dwordx4 a[108:111], v6, s[40:43], s62 offen offset:2048\n"
        "buffer_load_dwordx4 a[112:115], v6, s[40:43], s63 offen\n"
        "buffer_load_dwordx4 a[116:119], v6, s[40:43], s63 offen offset:2048\n"
        "buffer_load_dwordx4 a[120:123], v6, s[40:43], s64 offen\n"
        "buffer_load_dwordx4 a[124:127], v6, s[40:43], s64 offen offset:2048\n"
        "buffer_load_dwordx4 a[128:131], v6, s[40:43], s65 offen\n"
        "buffer_load_dwordx4 a[132:135], v6, s[40:43], s65 offen offset:2048\n"
        "buffer_load_dwordx4 a[136:139], v6, s[40:43], s66 offen\n"
        "buffer_load_dwordx4 a[140:143], v6, s[40:43], s66 offen offset:2048\n"
        "buffer_load_dwordx4 a[144:147], v7, s[40:43], s60 offen\n"
        "buffer_load_dwordx4 a[148:151], v7, s[40:43], s60 offen offset:2048\n"
        "buffer_load_dwordx4 a[152:155], v7, s[40:43], s61 offen\n"
        "buffer_load_dwordx4 a[156:159], v7, s[40:43], s61 offen offset:2048\n"
        "buffer_load_dwordx4 a[160:163], v7, s[40:43], s62 offen\n"
        "buffer_load_dwordx4 a[164:167], v7, s[40:43], s62 offen offset:2048\n"
        "buffer_load_dwordx4 a[168:171], v7, s[40:43], s63 offen\n"
        "buffer_load_dwordx4 a[172:175], v7, s[40:43], s63 offen offset:2048\n"
        "buffer_load_dwordx4 a[176:179], v7, s[40:43], s64 offen\n"
        "buffer_load_dwordx4 a[180:183], v7, s[40:43], s64 offen offset:2048\n"
        "buffer_load_dwordx4 a[184:187], v7, s[40:43], s65 offen\n"
        "buffer_load_dwordx4 a[188:191], v7, s[40:43], s65 offen offset:2048\n"
        "buffer_load_dwordx4 a[192:195], v7, s[40:43], s66 offen\n"
        "buffer_load_dwordx4 a[196:199], v7, s[40:43], s66 offen offset:2048\n"
        "buffer_load_dwordx4 a[200:203], v8, s[40:43], s60 offen\n"
        "buffer_load_dwordx4 a[204:207], v8, s[40:43], s60 offen offset:2048\n"
        "buffer_load_dwordx4 a[208:211], v8, s[40:43], s61 offen\n"
        "buffer_load_dwordx4 a[212:215], v8, s[40:43], s61 offen offset:2048\n"
        "buffer_load_dwordx4 a[216:219], v8, s[40:43], s62 offen\n"
        "buffer_load_dwordx4 a[220:223], v8, s[40:43], s62 offen offset:2048\n"
        "buffer_load_dwordx4 a[224:227], v8, s[40:43], s63 offen\n"
        "buffer_load_dwordx4 a[228:231], v8, s[40:43], s63 offen offset:2048\n"
        "buffer_load_dwordx4 a[232:235], v8, s[40:43], s64 offen\n"
        "buffer_load_dwordx4 a[236:239], v8, s[40:43], s64 offen offset:2048\n"
        "buffer_load_dwordx4 a[240:243], v8, s[40:43], s65 offen\n"
        "buffer_load_dwordx4 a[244:247], v8, s[40:43], s65 offen offset:2048\n"
        "buffer_load_dwordx4 a[248:251], v8, s[40:43], s66 offen\n"
        "buffer_load_dwordx4 a[252:255], v8, s[40:43], s66 offen offset:2048\n"
        "s_add_u32 s53, s53, 0\n"
        "s_add_u32 s54, s54, 0\n"
        "v_mov_b32 v28, 0\n"
        "v_mov_b32 v29, 0\n"
        "v_mov_b32 v30, 0\n"
        "v_mov_b32 v31, 0\n"
        "v_mov_b32 v32, 0\n"
        "v_mov_b32 v33, 0\n"
        "v_mov_b32 v34, 0\n"
        "v_mov_b32 v35, 0\n"
        "v_mov_b32 v36, 0\n"
        "v_mov_b32 v37, 0\n"
        "v_mov_b32 v38, 0\n"
        "v_mov_b32 v39, 0\n"
        "v_mov_b32 v40, 0\n"
        "v_mov_b32 v41, 0\n"
        "v_mov_b32 v42, 0\n"
        "v_mov_b32 v43, 0\n"
        "v_mov_b32 v44, 0\n"
        "v_mov_b32 v45, 0\n"
        "v_mov_b32 v46, 0\n"
        "v_mov_b32 v47, 0\n"
        "v_mov_b32 v48, 0\n"
        "v_mov_b32 v49, 0\n"
        "v_mov_b32 v50, 0\n"
        "v_mov_b32 v51, 0\n"
        "v_mov_b32 v52, 0\n"
        "v_mov_b32 v53, 0\n"
        "v_mov_b32 v54, 0\n"
        "v_mov_b32 v55, 0\n"
        "v_mov_b32 v56, 0\n"
        "v_mov_b32 v57, 0\n"
        "v_mov_b32 v58, 0\n"
        "v_mov_b32 v59, 0\n"
        "v_mov_b32 v60, 0\n"
        "v_mov_b32 v61, 0\n"
        "v_mov_b32 v62, 0\n"
        "v_mov_b32 v63, 0\n"
        "v_mov_b32 v64, 0\n"
        "v_mov_b32 v65, 0\n"
        "v_mov_b32 v66, 0\n"
        "v_mov_b32 v67, 0\n"
        "v_mov_b32 v68, 0\n"
        "v_mov_b32 v69, 0\n"
        "v_mov_b32 v70, 0\n"
        "v_mov_b32 v71, 0\n"
        "buffer_load_dword v10, v9, s[40:43], 0 offen\n"
        "s_waitcnt vmcnt(0)\n"
        "s_mov_b32 s50, 0\n"
        "s_mov_b32 s66, 0x11111111\n"
        "s_mov_b32 s67, 0x11111111\n"
        "s_branch .Lr0a_poll_%=\n"
        ".Lr0a_loop_%=:\n"
        "s_waitcnt vmcnt(1)\n"
        "v_mov_b32 v28, 0\n"
        "v_mov_b32 v29, 0\n"
        "v_mov_b32 v30, 0\n"
        "v_mov_b32 v31, 0\n"
        "v_mov_b32 v32, 0\n"
        "v_mov_b32 v33, 0\n"
        "v_mov_b32 v34, 0\n"
        "v_mov_b32 v35, 0\n"
        "v_mov_b32 v36, 0\n"
        "v_mov_b32 v37, 0\n"
        "v_mov_b32 v38, 0\n"
        "v_mov_b32 v39, 0\n"
        "v_mov_b32 v40, 0\n"
        "v_mov_b32 v41, 0\n"
        "v_mov_b32 v42, 0\n"
        "v_mov_b32 v43, 0\n"
        "v_mov_b32 v44, 0\n"
        "v_mov_b32 v45, 0\n"
        "v_mov_b32 v46, 0\n"
        "v_mov_b32 v47, 0\n"
        "v_mov_b32 v48, 0\n"
        "v_mov_b32 v49, 0\n"
        "v_mov_b32 v50, 0\n"
        "v_mov_b32 v51, 0\n"
        "v_mov_b32 v52, 0\n"
        "v_mov_b32 v53, 0\n"
        "v_mov_b32 v54, 0\n"
        "v_mov_b32 v55, 0\n"
        "v_mov_b32 v56, 0\n"
        "v_mov_b32 v57, 0\n"
        "v_mov_b32 v58, 0\n"
        "v_mov_b32 v59, 0\n"
        "v_mov_b32 v85, v85\n"
        "v_mov_b32 v85, v85\n"
        "v_mov_b32 v85, v85\n"
        "v_mov_b32 v85, v85\n"
        "v_mov_b32 v85, v85\n"
        "v_mov_b32 v85, v85\n"
        "v_mov_b32 v85, v85\n"
        "v_mov_b32 v85, v85\n"
        "v_mov_b32 v85, v85\n"
        "v_mov_b32 v85, v85\n"
        "v_mov_b32 v85, v85\n"
        "v_mov_b32 v85, v85\n"
        "v_mov_b32 v85, v85\n"
        "v_mov_b32 v85, v85\n"
        "v_mov_b32 v85, v85\n"
        "v_mov_b32 v85, v85\n"
        "v_mov_b32 v85, v85\n"
        "v_mov_b32 v85, v85\n"
        "v_mov_b32 v85, v85\n"
        "v_mov_b32 v85, v85\n"
        "v_mov_b32 v85, v85\n"
        "v_mov_b32 v85, v85\n"
        "v_mov_b32 v85, v85\n"
        "v_mov_b32 v85, v85\n"
        "v_mov_b32 v85, v85\n"
        "v_mov_b32 v85, v85\n"
        "v_mov_b32 v85, v85\n"
        "v_mov_b32 v85, v85\n"
        "v_mov_b32 v85, v85\n"
        "v_mov_b32 v85, v85\n"
        "v_mov_b32 v85, v85\n"
        "v_mov_b32 v85, v85\n"
        "v_mov_b32 v85, v85\n"
        "v_mov_b32 v85, v85\n"
        "v_mov_b32 v85, v85\n"
        "v_mov_b32 v85, v85\n"
        "v_mov_b32 v85, v85\n"
        "v_mov_b32 v85, v85\n"
        "v_mov_b32 v85, v85\n"
        "v_mov_b32 v85, v85\n"
        "v_mov_b32 v85, v85\n"
        "v_mov_b32 v85, v85\n"
        "v_mov_b32 v85, v85\n"
        ".Lr0a_poll_%=:\n"
        "buffer_load_dwordx4 v[0:3], v72, s[40:43], s47 offen sc1\n"
        "buffer_load_dwordx4 v[16:19], v72, s[40:43], s49 offen sc1\n"
        "s_waitcnt vmcnt(0)\n"
        ".Lr0a_mwait_%=:\n"
        "v_max3_u32 v60, v0, v1, v2\n"
        "v_max3_u32 v61, v3, v16, v17\n"
        "v_max3_u32 v60, v60, v18, v19\n"
        "ds_write_b128 v72, v[0:3] offset:4096\n"
        "ds_write_b128 v72, v[16:19] offset:8192\n"
        "v_smfmac_f32_16x16x64_bf16 v[28:31], v[0:3], v[88:95], v75\n"
        "v_max_u32 v60, v60, v61\n"
        "v_cmp_eq_u32 vcc, -1, v60\n"
        "v_smfmac_f32_16x16x64_bf16 v[32:35], v[0:3], v[144:151], v75\n"
        "s_cbranch_vccnz .Lr0a_rm_%=\n"
        ".Lr0a_mok_%=:\n"
        "v_smfmac_f32_16x16x64_bf16 v[36:39], v[0:3], v[200:207], v75\n"
        "v_smfmac_f32_16x16x64_bf16 v[40:43], v[0:3], a[0:7], v75\n"
        "s_waitcnt lgkmcnt(0)\n"
        "s_barrier\n"
        "ds_read_b128 v[8:11], v72 offset:6144\n"
        "ds_read_b128 v[4:7], v72 offset:5120\n"
        "ds_read_b128 v[12:15], v72 offset:7168\n"
        "ds_read_b128 v[20:23], v72 offset:9216\n"
        "ds_read_b128 v[24:27], v72 offset:10240\n"
        "v_smfmac_f32_16x16x64_bf16 v[28:31], v[16:19], v[120:127], v75\n"
        "buffer_store_dwordx4 v[64:67], v72, s[40:43], s53 offen sc1\n"
        "v_smfmac_f32_16x16x64_bf16 v[32:35], v[16:19], v[176:183], v75\n"
        "buffer_store_dwordx4 v[68:71], v72, s[40:43], s53 offen offset:1024 sc1\n"
        "v_smfmac_f32_16x16x64_bf16 v[36:39], v[16:19], v[232:239], v75\n"
        "v_smfmac_f32_16x16x64_bf16 v[40:43], v[16:19], a[8:15], v75\n"
        "ds_read_b128 v[64:67], v84\n"
        "s_waitcnt lgkmcnt(5)\n"
        "v_smfmac_f32_16x16x64_bf16 v[40:43], v[8:11], a[16:23], v75\n"
        "s_mov_b32 s46, s47\n"
        "s_add_u32 s51, s45, s56\n"
        "s_add_u32 s57, s44, 1\n"
        "v_smfmac_f32_16x16x64_bf16 v[28:31], v[8:11], v[104:111], v75\n"
        "v_readlane_b32 s45, v85, s57\n"
        "s_lshl_b32 s58, s44, 18\n"
        "s_add_u32 s53, s54, s58\n"
        "v_smfmac_f32_16x16x64_bf16 v[32:35], v[8:11], v[160:167], v75\n"
        "s_xor_b32 s47, s45, 0x84000\n"
        "s_andn2_b32 s45, s45, 0x1000\n"
        "s_add_u32 s49, s47, 32768\n"
        "v_smfmac_f32_16x16x64_bf16 v[36:39], v[8:11], v[216:223], v75\n"
        "s_add_u32 s52, s46, 65536\n"
        "s_waitcnt lgkmcnt(4)\n"
        "v_smfmac_f32_16x16x64_bf16 v[28:31], v[4:7], v[96:103], v75\n"
        "s_waitcnt lgkmcnt(3)\n"
        "v_smfmac_f32_16x16x64_bf16 v[28:31], v[12:15], v[112:119], v75\n"
        "v_pk_add_f32 v[40:41], v[40:41], v[42:43]\n"
        "s_waitcnt lgkmcnt(2)\n"
        "v_smfmac_f32_16x16x64_bf16 v[28:31], v[20:23], v[128:135], v75\n"
        "v_cvt_pk_bf16_f32 v40, v40, v41\n"
        "s_waitcnt lgkmcnt(1)\n"
        "v_smfmac_f32_16x16x64_bf16 v[28:31], v[24:27], v[136:143], v75\n"
        "ds_write_b32 v74, v40 offset:3072\n"
        "v_smfmac_f32_16x16x64_bf16 v[32:35], v[4:7], v[152:159], v75\n"
        "v_smfmac_f32_16x16x64_bf16 v[32:35], v[12:15], v[168:175], v75\n"
        "v_pk_add_f32 v[28:29], v[28:29], v[30:31]\n"
        "v_smfmac_f32_16x16x64_bf16 v[32:35], v[20:23], v[184:191], v75\n"
        "v_cvt_pk_bf16_f32 v28, v28, v29\n"
        "v_smfmac_f32_16x16x64_bf16 v[32:35], v[24:27], v[192:199], v75\n"
        "ds_write_b32 v74, v28 offset:0\n"
        "v_smfmac_f32_16x16x64_bf16 v[36:39], v[4:7], v[208:215], v75\n"
        "v_smfmac_f32_16x16x64_bf16 v[36:39], v[12:15], v[224:231], v75\n"
        "v_pk_add_f32 v[32:33], v[32:33], v[34:35]\n"
        "v_smfmac_f32_16x16x64_bf16 v[36:39], v[20:23], v[240:247], v75\n"
        "v_cvt_pk_bf16_f32 v32, v32, v33\n"
        "v_smfmac_f32_16x16x64_bf16 v[36:39], v[24:27], v[248:255], v75\n"
        "ds_write_b32 v74, v32 offset:1024\n"
        "v_smfmac_f32_16x16x64_bf16 v[44:47], v[0:3], a[32:39], v75\n"
        "s_nop 3\n"
        "v_pk_add_f32 v[36:37], v[36:37], v[38:39]\n"
        "v_cvt_pk_bf16_f32 v36, v36, v37\n"
        "ds_write_b32 v74, v36 offset:2048\n"
        "v_smfmac_f32_16x16x64_bf16 v[44:47], v[4:7], a[40:47], v75\n"
        "v_smfmac_f32_16x16x64_bf16 v[44:47], v[8:11], a[48:55], v75\n"
        "s_waitcnt vmcnt(2) lgkmcnt(0)\n"
        "s_barrier\n"
        "ds_read_b64_tr_b16 v[68:69], v73 offset:0\n"
        "ds_read_b64_tr_b16 v[70:71], v73 offset:512\n"
        "ds_read_b64_tr_b16 v[28:29], v73 offset:1024\n"
        "ds_read_b64_tr_b16 v[30:31], v73 offset:1536\n"
        "ds_read_b64_tr_b16 v[32:33], v73 offset:2048\n"
        "ds_read_b64_tr_b16 v[34:35], v73 offset:2560\n"
        "ds_read_b64_tr_b16 v[36:37], v73 offset:3072\n"
        "ds_read_b64_tr_b16 v[38:39], v73 offset:3584\n"
        "v_smfmac_f32_16x16x64_bf16 v[44:47], v[12:15], a[56:63], v75\n"
        "s_waitcnt lgkmcnt(6)\n"
        "v_mfma_f32_16x16x32_bf16 v[60:63], v[0:3], v[68:71], 0\n"
        "s_waitcnt lgkmcnt(4)\n"
        "v_mfma_f32_16x16x32_bf16 v[60:63], v[4:7], v[28:31], v[60:63]\n"
        "s_waitcnt lgkmcnt(2)\n"
        "v_mfma_f32_16x16x32_bf16 v[60:63], v[8:11], v[32:35], v[60:63]\n"
        "s_waitcnt lgkmcnt(0)\n"
        "v_mfma_f32_16x16x32_bf16 v[60:63], v[64:67], v[36:39], v[60:63]\n"
        "v_smfmac_f32_16x16x64_bf16 v[44:47], v[16:19], a[64:71], v75\n"
        "s_nop 3\n"
        "v_add_f32_dpp v40, v62, v60 row_ror:8 row_mask:0xf bank_mask:0x3\n"
        "s_nop 1\n"
        "v_add_f32_dpp v40, v61, v63 row_ror:8 row_mask:0xf bank_mask:0xc\n"
        "buffer_store_dword v40, v81, s[40:43], s52 offen sc1\n"
        "v_smfmac_f32_16x16x64_bf16 v[44:47], v[20:23], a[72:79], v75\n"
        "v_mov_b32 v60, 0\n"
        "v_smfmac_f32_16x16x64_bf16 v[44:47], v[24:27], a[80:87], v75\n"
        "v_mov_b32 v61, 0\n"
        "v_smfmac_f32_16x16x64_bf16 v[48:51], v[0:3], a[88:95], v75\n"
        "v_mov_b32 v62, 0\n"
        "v_smfmac_f32_16x16x64_bf16 v[48:51], v[4:7], a[96:103], v75\n"
        "v_mov_b32 v63, 0\n"
        "v_smfmac_f32_16x16x64_bf16 v[48:51], v[8:11], a[104:111], v75\n"
        "v_pk_add_f32 v[44:45], v[44:45], v[46:47]\n"
        "v_smfmac_f32_16x16x64_bf16 v[48:51], v[12:15], a[112:119], v75\n"
        "v_cvt_pk_bf16_f32 v64, v44, v45\n"
        "v_smfmac_f32_16x16x64_bf16 v[48:51], v[16:19], a[120:127], v75\n"
        "ds_write_b32 v78, v64 offset:0\n"
        "v_smfmac_f32_16x16x64_bf16 v[48:51], v[20:23], a[128:135], v75\n"
        "v_smfmac_f32_16x16x64_bf16 v[48:51], v[24:27], a[136:143], v75\n"
        "v_smfmac_f32_16x16x64_bf16 v[52:55], v[0:3], a[144:151], v75\n"
        "v_smfmac_f32_16x16x64_bf16 v[52:55], v[4:7], a[152:159], v75\n"
        "v_smfmac_f32_16x16x64_bf16 v[52:55], v[8:11], a[160:167], v75\n"
        "v_pk_add_f32 v[48:49], v[48:49], v[50:51]\n"
        "v_smfmac_f32_16x16x64_bf16 v[52:55], v[12:15], a[168:175], v75\n"
        "v_cvt_pk_bf16_f32 v65, v48, v49\n"
        "v_smfmac_f32_16x16x64_bf16 v[52:55], v[16:19], a[176:183], v75\n"
        "ds_write_b32 v78, v65 offset:256\n"
        "v_smfmac_f32_16x16x64_bf16 v[52:55], v[20:23], a[184:191], v75\n"
        "v_smfmac_f32_16x16x64_bf16 v[52:55], v[24:27], a[192:199], v75\n"
        "buffer_load_dwordx2 v[40:41], v77, s[40:43], s46 offen sc1\n"
        "buffer_load_dwordx4 v[28:31], v76, s[40:43], s46 offen sc1\n"
        "buffer_load_dwordx4 v[32:35], v76, s[40:43], s46 offen offset:256 sc1\n"
        "v_smfmac_f32_16x16x64_bf16 v[56:59], v[0:3], a[200:207], v75\n"
        "v_smfmac_f32_16x16x64_bf16 v[56:59], v[4:7], a[208:215], v75\n"
        "v_smfmac_f32_16x16x64_bf16 v[56:59], v[8:11], a[216:223], v75\n"
        "v_pk_add_f32 v[52:53], v[52:53], v[54:55]\n"
        "v_smfmac_f32_16x16x64_bf16 v[56:59], v[12:15], a[224:231], v75\n"
        "v_cvt_pk_bf16_f32 v66, v52, v53\n"
        "v_smfmac_f32_16x16x64_bf16 v[56:59], v[16:19], a[232:239], v75\n"
        "ds_write_b32 v78, v66 offset:512\n"
        "v_smfmac_f32_16x16x64_bf16 v[56:59], v[20:23], a[240:247], v75\n"
        "v_smfmac_f32_16x16x64_bf16 v[56:59], v[24:27], a[248:255], v75\n"
        "s_nop 7\n"
        "v_pk_add_f32 v[56:57], v[56:57], v[58:59]\n"
        "v_cvt_pk_bf16_f32 v67, v56, v57\n"
        "ds_write_b32 v78, v67 offset:768\n"
        "s_waitcnt lgkmcnt(0)\n"
        "ds_read_b128 v[8:11], v72 offset:11264\n"
        "s_cmp_eq_u32 s44, 63\n"
        "s_cbranch_scc1 .Lr0a_exit_%=\n"
        "s_add_u32 s44, s44, 1\n"
        "s_mov_b32 s57, 0\n"
        "s_mov_b32 s58, 0\n"
        "buffer_load_dwordx4 v[36:39], v72, s[40:43], s51 offen sc1\n"
        ".Lr0a_swait_%=:\n"
        "s_waitcnt vmcnt(1) lgkmcnt(0)\n"
        ".Lr0a_schain_%=:\n"
        "v_pk_add_f32 v[0:1], v[28:29], v[32:33]\n"
        "v_pk_add_f32 v[2:3], v[30:31], v[34:35]\n"
        "v_mov_b32 v13, v40\n"
        "v_max3_f32 v4, v0, v1, v13\n"
        "v_cmp_u_f32 vcc, v0, v13\n"
        "v_max3_f32 v4, v2, v3, v4\n"
        "s_cbranch_vccnz .Lr0a_sbad_%=\n"
        ".Lr0a_sgo_%=:\n"
        "s_nop 0\n"
        "v_max_f32_dpp v4, v4, v4 quad_perm:[2,3,0,1] row_mask:0xf bank_mask:0xf\n"
        "v_sub_f32 v12, v13, v4\n"
        "v_pk_add_f32 v[0:1], v[0:1], v[4:5] op_sel_hi:[1,0] neg_lo:[0,1] neg_hi:[0,1]\n"
        "v_exp_f32 v12, v12\n"
        "v_pk_add_f32 v[2:3], v[2:3], v[4:5] op_sel_hi:[1,0] neg_lo:[0,1] neg_hi:[0,1]\n"
        "v_exp_f32 v0, v0\n"
        "v_exp_f32 v1, v1\n"
        "v_exp_f32 v2, v2\n"
        "v_exp_f32 v3, v3\n"
        "v_fma_f32 v6, v12, v41, v0\n"
        "v_add_f32 v6, v6, v1\n"
        "v_add_f32 v7, v2, v3\n"
        "v_add_f32 v6, v6, v7\n"
        "s_nop 1\n"
        "v_add_f32_dpp v6, v6, v6 quad_perm:[2,3,0,1] row_mask:0xf bank_mask:0xf\n"
        "v_rcp_f32 v6, v6\n"
        "s_nop 0\n"
        "v_pk_mul_f32 v[0:1], v[0:1], v[6:7] op_sel_hi:[1,0]\n"
        "v_pk_mul_f32 v[2:3], v[2:3], v[6:7] op_sel_hi:[1,0]\n"
        "v_cvt_pk_bf16_f32 v0, v0, v1\n"
        "v_cvt_pk_bf16_f32 v1, v2, v3\n"
        "v_mul_f32 v12, v12, v6\n"
        "v_cvt_pk_bf16_f32 v12, v12, v12\n"
        ".Lr0a_redo_%=:\n"
        "v_smfmac_f32_16x16x32_bf16 v[60:63], v[0:1], v[8:11], v75\n"
        "v_and_b32 v2, v12, v79\n"
        "v_and_b32 v3, v12, v80\n"
        "s_waitcnt vmcnt(0)\n"
        "s_nop 0\n"
        "v_smfmac_f32_16x16x32_bf16 v[60:63], v[2:3], v[36:39], v75\n"
        "s_nop 7\n"
        "v_pk_add_f32 v[4:5], v[60:61], v[62:63]\n"
        "ds_write2_b32 v82, v4, v5 offset0:0 offset1:32\n"
        "v_cmp_u_f32 vcc, v4, v5\n"
        "s_cbranch_vccnz .Lr0a_obad_%=\n"
        ".Lr0a_sok_%=:\n"
        "s_waitcnt lgkmcnt(0)\n"
        "s_barrier\n"
        "ds_read_b128 v[0:3], v83\n"
        "ds_read_b128 v[4:7], v83 offset:16\n"
        "s_waitcnt lgkmcnt(1)\n"
        "v_pk_add_f32 v[0:1], v[0:1], v[2:3]\n"
        "s_waitcnt lgkmcnt(0)\n"
        "v_pk_add_f32 v[4:5], v[4:5], v[6:7]\n"
        "v_pk_add_f32 v[0:1], v[0:1], v[4:5]\n"
        "v_cvt_pk_bf16_f32 v0, v0, v1\n"
        "buffer_store_dword v0, v81, s[40:43], s47 offen sc1\n"
        "s_branch .Lr0a_loop_%=\n"
        ".Lr0a_rm_%=:\n"
        "s_add_u32 s59, s59, 1\n"
        "s_add_u32 s57, s57, 1\n"
        "s_cmp_eq_u32 s55, 0\n"
        "s_cbranch_scc1 .Lr0a_mok_%=\n"
        "s_sub_u32 s55, s55, 1\n"
        "s_waitcnt lgkmcnt(0)\n"
        "buffer_load_dwordx4 v[0:3], v72, s[40:43], s47 offen sc1\n"
        "buffer_load_dwordx4 v[16:19], v72, s[40:43], s49 offen sc1\n"
        "s_nop 7\n"
        "v_mov_b32 v28, 0\n"
        "v_mov_b32 v29, 0\n"
        "v_mov_b32 v30, 0\n"
        "v_mov_b32 v31, 0\n"
        "v_mov_b32 v32, 0\n"
        "v_mov_b32 v33, 0\n"
        "v_mov_b32 v34, 0\n"
        "v_mov_b32 v35, 0\n"
        "s_waitcnt vmcnt(0)\n"
        "s_branch .Lr0a_mwait_%=\n"
        ".Lr0a_sbad_%=:\n"
        "s_add_u32 s57, s57, 1\n"
        "s_cmp_eq_u32 s55, 0\n"
        "s_cbranch_scc1 .Lr0a_sgo_%=\n"
        ".Lr0a_sslow_%=:\n"
        "s_sub_u32 s55, s55, 1\n"
        "buffer_load_dwordx4 v[28:31], v76, s[40:43], s46 offen sc1\n"
        "buffer_load_dwordx4 v[32:35], v76, s[40:43], s46 offen offset:256 sc1\n"
        "buffer_load_dwordx2 v[40:41], v77, s[40:43], s46 offen sc1\n"
        "s_waitcnt vmcnt(0)\n"
        "s_branch .Lr0a_schain_%=\n"
        ".Lr0a_obad_%=:\n"
        "s_cmp_eq_u32 s55, 0\n"
        "s_cbranch_scc1 .Lr0a_sok_%=\n"
        "v_cmp_u_f32 vcc, v6, v6\n"
        "s_add_u32 s58, s58, 1\n"
        "v_mov_b32 v60, 0\n"
        "v_mov_b32 v61, 0\n"
        "v_mov_b32 v62, 0\n"
        "v_mov_b32 v63, 0\n"
        "s_cbranch_vccnz .Lr0a_sslow_%=\n"
        "s_sub_u32 s55, s55, 1\n"
        "buffer_load_dwordx4 v[36:39], v72, s[40:43], s51 offen sc1\n"
        "s_waitcnt vmcnt(0)\n"
        "s_nop 0\n"
        "s_branch .Lr0a_redo_%=\n"
        ".Lr0a_exit_%=:\n"
        "s_nop 7\n"
        "buffer_store_dwordx4 v[64:67], v72, s[40:43], s53 offen sc1\n"
        "buffer_store_dwordx4 v[68:71], v72, s[40:43], s53 offen offset:1024 sc1\n"
        "s_waitcnt vmcnt(0) lgkmcnt(0)\n"
        "s_barrier\n"
        "s_branch .Lrend_%=\n"
        ".Lr1_entry_%=:\n"
        "v_mbcnt_lo_u32_b32 v0, -1, 0\n"
        "v_mbcnt_hi_u32_b32 v0, -1, v0\n"
        "v_lshlrev_b32 v0, 2, v0\n"
        "ds_read_b32 v72, v0 offset:17920\n"
        "ds_read_b32 v73, v0 offset:18944\n"
        "ds_read_b32 v74, v0 offset:19968\n"
        "ds_read_b32 v75, v0 offset:20992\n"
        "ds_read_b32 v76, v0 offset:22016\n"
        "ds_read_b32 v77, v0 offset:23040\n"
        "ds_read_b32 v78, v0 offset:24064\n"
        "ds_read_b32 v79, v0 offset:25088\n"
        "ds_read_b32 v80, v0 offset:26112\n"
        "ds_read_b32 v81, v0 offset:27136\n"
        "ds_read_b32 v82, v0 offset:28160\n"
        "ds_read_b32 v83, v0 offset:29184\n"
        "ds_read_b32 v84, v0 offset:30208\n"
        "ds_read_b32 v85, v0 offset:31232\n"
        "s_waitcnt lgkmcnt(0)\n"
        "ds_read_b32 v1, v0 offset:32256\n"
        "ds_read_b32 v2, v0 offset:33280\n"
        "ds_read_b32 v3, v0 offset:34304\n"
        "ds_read_b32 v4, v0 offset:35328\n"
        "ds_read_b32 v5, v0 offset:36352\n"
        "ds_read_b32 v6, v0 offset:37376\n"
        "ds_read_b32 v7, v0 offset:38400\n"
        "ds_read_b32 v8, v0 offset:39424\n"
        "ds_read_b32 v9, v0 offset:40448\n"
        "s_waitcnt lgkmcnt(0)\n"
        "buffer_load_dword v10, v9, s[40:43], 0 offen\n"
        "v_readlane_b32 s45, v85, 0\n"
        "s_xor_b32 s47, s45, 0x84000\n"
        "s_andn2_b32 s45, s45, 0x1000\n"
        "s_add_u32 s49, s47, 32768\n"
        "s_getpc_b64 s[58:59]\n"
        "v_lshlrev_b32 v12, 5, v0\n"
        "v_min_u32 v12, .Lr1_exit_%=-.Lr1_loop_%=-64, v12\n"
        "v_add_u32 v12, .Lr1_loop_%=-.Lr1_entry_%=, v12\n"
        "global_load_dword v12, v12, s[58:59]\n"
        "s_mov_b32 s59, 0\n"
        "s_mov_b32 s60, 0\n"
        "s_mov_b32 s61, 4096\n"
        "s_mov_b32 s62, 8192\n"
        "s_mov_b32 s63, 12288\n"
        "s_mov_b32 s64, 16384\n"
        "s_mov_b32 s65, 20480\n"
        "s_mov_b32 s66, 24576\n"
        "buffer_load_dwordx4 v[88:91], v1, s[40:43], s60 offen\n"
        "buffer_load_dwordx4 v[92:95], v1, s[40:43], s60 offen offset:2048\n"
        "buffer_load_dwordx4 v[96:99], v1, s[40:43], s61 offen\n"
        "buffer_load_dwordx4 v[100:103], v1, s[40:43], s61 offen offset:2048\n"
        "buffer_load_dwordx4 v[104:107], v1, s[40:43], s62 offen\n"
        "buffer_load_dwordx4 v[108:111], v1, s[40:43], s62 offen offset:2048\n"
        "buffer_load_dwordx4 v[112:115], v1, s[40:43], s63 offen\n"
        "buffer_load_dwordx4 v[116:119], v1, s[40:43], s63 offen offset:2048\n"
        "buffer_load_dwordx4 v[120:123], v1, s[40:43], s64 offen\n"
        "buffer_load_dwordx4 v[124:127], v1, s[40:43], s64 offen offset:2048\n"
        "buffer_load_dwordx4 v[128:131], v1, s[40:43], s65 offen\n"
        "buffer_load_dwordx4 v[132:135], v1, s[40:43], s65 offen offset:2048\n"
        "buffer_load_dwordx4 v[136:139], v1, s[40:43], s66 offen\n"
        "buffer_load_dwordx4 v[140:143], v1, s[40:43], s66 offen offset:2048\n"
        "buffer_load_dwordx4 v[144:147], v2, s[40:43], s60 offen\n"
        "buffer_load_dwordx4 v[148:151], v2, s[40:43], s60 offen offset:2048\n"
        "buffer_load_dwordx4 v[152:155], v2, s[40:43], s61 offen\n"
        "buffer_load_dwordx4 v[156:159], v2, s[40:43], s61 offen offset:2048\n"
        "buffer_load_dwordx4 v[160:163], v2, s[40:43], s62 offen\n"
        "buffer_load_dwordx4 v[164:167], v2, s[40:43], s62 offen offset:2048\n"
        "buffer_load_dwordx4 v[168:171], v2, s[40:43], s63 offen\n"
        "buffer_load_dwordx4 v[172:175], v2, s[40:43], s63 offen offset:2048\n"
        "buffer_load_dwordx4 v[176:179], v2, s[40:43], s64 offen\n"
        "buffer_load_dwordx4 v[180:183], v2, s[40:43], s64 offen offset:2048\n"
        "buffer_load_dwordx4 v[184:187], v2, s[40:43], s65 offen\n"
        "buffer_load_dwordx4 v[188:191], v2, s[40:43], s65 offen offset:2048\n"
        "buffer_load_dwordx4 v[192:195], v2, s[40:43], s66 offen\n"
        "buffer_load_dwordx4 v[196:199], v2, s[40:43], s66 offen offset:2048\n"
        "buffer_load_dwordx4 v[200:203], v3, s[40:43], s60 offen\n"
        "buffer_load_dwordx4 v[204:207], v3, s[40:43], s60 offen offset:2048\n"
        "buffer_load_dwordx4 v[208:211], v3, s[40:43], s61 offen\n"
        "buffer_load_dwordx4 v[212:215], v3, s[40:43], s61 offen offset:2048\n"
        "buffer_load_dwordx4 v[216:219], v3, s[40:43], s62 offen\n"
        "buffer_load_dwordx4 v[220:223], v3, s[40:43], s62 offen offset:2048\n"
        "buffer_load_dwordx4 v[224:227], v3, s[40:43], s63 offen\n"
        "buffer_load_dwordx4 v[228:231], v3, s[40:43], s63 offen offset:2048\n"
        "buffer_load_dwordx4 v[232:235], v3, s[40:43], s64 offen\n"
        "buffer_load_dwordx4 v[236:239], v3, s[40:43], s64 offen offset:2048\n"
        "buffer_load_dwordx4 v[240:243], v3, s[40:43], s65 offen\n"
        "buffer_load_dwordx4 v[244:247], v3, s[40:43], s65 offen offset:2048\n"
        "buffer_load_dwordx4 v[248:251], v3, s[40:43], s66 offen\n"
        "buffer_load_dwordx4 v[252:255], v3, s[40:43], s66 offen offset:2048\n"
        "buffer_load_dwordx4 a[0:3], v4, s[40:43], s61 offen\n"
        "buffer_load_dwordx4 a[4:7], v4, s[40:43], s61 offen offset:2048\n"
        "buffer_load_dwordx4 a[8:11], v4, s[40:43], s65 offen\n"
        "buffer_load_dwordx4 a[12:15], v4, s[40:43], s65 offen offset:2048\n"
        "buffer_load_dwordx4 a[16:19], v4, s[40:43], s63 offen\n"
        "buffer_load_dwordx4 a[20:23], v4, s[40:43], s63 offen offset:2048\n"
        "buffer_load_dwordx4 a[24:27], v4, s[40:43], s66 offen\n"
        "buffer_load_dwordx4 a[28:31], v4, s[40:43], s66 offen offset:2048\n"
        "buffer_load_dwordx4 a[32:35], v5, s[40:43], s60 offen\n"
        "buffer_load_dwordx4 a[36:39], v5, s[40:43], s60 offen offset:2048\n"
        "buffer_load_dwordx4 a[40:43], v5, s[40:43], s61 offen\n"
        "buffer_load_dwordx4 a[44:47], v5, s[40:43], s61 offen offset:2048\n"
        "buffer_load_dwordx4 a[48:51], v5, s[40:43], s62 offen\n"
        "buffer_load_dwordx4 a[52:55], v5, s[40:43], s62 offen offset:2048\n"
        "buffer_load_dwordx4 a[56:59], v5, s[40:43], s63 offen\n"
        "buffer_load_dwordx4 a[60:63], v5, s[40:43], s63 offen offset:2048\n"
        "buffer_load_dwordx4 a[64:67], v5, s[40:43], s64 offen\n"
        "buffer_load_dwordx4 a[68:71], v5, s[40:43], s64 offen offset:2048\n"
        "buffer_load_dwordx4 a[72:75], v5, s[40:43], s65 offen\n"
        "buffer_load_dwordx4 a[76:79], v5, s[40:43], s65 offen offset:2048\n"
        "buffer_load_dwordx4 a[80:83], v5, s[40:43], s66 offen\n"
        "buffer_load_dwordx4 a[84:87], v5, s[40:43], s66 offen offset:2048\n"
        "buffer_load_dwordx4 a[88:91], v6, s[40:43], s60 offen\n"
        "buffer_load_dwordx4 a[92:95], v6, s[40:43], s60 offen offset:2048\n"
        "buffer_load_dwordx4 a[96:99], v6, s[40:43], s61 offen\n"
        "buffer_load_dwordx4 a[100:103], v6, s[40:43], s61 offen offset:2048\n"
        "buffer_load_dwordx4 a[104:107], v6, s[40:43], s62 offen\n"
        "buffer_load_dwordx4 a[108:111], v6, s[40:43], s62 offen offset:2048\n"
        "buffer_load_dwordx4 a[112:115], v6, s[40:43], s63 offen\n"
        "buffer_load_dwordx4 a[116:119], v6, s[40:43], s63 offen offset:2048\n"
        "buffer_load_dwordx4 a[120:123], v6, s[40:43], s64 offen\n"
        "buffer_load_dwordx4 a[124:127], v6, s[40:43], s64 offen offset:2048\n"
        "buffer_load_dwordx4 a[128:131], v6, s[40:43], s65 offen\n"
        "buffer_load_dwordx4 a[132:135], v6, s[40:43], s65 offen offset:2048\n"
        "buffer_load_dwordx4 a[136:139], v6, s[40:43], s66 offen\n"
        "buffer_load_dwordx4 a[140:143], v6, s[40:43], s66 offen offset:2048\n"
        "buffer_load_dwordx4 a[144:147], v7, s[40:43], s60 offen\n"
        "buffer_load_dwordx4 a[148:151], v7, s[40:43], s60 offen offset:2048\n"
        "buffer_load_dwordx4 a[152:155], v7, s[40:43], s61 offen\n"
        "buffer_load_dwordx4 a[156:159], v7, s[40:43], s61 offen offset:2048\n"
        "buffer_load_dwordx4 a[160:163], v7, s[40:43], s62 offen\n"
        "buffer_load_dwordx4 a[164:167], v7, s[40:43], s62 offen offset:2048\n"
        "buffer_load_dwordx4 a[168:171], v7, s[40:43], s63 offen\n"
        "buffer_load_dwordx4 a[172:175], v7, s[40:43], s63 offen offset:2048\n"
        "buffer_load_dwordx4 a[176:179], v7, s[40:43], s64 offen\n"
        "buffer_load_dwordx4 a[180:183], v7, s[40:43], s64 offen offset:2048\n"
        "buffer_load_dwordx4 a[184:187], v7, s[40:43], s65 offen\n"
        "buffer_load_dwordx4 a[188:191], v7, s[40:43], s65 offen offset:2048\n"
        "buffer_load_dwordx4 a[192:195], v7, s[40:43], s66 offen\n"
        "buffer_load_dwordx4 a[196:199], v7, s[40:43], s66 offen offset:2048\n"
        "buffer_load_dwordx4 a[200:203], v8, s[40:43], s60 offen\n"
        "buffer_load_dwordx4 a[204:207], v8, s[40:43], s60 offen offset:2048\n"
        "buffer_load_dwordx4 a[208:211], v8, s[40:43], s61 offen\n"
        "buffer_load_dwordx4 a[212:215], v8, s[40:43], s61 offen offset:2048\n"
        "buffer_load_dwordx4 a[216:219], v8, s[40:43], s62 offen\n"
        "buffer_load_dwordx4 a[220:223], v8, s[40:43], s62 offen offset:2048\n"
        "buffer_load_dwordx4 a[224:227], v8, s[40:43], s63 offen\n"
        "buffer_load_dwordx4 a[228:231], v8, s[40:43], s63 offen offset:2048\n"
        "buffer_load_dwordx4 a[232:235], v8, s[40:43], s64 offen\n"
        "buffer_load_dwordx4 a[236:239], v8, s[40:43], s64 offen offset:2048\n"
        "buffer_load_dwordx4 a[240:243], v8, s[40:43], s65 offen\n"
        "buffer_load_dwordx4 a[244:247], v8, s[40:43], s65 offen offset:2048\n"
        "buffer_load_dwordx4 a[248:251], v8, s[40:43], s66 offen\n"
        "buffer_load_dwordx4 a[252:255], v8, s[40:43], s66 offen offset:2048\n"
        "s_add_u32 s53, s53, 1024\n"
        "s_add_u32 s54, s54, 1024\n"
        "v_mov_b32 v28, 0\n"
        "v_mov_b32 v29, 0\n"
        "v_mov_b32 v30, 0\n"
        "v_mov_b32 v31, 0\n"
        "v_mov_b32 v32, 0\n"
        "v_mov_b32 v33, 0\n"
        "v_mov_b32 v34, 0\n"
        "v_mov_b32 v35, 0\n"
        "v_mov_b32 v36, 0\n"
        "v_mov_b32 v37, 0\n"
        "v_mov_b32 v38, 0\n"
        "v_mov_b32 v39, 0\n"
        "v_mov_b32 v40, 0\n"
        "v_mov_b32 v41, 0\n"
        "v_mov_b32 v42, 0\n"
        "v_mov_b32 v43, 0\n"
        "v_mov_b32 v44, 0\n"
        "v_mov_b32 v45, 0\n"
        "v_mov_b32 v46, 0\n"
        "v_mov_b32 v47, 0\n"
        "v_mov_b32 v48, 0\n"
        "v_mov_b32 v49, 0\n"
        "v_mov_b32 v50, 0\n"
        "v_mov_b32 v51, 0\n"
        "v_mov_b32 v52, 0\n"
        "v_mov_b32 v53, 0\n"
        "v_mov_b32 v54, 0\n"
        "v_mov_b32 v55, 0\n"
        "v_mov_b32 v56, 0\n"
        "v_mov_b32 v57, 0\n"
        "v_mov_b32 v58, 0\n"
        "v_mov_b32 v59, 0\n"
        "v_mov_b32 v60, 0\n"
        "v_mov_b32 v61, 0\n"
        "v_mov_b32 v62, 0\n"
        "v_mov_b32 v63, 0\n"
        "v_mov_b32 v64, 0\n"
        "v_mov_b32 v65, 0\n"
        "v_mov_b32 v66, 0\n"
        "v_mov_b32 v67, 0\n"
        "v_mov_b32 v68, 0\n"
        "v_mov_b32 v69, 0\n"
        "v_mov_b32 v70, 0\n"
        "v_mov_b32 v71, 0\n"
        "buffer_load_dword v10, v9, s[40:43], 0 offen\n"
        "s_waitcnt vmcnt(0)\n"
        "s_mov_b32 s50, 0\n"
        "s_mov_b32 s66, 0x11111111\n"
        "s_mov_b32 s67, 0x11111111\n"
        "s_branch .Lr1_poll_%=\n"
        ".Lr1_loop_%=:\n"
        "s_waitcnt vmcnt(1)\n"
        "v_mov_b32 v28, 0\n"
        "v_mov_b32 v29, 0\n"
        "v_mov_b32 v30, 0\n"
        "v_mov_b32 v31, 0\n"
        "v_mov_b32 v32, 0\n"
        "v_mov_b32 v33, 0\n"
        "v_mov_b32 v34, 0\n"
        "v_mov_b32 v35, 0\n"
        "v_mov_b32 v36, 0\n"
        "v_mov_b32 v37, 0\n"
        "v_mov_b32 v38, 0\n"
        "v_mov_b32 v39, 0\n"
        "v_mov_b32 v40, 0\n"
        "v_mov_b32 v41, 0\n"
        "v_mov_b32 v42, 0\n"
        "v_mov_b32 v43, 0\n"
        "v_mov_b32 v44, 0\n"
        "v_mov_b32 v45, 0\n"
        "v_mov_b32 v46, 0\n"
        "v_mov_b32 v47, 0\n"
        "v_mov_b32 v48, 0\n"
        "v_mov_b32 v49, 0\n"
        "v_mov_b32 v50, 0\n"
        "v_mov_b32 v51, 0\n"
        "v_mov_b32 v52, 0\n"
        "v_mov_b32 v53, 0\n"
        "v_mov_b32 v54, 0\n"
        "v_mov_b32 v55, 0\n"
        "v_mov_b32 v56, 0\n"
        "v_mov_b32 v57, 0\n"
        "v_mov_b32 v58, 0\n"
        "v_mov_b32 v59, 0\n"
        "v_mov_b32 v85, v85\n"
        "v_mov_b32 v85, v85\n"
        "v_mov_b32 v85, v85\n"
        "v_mov_b32 v85, v85\n"
        "v_mov_b32 v85, v85\n"
        "v_mov_b32 v85, v85\n"
        "v_mov_b32 v85, v85\n"
        "v_mov_b32 v85, v85\n"
        "v_mov_b32 v85, v85\n"
        "v_mov_b32 v85, v85\n"
        "v_mov_b32 v85, v85\n"
        "v_mov_b32 v85, v85\n"
        "v_mov_b32 v85, v85\n"
        "v_mov_b32 v85, v85\n"
        "v_mov_b32 v85, v85\n"
        "v_mov_b32 v85, v85\n"
        "v_mov_b32 v85, v85\n"
        "v_mov_b32 v85, v85\n"
        "v_mov_b32 v85, v85\n"
        "v_mov_b32 v85, v85\n"
        "v_mov_b32 v85, v85\n"
        "v_mov_b32 v85, v85\n"
        "v_mov_b32 v85, v85\n"
        "v_mov_b32 v85, v85\n"
        "v_mov_b32 v85, v85\n"
        "v_mov_b32 v85, v85\n"
        "v_mov_b32 v85, v85\n"
        "v_mov_b32 v85, v85\n"
        "v_mov_b32 v85, v85\n"
        "v_mov_b32 v85, v85\n"
        "v_mov_b32 v85, v85\n"
        "v_mov_b32 v85, v85\n"
        "v_mov_b32 v85, v85\n"
        "v_mov_b32 v85, v85\n"
        "v_mov_b32 v85, v85\n"
        "v_mov_b32 v85, v85\n"
        "v_mov_b32 v85, v85\n"
        "v_mov_b32 v85, v85\n"
        "v_mov_b32 v85, v85\n"
        "v_mov_b32 v85, v85\n"
        "v_mov_b32 v85, v85\n"
        "v_mov_b32 v85, v85\n"
        "v_mov_b32 v85, v85\n"
        ".Lr1_poll_%=:\n"
        "buffer_load_dwordx4 v[4:7], v72, s[40:43], s47 offen sc1\n"
        "buffer_load_dwordx4 v[20:23], v72, s[40:43], s49 offen sc1\n"
        "s_waitcnt vmcnt(0)\n"
        ".Lr1_mwait_%=:\n"
        "v_max3_u32 v60, v4, v5, v6\n"
        "v_max3_u32 v61, v7, v20, v21\n"
        "v_max3_u32 v60, v60, v22, v23\n"
        "ds_write_b128 v72, v[4:7] offset:4096\n"
        "ds_write_b128 v72, v[20:23] offset:8192\n"
        "v_smfmac_f32_16x16x64_bf16 v[28:31], v[4:7], v[96:103], v75\n"
        "v_max_u32 v60, v60, v61\n"
        "v_cmp_eq_u32 vcc, -1, v60\n"
        "v_smfmac_f32_16x16x64_bf16 v[32:35], v[4:7], v[152:159], v75\n"
        "s_cbranch_vccnz .Lr1_rm_%=\n"
        ".Lr1_mok_%=:\n"
        "v_smfmac_f32_16x16x64_bf16 v[36:39], v[4:7], v[208:215], v75\n"
        "v_smfmac_f32_16x16x64_bf16 v[40:43], v[4:7], a[0:7], v75\n"
        "s_waitcnt lgkmcnt(0)\n"
        "s_barrier\n"
        "ds_read_b128 v[12:15], v72 offset:6144\n"
        "ds_read_b128 v[24:27], v72 offset:9216\n"
        "ds_read_b128 v[0:3], v72 offset:3072\n"
        "ds_read_b128 v[8:11], v72 offset:5120\n"
        "ds_read_b128 v[16:19], v72 offset:7168\n"
        "v_smfmac_f32_16x16x64_bf16 v[28:31], v[20:23], v[128:135], v75\n"
        "buffer_store_dwordx4 v[64:67], v72, s[40:43], s53 offen sc1\n"
        "v_smfmac_f32_16x16x64_bf16 v[32:35], v[20:23], v[184:191], v75\n"
        "buffer_store_dwordx4 v[68:71], v72, s[40:43], s53 offen offset:1024 sc1\n"
        "v_smfmac_f32_16x16x64_bf16 v[36:39], v[20:23], v[240:247], v75\n"
        "v_smfmac_f32_16x16x64_bf16 v[40:43], v[20:23], a[8:15], v75\n"
        "s_waitcnt lgkmcnt(4)\n"
        "v_smfmac_f32_16x16x64_bf16 v[40:43], v[12:15], a[16:23], v75\n"
        "s_mov_b32 s46, s47\n"
        "s_add_u32 s51, s45, s56\n"
        "s_add_u32 s57, s44, 1\n"
        "v_smfmac_f32_16x16x64_bf16 v[28:31], v[12:15], v[112:119], v75\n"
        "v_readlane_b32 s45, v85, s57\n"
        "s_lshl_b32 s58, s44, 18\n"
        "s_add_u32 s53, s54, s58\n"
        "v_smfmac_f32_16x16x64_bf16 v[32:35], v[12:15], v[168:175], v75\n"
        "s_xor_b32 s47, s45, 0x84000\n"
        "s_andn2_b32 s45, s45, 0x1000\n"
        "s_add_u32 s49, s47, 32768\n"
        "v_smfmac_f32_16x16x64_bf16 v[36:39], v[12:15], v[224:231], v75\n"
        "s_waitcnt lgkmcnt(3)\n"
        "v_smfmac_f32_16x16x64_bf16 v[40:43], v[24:27], a[24:31], v75\n"
        "v_smfmac_f32_16x16x64_bf16 v[28:31], v[24:27], v[136:143], v75\n"
        "s_waitcnt lgkmcnt(2)\n"
        "v_smfmac_f32_16x16x64_bf16 v[28:31], v[0:3], v[88:95], v75\n"
        "v_pk_add_f32 v[40:41], v[40:41], v[42:43]\n"
        "s_waitcnt lgkmcnt(1)\n"
        "v_smfmac_f32_16x16x64_bf16 v[28:31], v[8:11], v[104:111], v75\n"
        "v_cvt_pk_bf16_f32 v40, v40, v41\n"
        "s_waitcnt lgkmcnt(0)\n"
        "v_smfmac_f32_16x16x64_bf16 v[28:31], v[16:19], v[120:127], v75\n"
        "ds_write_b32 v74, v40 offset:3328\n"
        "v_smfmac_f32_16x16x64_bf16 v[32:35], v[24:27], v[192:199], v75\n"
        "v_smfmac_f32_16x16x64_bf16 v[32:35], v[0:3], v[144:151], v75\n"
        "v_pk_add_f32 v[28:29], v[28:29], v[30:31]\n"
        "v_smfmac_f32_16x16x64_bf16 v[32:35], v[8:11], v[160:167], v75\n"
        "v_cvt_pk_bf16_f32 v28, v28, v29\n"
        "v_smfmac_f32_16x16x64_bf16 v[32:35], v[16:19], v[176:183], v75\n"
        "ds_write_b32 v74, v28 offset:128\n"
        "v_smfmac_f32_16x16x64_bf16 v[36:39], v[24:27], v[248:255], v75\n"
        "v_smfmac_f32_16x16x64_bf16 v[36:39], v[0:3], v[200:207], v75\n"
        "v_pk_add_f32 v[32:33], v[32:33], v[34:35]\n"
        "v_smfmac_f32_16x16x64_bf16 v[36:39], v[8:11], v[216:223], v75\n"
        "v_cvt_pk_bf16_f32 v32, v32, v33\n"
        "v_smfmac_f32_16x16x64_bf16 v[36:39], v[16:19], v[232:239], v75\n"
        "ds_write_b32 v74, v32 offset:1152\n"
        "v_smfmac_f32_16x16x64_bf16 v[44:47], v[0:3], a[32:39], v75\n"
        "s_nop 3\n"
        "v_pk_add_f32 v[36:37], v[36:37], v[38:39]\n"
        "v_cvt_pk_bf16_f32 v36, v36, v37\n"
        "ds_write_b32 v74, v36 offset:2176\n"
        "v_smfmac_f32_16x16x64_bf16 v[44:47], v[4:7], a[40:47], v75\n"
        "v_smfmac_f32_16x16x64_bf16 v[44:47], v[8:11], a[48:55], v75\n"
        "s_waitcnt vmcnt(2) lgkmcnt(0)\n"
        "s_barrier\n"
        "ds_read_b64_tr_b16 v[68:69], v73 offset:1024\n"
        "ds_read_b64_tr_b16 v[70:71], v73 offset:1536\n"
        "v_smfmac_f32_16x16x64_bf16 v[44:47], v[12:15], a[56:63], v75\n"
        "v_mov_b32 v60, 0\n"
        "v_smfmac_f32_16x16x64_bf16 v[44:47], v[16:19], a[64:71], v75\n"
        "v_mov_b32 v61, 0\n"
        "v_smfmac_f32_16x16x64_bf16 v[44:47], v[20:23], a[72:79], v75\n"
        "v_mov_b32 v62, 0\n"
        "v_smfmac_f32_16x16x64_bf16 v[44:47], v[24:27], a[80:87], v75\n"
        "v_mov_b32 v63, 0\n"
        "v_smfmac_f32_16x16x64_bf16 v[48:51], v[0:3], a[88:95], v75\n"
        "v_smfmac_f32_16x16x64_bf16 v[48:51], v[4:7], a[96:103], v75\n"
        "v_smfmac_f32_16x16x64_bf16 v[48:51], v[8:11], a[104:111], v75\n"
        "v_pk_add_f32 v[44:45], v[44:45], v[46:47]\n"
        "v_smfmac_f32_16x16x64_bf16 v[48:51], v[12:15], a[112:119], v75\n"
        "v_cvt_pk_bf16_f32 v64, v44, v45\n"
        "v_smfmac_f32_16x16x64_bf16 v[48:51], v[16:19], a[120:127], v75\n"
        "ds_write_b32 v78, v64 offset:0\n"
        "v_smfmac_f32_16x16x64_bf16 v[48:51], v[20:23], a[128:135], v75\n"
        "v_smfmac_f32_16x16x64_bf16 v[48:51], v[24:27], a[136:143], v75\n"
        "v_smfmac_f32_16x16x64_bf16 v[52:55], v[0:3], a[144:151], v75\n"
        "v_smfmac_f32_16x16x64_bf16 v[52:55], v[4:7], a[152:159], v75\n"
        "v_smfmac_f32_16x16x64_bf16 v[52:55], v[8:11], a[160:167], v75\n"
        "v_pk_add_f32 v[48:49], v[48:49], v[50:51]\n"
        "v_smfmac_f32_16x16x64_bf16 v[52:55], v[12:15], a[168:175], v75\n"
        "v_cvt_pk_bf16_f32 v65, v48, v49\n"
        "v_smfmac_f32_16x16x64_bf16 v[52:55], v[16:19], a[176:183], v75\n"
        "ds_write_b32 v78, v65 offset:256\n"
        "v_smfmac_f32_16x16x64_bf16 v[52:55], v[20:23], a[184:191], v75\n"
        "v_smfmac_f32_16x16x64_bf16 v[52:55], v[24:27], a[192:199], v75\n"
        "v_smfmac_f32_16x16x64_bf16 v[56:59], v[0:3], a[200:207], v75\n"
        "v_smfmac_f32_16x16x64_bf16 v[56:59], v[4:7], a[208:215], v75\n"
        "v_smfmac_f32_16x16x64_bf16 v[56:59], v[8:11], a[216:223], v75\n"
        "v_pk_add_f32 v[52:53], v[52:53], v[54:55]\n"
        "v_smfmac_f32_16x16x64_bf16 v[56:59], v[12:15], a[224:231], v75\n"
        "v_cvt_pk_bf16_f32 v66, v52, v53\n"
        "v_smfmac_f32_16x16x64_bf16 v[56:59], v[16:19], a[232:239], v75\n"
        "ds_write_b32 v78, v66 offset:512\n"
        "buffer_load_dwordx2 v[40:41], v77, s[40:43], s46 offen sc1\n"
        "buffer_load_dwordx4 v[28:31], v76, s[40:43], s46 offen sc1\n"
        "buffer_load_dwordx4 v[32:35], v76, s[40:43], s46 offen offset:256 sc1\n"
        "v_smfmac_f32_16x16x64_bf16 v[56:59], v[20:23], a[240:247], v75\n"
        "v_smfmac_f32_16x16x64_bf16 v[56:59], v[24:27], a[248:255], v75\n"
        "s_nop 7\n"
        "v_pk_add_f32 v[56:57], v[56:57], v[58:59]\n"
        "v_cvt_pk_bf16_f32 v67, v56, v57\n"
        "ds_write_b32 v78, v67 offset:768\n"
        "s_waitcnt lgkmcnt(0)\n"
        "ds_read_b128 v[8:11], v72 offset:11264\n"
        "s_cmp_eq_u32 s44, 63\n"
        "s_cbranch_scc1 .Lr1_exit_%=\n"
        "s_add_u32 s44, s44, 1\n"
        "s_mov_b32 s57, 0\n"
        "s_mov_b32 s58, 0\n"
        "buffer_load_dwordx4 v[36:39], v72, s[40:43], s51 offen sc1\n"
        ".Lr1_swait_%=:\n"
        "s_waitcnt vmcnt(1) lgkmcnt(0)\n"
        ".Lr1_schain_%=:\n"
        "v_pk_add_f32 v[0:1], v[28:29], v[32:33]\n"
        "v_pk_add_f32 v[2:3], v[30:31], v[34:35]\n"
        "v_mov_b32 v13, v40\n"
        "v_max3_f32 v4, v0, v1, v13\n"
        "v_cmp_u_f32 vcc, v0, v13\n"
        "v_max3_f32 v4, v2, v3, v4\n"
        "s_cbranch_vccnz .Lr1_sbad_%=\n"
        ".Lr1_sgo_%=:\n"
        "s_nop 0\n"
        "v_max_f32_dpp v4, v4, v4 quad_perm:[2,3,0,1] row_mask:0xf bank_mask:0xf\n"
        "v_sub_f32 v12, v13, v4\n"
        "v_pk_add_f32 v[0:1], v[0:1], v[4:5] op_sel_hi:[1,0] neg_lo:[0,1] neg_hi:[0,1]\n"
        "v_exp_f32 v12, v12\n"
        "v_pk_add_f32 v[2:3], v[2:3], v[4:5] op_sel_hi:[1,0] neg_lo:[0,1] neg_hi:[0,1]\n"
        "v_exp_f32 v0, v0\n"
        "v_exp_f32 v1, v1\n"
        "v_exp_f32 v2, v2\n"
        "v_exp_f32 v3, v3\n"
        "v_fma_f32 v6, v12, v41, v0\n"
        "v_add_f32 v6, v6, v1\n"
        "v_add_f32 v7, v2, v3\n"
        "v_add_f32 v6, v6, v7\n"
        "s_nop 1\n"
        "v_add_f32_dpp v6, v6, v6 quad_perm:[2,3,0,1] row_mask:0xf bank_mask:0xf\n"
        "v_rcp_f32 v6, v6\n"
        "s_nop 0\n"
        "v_pk_mul_f32 v[0:1], v[0:1], v[6:7] op_sel_hi:[1,0]\n"
        "v_pk_mul_f32 v[2:3], v[2:3], v[6:7] op_sel_hi:[1,0]\n"
        "v_cvt_pk_bf16_f32 v0, v0, v1\n"
        "v_cvt_pk_bf16_f32 v1, v2, v3\n"
        "v_mul_f32 v12, v12, v6\n"
        "v_cvt_pk_bf16_f32 v12, v12, v12\n"
        ".Lr1_redo_%=:\n"
        "v_smfmac_f32_16x16x32_bf16 v[60:63], v[0:1], v[8:11], v75\n"
        "v_and_b32 v2, v12, v79\n"
        "v_and_b32 v3, v12, v80\n"
        "s_waitcnt vmcnt(0)\n"
        "s_nop 0\n"
        "v_smfmac_f32_16x16x32_bf16 v[60:63], v[2:3], v[36:39], v75\n"
        "s_nop 7\n"
        "v_pk_add_f32 v[4:5], v[60:61], v[62:63]\n"
        "ds_write2_b32 v82, v4, v5 offset0:2 offset1:34\n"
        "v_cmp_u_f32 vcc, v4, v5\n"
        "s_cbranch_vccnz .Lr1_obad_%=\n"
        ".Lr1_sok_%=:\n"
        "s_waitcnt lgkmcnt(0)\n"
        "s_barrier\n"
        "ds_read_b128 v[0:3], v83\n"
        "ds_read_b128 v[4:7], v83 offset:16\n"
        "s_waitcnt lgkmcnt(1)\n"
        "v_pk_add_f32 v[0:1], v[0:1], v[2:3]\n"
        "s_waitcnt lgkmcnt(0)\n"
        "v_pk_add_f32 v[4:5], v[4:5], v[6:7]\n"
        "v_pk_add_f32 v[0:1], v[0:1], v[4:5]\n"
        "v_cvt_pk_bf16_f32 v0, v0, v1\n"
        "buffer_store_dword v0, v81, s[40:43], s45 offen sc1\n"
        "s_branch .Lr1_loop_%=\n"
        ".Lr1_rm_%=:\n"
        "s_add_u32 s59, s59, 1\n"
        "s_add_u32 s57, s57, 1\n"
        "s_cmp_eq_u32 s55, 0\n"
        "s_cbranch_scc1 .Lr1_mok_%=\n"
        "s_sub_u32 s55, s55, 1\n"
        "s_waitcnt lgkmcnt(0)\n"
        "buffer_load_dwordx4 v[4:7], v72, s[40:43], s47 offen sc1\n"
        "buffer_load_dwordx4 v[20:23], v72, s[40:43], s49 offen sc1\n"
        "s_nop 7\n"
        "v_mov_b32 v28, 0\n"
        "v_mov_b32 v29, 0\n"
        "v_mov_b32 v30, 0\n"
        "v_mov_b32 v31, 0\n"
        "v_mov_b32 v32, 0\n"
        "v_mov_b32 v33, 0\n"
        "v_mov_b32 v34, 0\n"
        "v_mov_b32 v35, 0\n"
        "s_waitcnt vmcnt(0)\n"
        "s_branch .Lr1_mwait_%=\n"
        ".Lr1_sbad_%=:\n"
        "s_add_u32 s57, s57, 1\n"
        "s_cmp_eq_u32 s55, 0\n"
        "s_cbranch_scc1 .Lr1_sgo_%=\n"
        ".Lr1_sslow_%=:\n"
        "s_sub_u32 s55, s55, 1\n"
        "buffer_load_dwordx4 v[28:31], v76, s[40:43], s46 offen sc1\n"
        "buffer_load_dwordx4 v[32:35], v76, s[40:43], s46 offen offset:256 sc1\n"
        "buffer_load_dwordx2 v[40:41], v77, s[40:43], s46 offen sc1\n"
        "s_waitcnt vmcnt(0)\n"
        "s_branch .Lr1_schain_%=\n"
        ".Lr1_obad_%=:\n"
        "s_cmp_eq_u32 s55, 0\n"
        "s_cbranch_scc1 .Lr1_sok_%=\n"
        "v_cmp_u_f32 vcc, v6, v6\n"
        "s_add_u32 s58, s58, 1\n"
        "v_mov_b32 v60, 0\n"
        "v_mov_b32 v61, 0\n"
        "v_mov_b32 v62, 0\n"
        "v_mov_b32 v63, 0\n"
        "s_cbranch_vccnz .Lr1_sslow_%=\n"
        "s_sub_u32 s55, s55, 1\n"
        "buffer_load_dwordx4 v[36:39], v72, s[40:43], s51 offen sc1\n"
        "s_waitcnt vmcnt(0)\n"
        "s_nop 0\n"
        "s_branch .Lr1_redo_%=\n"
        ".Lr1_exit_%=:\n"
        "s_nop 7\n"
        "buffer_store_dwordx4 v[64:67], v72, s[40:43], s53 offen sc1\n"
        "buffer_store_dwordx4 v[68:71], v72, s[40:43], s53 offen offset:1024 sc1\n"
        "s_waitcnt vmcnt(0) lgkmcnt(0)\n"
        "s_barrier\n"
        "s_branch .Lrend_%=\n"
        ".Lr2_entry_%=:\n"
        "v_mbcnt_lo_u32_b32 v0, -1, 0\n"
        "v_mbcnt_hi_u32_b32 v0, -1, v0\n"
        "v_lshlrev_b32 v0, 2, v0\n"
        "ds_read_b32 v72, v0 offset:18176\n"
        "ds_read_b32 v73, v0 offset:19200\n"
        "ds_read_b32 v74, v0 offset:20224\n"
        "ds_read_b32 v75, v0 offset:21248\n"
        "ds_read_b32 v76, v0 offset:22272\n"
        "ds_read_b32 v77, v0 offset:23296\n"
        "ds_read_b32 v78, v0 offset:24320\n"
        "ds_read_b32 v79, v0 offset:25344\n"
        "ds_read_b32 v80, v0 offset:26368\n"
        "ds_read_b32 v81, v0 offset:27392\n"
        "ds_read_b32 v82, v0 offset:28416\n"
        "ds_read_b32 v83, v0 offset:29440\n"
        "ds_read_b32 v84, v0 offset:30464\n"
        "ds_read_b32 v85, v0 offset:31488\n"
        "s_waitcnt lgkmcnt(0)\n"
        "ds_read_b32 v1, v0 offset:32512\n"
        "ds_read_b32 v2, v0 offset:33536\n"
        "ds_read_b32 v3, v0 offset:34560\n"
        "ds_read_b32 v4, v0 offset:35584\n"
        "ds_read_b32 v5, v0 offset:36608\n"
        "ds_read_b32 v6, v0 offset:37632\n"
        "ds_read_b32 v7, v0 offset:38656\n"
        "ds_read_b32 v8, v0 offset:39680\n"
        "ds_read_b32 v9, v0 offset:40704\n"
        "s_waitcnt lgkmcnt(0)\n"
        "buffer_load_dword v10, v9, s[40:43], 0 offen\n"
        "v_readlane_b32 s45, v85, 0\n"
        "s_xor_b32 s47, s45, 0x84000\n"
        "s_andn2_b32 s45, s45, 0x1000\n"
        "s_add_u32 s49, s47, 32768\n"
        "s_getpc_b64 s[58:59]\n"
        "v_lshlrev_b32 v12, 5, v0\n"
        "v_min_u32 v12, .Lr2_exit_%=-.Lr2_loop_%=-64, v12\n"
        "v_add_u32 v12, .Lr2_loop_%=-.Lr2_entry_%=, v12\n"
        "global_load_dword v12, v12, s[58:59]\n"
        "s_mov_b32 s59, 0\n"
        "buffer_load_dword v11, v81, s[40:43], s47 offen sc1\n"
        "s_mov_b32 s60, 0\n"
        "s_mov_b32 s61, 4096\n"
        "s_mov_b32 s62, 8192\n"
        "s_mov_b32 s63, 12288\n"
        "s_mov_b32 s64, 16384\n"
        "s_mov_b32 s65, 20480\n"
        "s_mov_b32 s66, 24576\n"
        "buffer_load_dwordx4 v[88:91], v1, s[40:43], s60 offen\n"
        "buffer_load_dwordx4 v[92:95], v1, s[40:43], s60 offen offset:2048\n"
        "buffer_load_dwordx4 v[96:99], v1, s[40:43], s61 offen\n"
        "buffer_load_dwordx4 v[100:103], v1, s[40:43], s61 offen offset:2048\n"
        "buffer_load_dwordx4 v[104:107], v1, s[40:43], s62 offen\n"
        "buffer_load_dwordx4 v[108:111], v1, s[40:43], s62 offen offset:2048\n"
        "buffer_load_dwordx4 v[112:115], v1, s[40:43], s63 offen\n"
        "buffer_load_dwordx4 v[116:119], v1, s[40:43], s63 offen offset:2048\n"
        "buffer_load_dwordx4 v[120:123], v1, s[40:43], s64 offen\n"
        "buffer_load_dwordx4 v[124:127], v1, s[40:43], s64 offen offset:2048\n"
        "buffer_load_dwordx4 v[128:131], v1, s[40:43], s65 offen\n"
        "buffer_load_dwordx4 v[132:135], v1, s[40:43], s65 offen offset:2048\n"
        "buffer_load_dwordx4 v[136:139], v1, s[40:43], s66 offen\n"
        "buffer_load_dwordx4 v[140:143], v1, s[40:43], s66 offen offset:2048\n"
        "buffer_load_dwordx4 v[144:147], v2, s[40:43], s60 offen\n"
        "buffer_load_dwordx4 v[148:151], v2, s[40:43], s60 offen offset:2048\n"
        "buffer_load_dwordx4 v[152:155], v2, s[40:43], s61 offen\n"
        "buffer_load_dwordx4 v[156:159], v2, s[40:43], s61 offen offset:2048\n"
        "buffer_load_dwordx4 v[160:163], v2, s[40:43], s62 offen\n"
        "buffer_load_dwordx4 v[164:167], v2, s[40:43], s62 offen offset:2048\n"
        "buffer_load_dwordx4 v[168:171], v2, s[40:43], s63 offen\n"
        "buffer_load_dwordx4 v[172:175], v2, s[40:43], s63 offen offset:2048\n"
        "buffer_load_dwordx4 v[176:179], v2, s[40:43], s64 offen\n"
        "buffer_load_dwordx4 v[180:183], v2, s[40:43], s64 offen offset:2048\n"
        "buffer_load_dwordx4 v[184:187], v2, s[40:43], s65 offen\n"
        "buffer_load_dwordx4 v[188:191], v2, s[40:43], s65 offen offset:2048\n"
        "buffer_load_dwordx4 v[192:195], v2, s[40:43], s66 offen\n"
        "buffer_load_dwordx4 v[196:199], v2, s[40:43], s66 offen offset:2048\n"
        "buffer_load_dwordx4 v[200:203], v3, s[40:43], s60 offen\n"
        "buffer_load_dwordx4 v[204:207], v3, s[40:43], s60 offen offset:2048\n"
        "buffer_load_dwordx4 v[208:211], v3, s[40:43], s61 offen\n"
        "buffer_load_dwordx4 v[212:215], v3, s[40:43], s61 offen offset:2048\n"
        "buffer_load_dwordx4 v[216:219], v3, s[40:43], s62 offen\n"
        "buffer_load_dwordx4 v[220:223], v3, s[40:43], s62 offen offset:2048\n"
        "buffer_load_dwordx4 v[224:227], v3, s[40:43], s63 offen\n"
        "buffer_load_dwordx4 v[228:231], v3, s[40:43], s63 offen offset:2048\n"
        "buffer_load_dwordx4 v[232:235], v3, s[40:43], s64 offen\n"
        "buffer_load_dwordx4 v[236:239], v3, s[40:43], s64 offen offset:2048\n"
        "buffer_load_dwordx4 v[240:243], v3, s[40:43], s65 offen\n"
        "buffer_load_dwordx4 v[244:247], v3, s[40:43], s65 offen offset:2048\n"
        "buffer_load_dwordx4 v[248:251], v3, s[40:43], s66 offen\n"
        "buffer_load_dwordx4 v[252:255], v3, s[40:43], s66 offen offset:2048\n"
        "buffer_load_dwordx4 a[0:3], v4, s[40:43], s62 offen\n"
        "buffer_load_dwordx4 a[4:7], v4, s[40:43], s62 offen offset:2048\n"
        "buffer_load_dwordx4 a[8:11], v4, s[40:43], s66 offen\n"
        "buffer_load_dwordx4 a[12:15], v4, s[40:43], s66 offen offset:2048\n"
        "buffer_load_dwordx4 a[16:19], v4, s[40:43], s64 offen\n"
        "buffer_load_dwordx4 a[20:23], v4, s[40:43], s64 offen offset:2048\n"
        "buffer_load_dwordx4 a[24:27], v4, s[40:43], s65 offen\n"
        "buffer_load_dwordx4 a[28:31], v4, s[40:43], s65 offen offset:2048\n"
        "buffer_load_dwordx4 a[32:35], v5, s[40:43], s60 offen\n"
        "buffer_load_dwordx4 a[36:39], v5, s[40:43], s60 offen offset:2048\n"
        "buffer_load_dwordx4 a[40:43], v5, s[40:43], s61 offen\n"
        "buffer_load_dwordx4 a[44:47], v5, s[40:43], s61 offen offset:2048\n"
        "buffer_load_dwordx4 a[48:51], v5, s[40:43], s62 offen\n"
        "buffer_load_dwordx4 a[52:55], v5, s[40:43], s62 offen offset:2048\n"
        "buffer_load_dwordx4 a[56:59], v5, s[40:43], s63 offen\n"
        "buffer_load_dwordx4 a[60:63], v5, s[40:43], s63 offen offset:2048\n"
        "buffer_load_dwordx4 a[64:67], v5, s[40:43], s64 offen\n"
        "buffer_load_dwordx4 a[68:71], v5, s[40:43], s64 offen offset:2048\n"
        "buffer_load_dwordx4 a[72:75], v5, s[40:43], s65 offen\n"
        "buffer_load_dwordx4 a[76:79], v5, s[40:43], s65 offen offset:2048\n"
        "buffer_load_dwordx4 a[80:83], v5, s[40:43], s66 offen\n"
        "buffer_load_dwordx4 a[84:87], v5, s[40:43], s66 offen offset:2048\n"
        "buffer_load_dwordx4 a[88:91], v6, s[40:43], s60 offen\n"
        "buffer_load_dwordx4 a[92:95], v6, s[40:43], s60 offen offset:2048\n"
        "buffer_load_dwordx4 a[96:99], v6, s[40:43], s61 offen\n"
        "buffer_load_dwordx4 a[100:103], v6, s[40:43], s61 offen offset:2048\n"
        "buffer_load_dwordx4 a[104:107], v6, s[40:43], s62 offen\n"
        "buffer_load_dwordx4 a[108:111], v6, s[40:43], s62 offen offset:2048\n"
        "buffer_load_dwordx4 a[112:115], v6, s[40:43], s63 offen\n"
        "buffer_load_dwordx4 a[116:119], v6, s[40:43], s63 offen offset:2048\n"
        "buffer_load_dwordx4 a[120:123], v6, s[40:43], s64 offen\n"
        "buffer_load_dwordx4 a[124:127], v6, s[40:43], s64 offen offset:2048\n"
        "buffer_load_dwordx4 a[128:131], v6, s[40:43], s65 offen\n"
        "buffer_load_dwordx4 a[132:135], v6, s[40:43], s65 offen offset:2048\n"
        "buffer_load_dwordx4 a[136:139], v6, s[40:43], s66 offen\n"
        "buffer_load_dwordx4 a[140:143], v6, s[40:43], s66 offen offset:2048\n"
        "buffer_load_dwordx4 a[144:147], v7, s[40:43], s60 offen\n"
        "buffer_load_dwordx4 a[148:151], v7, s[40:43], s60 offen offset:2048\n"
        "buffer_load_dwordx4 a[152:155], v7, s[40:43], s61 offen\n"
        "buffer_load_dwordx4 a[156:159], v7, s[40:43], s61 offen offset:2048\n"
        "buffer_load_dwordx4 a[160:163], v7, s[40:43], s62 offen\n"
        "buffer_load_dwordx4 a[164:167], v7, s[40:43], s62 offen offset:2048\n"
        "buffer_load_dwordx4 a[168:171], v7, s[40:43], s63 offen\n"
        "buffer_load_dwordx4 a[172:175], v7, s[40:43], s63 offen offset:2048\n"
        "buffer_load_dwordx4 a[176:179], v7, s[40:43], s64 offen\n"
        "buffer_load_dwordx4 a[180:183], v7, s[40:43], s64 offen offset:2048\n"
        "buffer_load_dwordx4 a[184:187], v7, s[40:43], s65 offen\n"
        "buffer_load_dwordx4 a[188:191], v7, s[40:43], s65 offen offset:2048\n"
        "buffer_load_dwordx4 a[192:195], v7, s[40:43], s66 offen\n"
        "buffer_load_dwordx4 a[196:199], v7, s[40:43], s66 offen offset:2048\n"
        "buffer_load_dwordx4 a[200:203], v8, s[40:43], s60 offen\n"
        "buffer_load_dwordx4 a[204:207], v8, s[40:43], s60 offen offset:2048\n"
        "buffer_load_dwordx4 a[208:211], v8, s[40:43], s61 offen\n"
        "buffer_load_dwordx4 a[212:215], v8, s[40:43], s61 offen offset:2048\n"
        "buffer_load_dwordx4 a[216:219], v8, s[40:43], s62 offen\n"
        "buffer_load_dwordx4 a[220:223], v8, s[40:43], s62 offen offset:2048\n"
        "buffer_load_dwordx4 a[224:227], v8, s[40:43], s63 offen\n"
        "buffer_load_dwordx4 a[228:231], v8, s[40:43], s63 offen offset:2048\n"
        "buffer_load_dwordx4 a[232:235], v8, s[40:43], s64 offen\n"
        "buffer_load_dwordx4 a[236:239], v8, s[40:43], s64 offen offset:2048\n"
        "buffer_load_dwordx4 a[240:243], v8, s[40:43], s65 offen\n"
        "buffer_load_dwordx4 a[244:247], v8, s[40:43], s65 offen offset:2048\n"
        "buffer_load_dwordx4 a[248:251], v8, s[40:43], s66 offen\n"
        "buffer_load_dwordx4 a[252:255], v8, s[40:43], s66 offen offset:2048\n"
        "s_add_u32 s53, s53, 2048\n"
        "s_add_u32 s54, s54, 2048\n"
        "v_mov_b32 v28, 0\n"
        "v_mov_b32 v29, 0\n"
        "v_mov_b32 v30, 0\n"
        "v_mov_b32 v31, 0\n"
        "v_mov_b32 v32, 0\n"
        "v_mov_b32 v33, 0\n"
        "v_mov_b32 v34, 0\n"
        "v_mov_b32 v35, 0\n"
        "v_mov_b32 v36, 0\n"
        "v_mov_b32 v37, 0\n"
        "v_mov_b32 v38, 0\n"
        "v_mov_b32 v39, 0\n"
        "v_mov_b32 v40, 0\n"
        "v_mov_b32 v41, 0\n"
        "v_mov_b32 v42, 0\n"
        "v_mov_b32 v43, 0\n"
        "v_mov_b32 v44, 0\n"
        "v_mov_b32 v45, 0\n"
        "v_mov_b32 v46, 0\n"
        "v_mov_b32 v47, 0\n"
        "v_mov_b32 v48, 0\n"
        "v_mov_b32 v49, 0\n"
        "v_mov_b32 v50, 0\n"
        "v_mov_b32 v51, 0\n"
        "v_mov_b32 v52, 0\n"
        "v_mov_b32 v53, 0\n"
        "v_mov_b32 v54, 0\n"
        "v_mov_b32 v55, 0\n"
        "v_mov_b32 v56, 0\n"
        "v_mov_b32 v57, 0\n"
        "v_mov_b32 v58, 0\n"
        "v_mov_b32 v59, 0\n"
        "v_mov_b32 v60, 0\n"
        "v_mov_b32 v61, 0\n"
        "v_mov_b32 v62, 0\n"
        "v_mov_b32 v63, 0\n"
        "v_mov_b32 v64, 0\n"
        "v_mov_b32 v65, 0\n"
        "v_mov_b32 v66, 0\n"
        "v_mov_b32 v67, 0\n"
        "v_mov_b32 v68, 0\n"
        "v_mov_b32 v69, 0\n"
        "v_mov_b32 v70, 0\n"
        "v_mov_b32 v71, 0\n"
        "buffer_load_dword v10, v9, s[40:43], 0 offen\n"
        "s_waitcnt vmcnt(0)\n"
        "s_mov_b32 s50, 0\n"
        "s_mov_b32 s66, 0x11111111\n"
        "s_mov_b32 s67, 0x11111111\n"
        "s_branch .Lr2_poll_%=\n"
        ".Lr2_loop_%=:\n"
        "s_waitcnt vmcnt(0)\n"
        "v_mov_b32 v28, 0\n"
        "v_mov_b32 v29, 0\n"
        "v_mov_b32 v30, 0\n"
        "v_mov_b32 v31, 0\n"
        "v_mov_b32 v32, 0\n"
        "v_mov_b32 v33, 0\n"
        "v_mov_b32 v34, 0\n"
        "v_mov_b32 v35, 0\n"
        "v_mov_b32 v36, 0\n"
        "v_mov_b32 v37, 0\n"
        "v_mov_b32 v38, 0\n"
        "v_mov_b32 v39, 0\n"
        "v_mov_b32 v40, 0\n"
        "v_mov_b32 v41, 0\n"
        "v_mov_b32 v42, 0\n"
        "v_mov_b32 v43, 0\n"
        "v_mov_b32 v44, 0\n"
        "v_mov_b32 v45, 0\n"
        "v_mov_b32 v46, 0\n"
        "v_mov_b32 v47, 0\n"
        "v_mov_b32 v48, 0\n"
        "v_mov_b32 v49, 0\n"
        "v_mov_b32 v50, 0\n"
        "v_mov_b32 v51, 0\n"
        "v_mov_b32 v52, 0\n"
        "v_mov_b32 v53, 0\n"
        "v_mov_b32 v54, 0\n"
        "v_mov_b32 v55, 0\n"
        "v_mov_b32 v56, 0\n"
        "v_mov_b32 v57, 0\n"
        "v_mov_b32 v58, 0\n"
        "v_mov_b32 v59, 0\n"
        "v_mov_b32 v85, v85\n"
        "v_mov_b32 v85, v85\n"
        "v_mov_b32 v85, v85\n"
        "v_mov_b32 v85, v85\n"
        "v_mov_b32 v85, v85\n"
        "v_mov_b32 v85, v85\n"
        "v_mov_b32 v85, v85\n"
        "v_mov_b32 v85, v85\n"
        "v_mov_b32 v85, v85\n"
        "v_mov_b32 v85, v85\n"
        "v_mov_b32 v85, v85\n"
        "v_mov_b32 v85, v85\n"
        "v_mov_b32 v85, v85\n"
        "v_mov_b32 v85, v85\n"
        "v_mov_b32 v85, v85\n"
        "v_mov_b32 v85, v85\n"
        "v_mov_b32 v85, v85\n"
        "v_mov_b32 v85, v85\n"
        "v_mov_b32 v85, v85\n"
        "v_mov_b32 v85, v85\n"
        "v_mov_b32 v85, v85\n"
        "v_mov_b32 v85, v85\n"
        "v_mov_b32 v85, v85\n"
        "v_mov_b32 v85, v85\n"
        "v_mov_b32 v85, v85\n"
        "v_mov_b32 v85, v85\n"
        "v_mov_b32 v85, v85\n"
        "v_mov_b32 v85, v85\n"
        "v_mov_b32 v85, v85\n"
        "v_mov_b32 v85, v85\n"
        "v_mov_b32 v85, v85\n"
        "v_mov_b32 v85, v85\n"
        "v_mov_b32 v85, v85\n"
        "v_mov_b32 v85, v85\n"
        "v_mov_b32 v85, v85\n"
        "v_mov_b32 v85, v85\n"
        "v_mov_b32 v85, v85\n"
        "v_mov_b32 v85, v85\n"
        "v_mov_b32 v85, v85\n"
        "v_mov_b32 v85, v85\n"
        "v_mov_b32 v85, v85\n"
        "v_mov_b32 v85, v85\n"
        "v_mov_b32 v85, v85\n"
        "v_mov_b32 v85, v85\n"
        "v_mov_b32 v85, v85\n"
        "v_mov_b32 v85, v85\n"
        "v_mov_b32 v85, v85\n"
        "v_mov_b32 v85, v85\n"
        "v_mov_b32 v85, v85\n"
        "v_mov_b32 v85, v85\n"
        "v_mov_b32 v85, v85\n"
        "v_mov_b32 v85, v85\n"
        "v_mov_b32 v85, v85\n"
        "v_mov_b32 v85, v85\n"
        "v_mov_b32 v85, v85\n"
        "v_mov_b32 v85, v85\n"
        "v_mov_b32 v85, v85\n"
        "v_mov_b32 v85, v85\n"
        "v_mov_b32 v85, v85\n"
        "v_mov_b32 v85, v85\n"
        "v_mov_b32 v85, v85\n"
        "v_mov_b32 v85, v85\n"
        "v_mov_b32 v85, v85\n"
        "v_mov_b32 v85, v85\n"
        "v_mov_b32 v85, v85\n"
        "v_mov_b32 v85, v85\n"
        "v_mov_b32 v85, v85\n"
        "v_mov_b32 v85, v85\n"
        "v_mov_b32 v85, v85\n"
        "v_mov_b32 v85, v85\n"
        "v_mov_b32 v85, v85\n"
        "v_mov_b32 v85, v85\n"
        "v_mov_b32 v85, v85\n"
        "v_mov_b32 v85, v85\n"
        "v_mov_b32 v85, v85\n"
        "v_mov_b32 v85, v85\n"
        "v_mov_b32 v85, v85\n"
        "v_mov_b32 v85, v85\n"
        ".Lr2_poll_%=:\n"
        "buffer_load_dwordx4 v[8:11], v72, s[40:43], s47 offen sc1\n"
        "buffer_load_dwordx4 v[24:27], v72, s[40:43], s49 offen sc1\n"
        "s_waitcnt vmcnt(0)\n"
        ".Lr2_mwait_%=:\n"
        "v_max3_u32 v60, v8, v9, v10\n"
        "v_max3_u32 v61, v11, v24, v25\n"
        "v_max3_u32 v60, v60, v26, v27\n"
        "ds_write_b128 v72, v[8:11] offset:4096\n"
        "ds_write_b128 v72, v[24:27] offset:8192\n"
        "v_smfmac_f32_16x16x64_bf16 v[28:31], v[8:11], v[104:111], v75\n"
        "v_max_u32 v60, v60, v61\n"
        "v_cmp_eq_u32 vcc, -1, v60\n"
        "v_smfmac_f32_16x16x64_bf16 v[32:35], v[8:11], v[160:167], v75\n"
        "s_cbranch_vccnz .Lr2_rm_%=\n"
        ".Lr2_mok_%=:\n"
        "v_smfmac_f32_16x16x64_bf16 v[36:39], v[8:11], v[216:223], v75\n"
        "v_smfmac_f32_16x16x64_bf16 v[40:43], v[8:11], a[0:7], v75\n"
        "s_waitcnt lgkmcnt(0)\n"
        "s_barrier\n"
        "ds_read_b128 v[16:19], v72 offset:6144\n"
        "ds_read_b128 v[20:23], v72 offset:7168\n"
        "ds_read_b128 v[0:3], v72 offset:2048\n"
        "ds_read_b128 v[4:7], v72 offset:3072\n"
        "ds_read_b128 v[12:15], v72 offset:5120\n"
        "v_smfmac_f32_16x16x64_bf16 v[28:31], v[24:27], v[136:143], v75\n"
        "buffer_store_dwordx4 v[64:67], v72, s[40:43], s53 offen sc1\n"
        "v_smfmac_f32_16x16x64_bf16 v[32:35], v[24:27], v[192:199], v75\n"
        "buffer_store_dwordx4 v[68:71], v72, s[40:43], s53 offen offset:1024 sc1\n"
        "v_smfmac_f32_16x16x64_bf16 v[36:39], v[24:27], v[248:255], v75\n"
        "v_smfmac_f32_16x16x64_bf16 v[40:43], v[24:27], a[8:15], v75\n"
        "s_waitcnt lgkmcnt(4)\n"
        "v_smfmac_f32_16x16x64_bf16 v[40:43], v[16:19], a[16:23], v75\n"
        "s_mov_b32 s46, s47\n"
        "s_add_u32 s51, s45, s56\n"
        "s_add_u32 s57, s44, 1\n"
        "v_smfmac_f32_16x16x64_bf16 v[28:31], v[16:19], v[120:127], v75\n"
        "v_readlane_b32 s45, v85, s57\n"
        "s_lshl_b32 s58, s44, 18\n"
        "s_add_u32 s53, s54, s58\n"
        "v_smfmac_f32_16x16x64_bf16 v[32:35], v[16:19], v[176:183], v75\n"
        "s_xor_b32 s47, s45, 0x84000\n"
        "s_andn2_b32 s45, s45, 0x1000\n"
        "s_add_u32 s49, s47, 32768\n"
        "v_smfmac_f32_16x16x64_bf16 v[36:39], v[16:19], v[232:239], v75\n"
        "s_waitcnt lgkmcnt(3)\n"
        "v_smfmac_f32_16x16x64_bf16 v[40:43], v[20:23], a[24:31], v75\n"
        "v_smfmac_f32_16x16x64_bf16 v[28:31], v[20:23], v[128:135], v75\n"
        "s_waitcnt lgkmcnt(2)\n"
        "v_smfmac_f32_16x16x64_bf16 v[28:31], v[0:3], v[88:95], v75\n"
        "v_pk_add_f32 v[40:41], v[40:41], v[42:43]\n"
        "s_waitcnt lgkmcnt(1)\n"
        "v_smfmac_f32_16x16x64_bf16 v[28:31], v[4:7], v[96:103], v75\n"
        "v_cvt_pk_bf16_f32 v40, v40, v41\n"
        "s_waitcnt lgkmcnt(0)\n"
        "v_smfmac_f32_16x16x64_bf16 v[28:31], v[12:15], v[112:119], v75\n"
        "ds_write_b32 v74, v40 offset:3200\n"
        "v_smfmac_f32_16x16x64_bf16 v[32:35], v[20:23], v[184:191], v75\n"
        "v_smfmac_f32_16x16x64_bf16 v[32:35], v[0:3], v[144:151], v75\n"
        "v_pk_add_f32 v[28:29], v[28:29], v[30:31]\n"
        "v_smfmac_f32_16x16x64_bf16 v[32:35], v[4:7], v[152:159], v75\n"
        "v_cvt_pk_bf16_f32 v28, v28, v29\n"
        "v_smfmac_f32_16x16x64_bf16 v[32:35], v[12:15], v[168:175], v75\n"
        "ds_write_b32 v74, v28 offset:256\n"
        "v_smfmac_f32_16x16x64_bf16 v[36:39], v[20:23], v[240:247], v75\n"
        "v_smfmac_f32_16x16x64_bf16 v[36:39], v[0:3], v[200:207], v75\n"
        "v_pk_add_f32 v[32:33], v[32:33], v[34:35]\n"
        "v_smfmac_f32_16x16x64_bf16 v[36:39], v[4:7], v[208:215], v75\n"
        "v_cvt_pk_bf16_f32 v32, v32, v33\n"
        "v_smfmac_f32_16x16x64_bf16 v[36:39], v[12:15], v[224:231], v75\n"
        "ds_write_b32 v74, v32 offset:1280\n"
        "v_smfmac_f32_16x16x64_bf16 v[44:47], v[0:3], a[32:39], v75\n"
        "s_nop 3\n"
        "v_pk_add_f32 v[36:37], v[36:37], v[38:39]\n"
        "v_cvt_pk_bf16_f32 v36, v36, v37\n"
        "ds_write_b32 v74, v36 offset:2304\n"
        "v_smfmac_f32_16x16x64_bf16 v[44:47], v[4:7], a[40:47], v75\n"
        "v_smfmac_f32_16x16x64_bf16 v[44:47], v[8:11], a[48:55], v75\n"
        "s_waitcnt vmcnt(2) lgkmcnt(0)\n"
        "s_barrier\n"
        "ds_read_b64_tr_b16 v[68:69], v73 offset:2048\n"
        "ds_read_b64_tr_b16 v[70:71], v73 offset:2560\n"
        "v_smfmac_f32_16x16x64_bf16 v[44:47], v[12:15], a[56:63], v75\n"
        "v_mov_b32 v60, 0\n"
        "v_smfmac_f32_16x16x64_bf16 v[44:47], v[16:19], a[64:71], v75\n"
        "v_mov_b32 v61, 0\n"
        "v_smfmac_f32_16x16x64_bf16 v[44:47], v[20:23], a[72:79], v75\n"
        "v_mov_b32 v62, 0\n"
        "v_smfmac_f32_16x16x64_bf16 v[44:47], v[24:27], a[80:87], v75\n"
        "v_mov_b32 v63, 0\n"
        "v_smfmac_f32_16x16x64_bf16 v[48:51], v[0:3], a[88:95], v75\n"
        "v_smfmac_f32_16x16x64_bf16 v[48:51], v[4:7], a[96:103], v75\n"
        "v_smfmac_f32_16x16x64_bf16 v[48:51], v[8:11], a[104:111], v75\n"
        "v_pk_add_f32 v[44:45], v[44:45], v[46:47]\n"
        "v_smfmac_f32_16x16x64_bf16 v[48:51], v[12:15], a[112:119], v75\n"
        "v_cvt_pk_bf16_f32 v64, v44, v45\n"
        "v_smfmac_f32_16x16x64_bf16 v[48:51], v[16:19], a[120:127], v75\n"
        "ds_write_b32 v78, v64 offset:0\n"
        "v_smfmac_f32_16x16x64_bf16 v[48:51], v[20:23], a[128:135], v75\n"
        "v_smfmac_f32_16x16x64_bf16 v[48:51], v[24:27], a[136:143], v75\n"
        "v_smfmac_f32_16x16x64_bf16 v[52:55], v[0:3], a[144:151], v75\n"
        "v_smfmac_f32_16x16x64_bf16 v[52:55], v[4:7], a[152:159], v75\n"
        "v_smfmac_f32_16x16x64_bf16 v[52:55], v[8:11], a[160:167], v75\n"
        "v_pk_add_f32 v[48:49], v[48:49], v[50:51]\n"
        "v_smfmac_f32_16x16x64_bf16 v[52:55], v[12:15], a[168:175], v75\n"
        "v_cvt_pk_bf16_f32 v65, v48, v49\n"
        "v_smfmac_f32_16x16x64_bf16 v[52:55], v[16:19], a[176:183], v75\n"
        "ds_write_b32 v78, v65 offset:256\n"
        "v_smfmac_f32_16x16x64_bf16 v[52:55], v[20:23], a[184:191], v75\n"
        "v_smfmac_f32_16x16x64_bf16 v[52:55], v[24:27], a[192:199], v75\n"
        "v_smfmac_f32_16x16x64_bf16 v[56:59], v[0:3], a[200:207], v75\n"
        "v_smfmac_f32_16x16x64_bf16 v[56:59], v[4:7], a[208:215], v75\n"
        "v_smfmac_f32_16x16x64_bf16 v[56:59], v[8:11], a[216:223], v75\n"
        "v_pk_add_f32 v[52:53], v[52:53], v[54:55]\n"
        "v_smfmac_f32_16x16x64_bf16 v[56:59], v[12:15], a[224:231], v75\n"
        "v_cvt_pk_bf16_f32 v66, v52, v53\n"
        "v_smfmac_f32_16x16x64_bf16 v[56:59], v[16:19], a[232:239], v75\n"
        "ds_write_b32 v78, v66 offset:512\n"
        "buffer_load_dwordx2 v[40:41], v77, s[40:43], s46 offen sc1\n"
        "buffer_load_dwordx4 v[28:31], v76, s[40:43], s46 offen sc1\n"
        "buffer_load_dwordx4 v[32:35], v76, s[40:43], s46 offen offset:256 sc1\n"
        "v_smfmac_f32_16x16x64_bf16 v[56:59], v[20:23], a[240:247], v75\n"
        "v_smfmac_f32_16x16x64_bf16 v[56:59], v[24:27], a[248:255], v75\n"
        "s_nop 7\n"
        "v_pk_add_f32 v[56:57], v[56:57], v[58:59]\n"
        "v_cvt_pk_bf16_f32 v67, v56, v57\n"
        "ds_write_b32 v78, v67 offset:768\n"
        "s_waitcnt lgkmcnt(0)\n"
        "ds_read_b128 v[8:11], v72 offset:11264\n"
        "s_cmp_eq_u32 s44, 63\n"
        "s_cbranch_scc1 .Lr2_exit_%=\n"
        "s_add_u32 s44, s44, 1\n"
        "s_mov_b32 s57, 0\n"
        "s_mov_b32 s58, 0\n"
        "buffer_load_dwordx4 v[36:39], v72, s[40:43], s51 offen sc1\n"
        ".Lr2_swait_%=:\n"
        "s_waitcnt vmcnt(1) lgkmcnt(0)\n"
        ".Lr2_schain_%=:\n"
        "v_pk_add_f32 v[0:1], v[28:29], v[32:33]\n"
        "v_pk_add_f32 v[2:3], v[30:31], v[34:35]\n"
        "v_mov_b32 v13, v40\n"
        "v_max3_f32 v4, v0, v1, v13\n"
        "v_cmp_u_f32 vcc, v0, v13\n"
        "v_max3_f32 v4, v2, v3, v4\n"
        "s_cbranch_vccnz .Lr2_sbad_%=\n"
        ".Lr2_sgo_%=:\n"
        "s_nop 0\n"
        "v_max_f32_dpp v4, v4, v4 quad_perm:[2,3,0,1] row_mask:0xf bank_mask:0xf\n"
        "v_sub_f32 v12, v13, v4\n"
        "v_pk_add_f32 v[0:1], v[0:1], v[4:5] op_sel_hi:[1,0] neg_lo:[0,1] neg_hi:[0,1]\n"
        "v_exp_f32 v12, v12\n"
        "v_pk_add_f32 v[2:3], v[2:3], v[4:5] op_sel_hi:[1,0] neg_lo:[0,1] neg_hi:[0,1]\n"
        "v_exp_f32 v0, v0\n"
        "v_exp_f32 v1, v1\n"
        "v_exp_f32 v2, v2\n"
        "v_exp_f32 v3, v3\n"
        "v_fma_f32 v6, v12, v41, v0\n"
        "v_add_f32 v6, v6, v1\n"
        "v_add_f32 v7, v2, v3\n"
        "v_add_f32 v6, v6, v7\n"
        "s_nop 1\n"
        "v_add_f32_dpp v6, v6, v6 quad_perm:[2,3,0,1] row_mask:0xf bank_mask:0xf\n"
        "v_rcp_f32 v6, v6\n"
        "s_nop 0\n"
        "v_pk_mul_f32 v[0:1], v[0:1], v[6:7] op_sel_hi:[1,0]\n"
        "v_pk_mul_f32 v[2:3], v[2:3], v[6:7] op_sel_hi:[1,0]\n"
        "v_cvt_pk_bf16_f32 v0, v0, v1\n"
        "v_cvt_pk_bf16_f32 v1, v2, v3\n"
        "v_mul_f32 v12, v12, v6\n"
        "v_cvt_pk_bf16_f32 v12, v12, v12\n"
        ".Lr2_redo_%=:\n"
        "v_smfmac_f32_16x16x32_bf16 v[60:63], v[0:1], v[8:11], v75\n"
        "v_and_b32 v2, v12, v79\n"
        "v_and_b32 v3, v12, v80\n"
        "s_waitcnt vmcnt(0)\n"
        "s_nop 0\n"
        "v_smfmac_f32_16x16x32_bf16 v[60:63], v[2:3], v[36:39], v75\n"
        "s_nop 7\n"
        "v_pk_add_f32 v[4:5], v[60:61], v[62:63]\n"
        "ds_write2_b32 v82, v4, v5 offset0:4 offset1:36\n"
        "v_cmp_u_f32 vcc, v4, v5\n"
        "s_cbranch_vccnz .Lr2_obad_%=\n"
        ".Lr2_sok_%=:\n"
        "s_waitcnt lgkmcnt(0)\n"
        "s_barrier\n"
        "s_branch .Lr2_loop_%=\n"
        ".Lr2_rm_%=:\n"
        "s_add_u32 s59, s59, 1\n"
        "s_add_u32 s57, s57, 1\n"
        "s_cmp_eq_u32 s55, 0\n"
        "s_cbranch_scc1 .Lr2_mok_%=\n"
        "s_sub_u32 s55, s55, 1\n"
        "s_waitcnt lgkmcnt(0)\n"
        "buffer_load_dwordx4 v[8:11], v72, s[40:43], s47 offen sc1\n"
        "buffer_load_dwordx4 v[24:27], v72, s[40:43], s49 offen sc1\n"
        "s_nop 7\n"
        "v_mov_b32 v28, 0\n"
        "v_mov_b32 v29, 0\n"
        "v_mov_b32 v30, 0\n"
        "v_mov_b32 v31, 0\n"
        "v_mov_b32 v32, 0\n"
        "v_mov_b32 v33, 0\n"
        "v_mov_b32 v34, 0\n"
        "v_mov_b32 v35, 0\n"
        "s_waitcnt vmcnt(0)\n"
        "s_branch .Lr2_mwait_%=\n"
        ".Lr2_sbad_%=:\n"
        "s_add_u32 s57, s57, 1\n"
        "s_cmp_eq_u32 s55, 0\n"
        "s_cbranch_scc1 .Lr2_sgo_%=\n"
        ".Lr2_sslow_%=:\n"
        "s_sub_u32 s55, s55, 1\n"
        "buffer_load_dwordx4 v[28:31], v76, s[40:43], s46 offen sc1\n"
        "buffer_load_dwordx4 v[32:35], v76, s[40:43], s46 offen offset:256 sc1\n"
        "buffer_load_dwordx2 v[40:41], v77, s[40:43], s46 offen sc1\n"
        "s_waitcnt vmcnt(0)\n"
        "s_branch .Lr2_schain_%=\n"
        ".Lr2_obad_%=:\n"
        "s_cmp_eq_u32 s55, 0\n"
        "s_cbranch_scc1 .Lr2_sok_%=\n"
        "v_cmp_u_f32 vcc, v6, v6\n"
        "s_add_u32 s58, s58, 1\n"
        "v_mov_b32 v60, 0\n"
        "v_mov_b32 v61, 0\n"
        "v_mov_b32 v62, 0\n"
        "v_mov_b32 v63, 0\n"
        "s_cbranch_vccnz .Lr2_sslow_%=\n"
        "s_sub_u32 s55, s55, 1\n"
        "buffer_load_dwordx4 v[36:39], v72, s[40:43], s51 offen sc1\n"
        "s_waitcnt vmcnt(0)\n"
        "s_nop 0\n"
        "s_branch .Lr2_redo_%=\n"
        ".Lr2_exit_%=:\n"
        "s_nop 7\n"
        "buffer_store_dwordx4 v[64:67], v72, s[40:43], s53 offen sc1\n"
        "buffer_store_dwordx4 v[68:71], v72, s[40:43], s53 offen offset:1024 sc1\n"
        "s_waitcnt vmcnt(0) lgkmcnt(0)\n"
        "s_barrier\n"
        "s_branch .Lrend_%=\n"
        ".Lr3_entry_%=:\n"
        "v_mbcnt_lo_u32_b32 v0, -1, 0\n"
        "v_mbcnt_hi_u32_b32 v0, -1, v0\n"
        "v_lshlrev_b32 v0, 2, v0\n"
        "ds_read_b32 v72, v0 offset:18432\n"
        "ds_read_b32 v73, v0 offset:19456\n"
        "ds_read_b32 v74, v0 offset:20480\n"
        "ds_read_b32 v75, v0 offset:21504\n"
        "ds_read_b32 v76, v0 offset:22528\n"
        "ds_read_b32 v77, v0 offset:23552\n"
        "ds_read_b32 v78, v0 offset:24576\n"
        "ds_read_b32 v79, v0 offset:25600\n"
        "ds_read_b32 v80, v0 offset:26624\n"
        "ds_read_b32 v81, v0 offset:27648\n"
        "ds_read_b32 v82, v0 offset:28672\n"
        "ds_read_b32 v83, v0 offset:29696\n"
        "ds_read_b32 v84, v0 offset:30720\n"
        "ds_read_b32 v85, v0 offset:31744\n"
        "s_waitcnt lgkmcnt(0)\n"
        "ds_read_b32 v1, v0 offset:32768\n"
        "ds_read_b32 v2, v0 offset:33792\n"
        "ds_read_b32 v3, v0 offset:34816\n"
        "ds_read_b32 v4, v0 offset:35840\n"
        "ds_read_b32 v5, v0 offset:36864\n"
        "ds_read_b32 v6, v0 offset:37888\n"
        "ds_read_b32 v7, v0 offset:38912\n"
        "ds_read_b32 v8, v0 offset:39936\n"
        "ds_read_b32 v9, v0 offset:40960\n"
        "s_waitcnt lgkmcnt(0)\n"
        "buffer_load_dword v10, v9, s[40:43], 0 offen\n"
        "v_readlane_b32 s45, v85, 0\n"
        "s_xor_b32 s47, s45, 0x84000\n"
        "s_andn2_b32 s45, s45, 0x1000\n"
        "s_add_u32 s49, s47, 4294964224\n"
        "s_getpc_b64 s[58:59]\n"
        "v_lshlrev_b32 v12, 5, v0\n"
        "v_min_u32 v12, .Lr3_exit_%=-.Lr3_loop_%=-64, v12\n"
        "v_add_u32 v12, .Lr3_loop_%=-.Lr3_entry_%=, v12\n"
        "global_load_dword v12, v12, s[58:59]\n"
        "s_mov_b32 s59, 0\n"
        "s_mov_b32 s60, 0\n"
        "s_mov_b32 s61, 4096\n"
        "s_mov_b32 s62, 8192\n"
        "s_mov_b32 s63, 12288\n"
        "s_mov_b32 s64, 16384\n"
        "s_mov_b32 s65, 20480\n"
        "s_mov_b32 s66, 24576\n"
        "buffer_load_dwordx4 v[88:91], v1, s[40:43], s60 offen\n"
        "buffer_load_dwordx4 v[92:95], v1, s[40:43], s60 offen offset:2048\n"
        "buffer_load_dwordx4 v[96:99], v1, s[40:43], s61 offen\n"
        "buffer_load_dwordx4 v[100:103], v1, s[40:43], s61 offen offset:2048\n"
        "buffer_load_dwordx4 v[104:107], v1, s[40:43], s62 offen\n"
        "buffer_load_dwordx4 v[108:111], v1, s[40:43], s62 offen offset:2048\n"
        "buffer_load_dwordx4 v[112:115], v1, s[40:43], s63 offen\n"
        "buffer_load_dwordx4 v[116:119], v1, s[40:43], s63 offen offset:2048\n"
        "buffer_load_dwordx4 v[120:123], v1, s[40:43], s64 offen\n"
        "buffer_load_dwordx4 v[124:127], v1, s[40:43], s64 offen offset:2048\n"
        "buffer_load_dwordx4 v[128:131], v1, s[40:43], s65 offen\n"
        "buffer_load_dwordx4 v[132:135], v1, s[40:43], s65 offen offset:2048\n"
        "buffer_load_dwordx4 v[136:139], v1, s[40:43], s66 offen\n"
        "buffer_load_dwordx4 v[140:143], v1, s[40:43], s66 offen offset:2048\n"
        "buffer_load_dwordx4 v[144:147], v2, s[40:43], s60 offen\n"
        "buffer_load_dwordx4 v[148:151], v2, s[40:43], s60 offen offset:2048\n"
        "buffer_load_dwordx4 v[152:155], v2, s[40:43], s61 offen\n"
        "buffer_load_dwordx4 v[156:159], v2, s[40:43], s61 offen offset:2048\n"
        "buffer_load_dwordx4 v[160:163], v2, s[40:43], s62 offen\n"
        "buffer_load_dwordx4 v[164:167], v2, s[40:43], s62 offen offset:2048\n"
        "buffer_load_dwordx4 v[168:171], v2, s[40:43], s63 offen\n"
        "buffer_load_dwordx4 v[172:175], v2, s[40:43], s63 offen offset:2048\n"
        "buffer_load_dwordx4 v[176:179], v2, s[40:43], s64 offen\n"
        "buffer_load_dwordx4 v[180:183], v2, s[40:43], s64 offen offset:2048\n"
        "buffer_load_dwordx4 v[184:187], v2, s[40:43], s65 offen\n"
        "buffer_load_dwordx4 v[188:191], v2, s[40:43], s65 offen offset:2048\n"
        "buffer_load_dwordx4 v[192:195], v2, s[40:43], s66 offen\n"
        "buffer_load_dwordx4 v[196:199], v2, s[40:43], s66 offen offset:2048\n"
        "buffer_load_dwordx4 v[200:203], v3, s[40:43], s60 offen\n"
        "buffer_load_dwordx4 v[204:207], v3, s[40:43], s60 offen offset:2048\n"
        "buffer_load_dwordx4 v[208:211], v3, s[40:43], s61 offen\n"
        "buffer_load_dwordx4 v[212:215], v3, s[40:43], s61 offen offset:2048\n"
        "buffer_load_dwordx4 v[216:219], v3, s[40:43], s62 offen\n"
        "buffer_load_dwordx4 v[220:223], v3, s[40:43], s62 offen offset:2048\n"
        "buffer_load_dwordx4 v[224:227], v3, s[40:43], s63 offen\n"
        "buffer_load_dwordx4 v[228:231], v3, s[40:43], s63 offen offset:2048\n"
        "buffer_load_dwordx4 v[232:235], v3, s[40:43], s64 offen\n"
        "buffer_load_dwordx4 v[236:239], v3, s[40:43], s64 offen offset:2048\n"
        "buffer_load_dwordx4 v[240:243], v3, s[40:43], s65 offen\n"
        "buffer_load_dwordx4 v[244:247], v3, s[40:43], s65 offen offset:2048\n"
        "buffer_load_dwordx4 v[248:251], v3, s[40:43], s66 offen\n"
        "buffer_load_dwordx4 v[252:255], v3, s[40:43], s66 offen offset:2048\n"
        "buffer_load_dwordx4 a[0:3], v4, s[40:43], s63 offen\n"
        "buffer_load_dwordx4 a[4:7], v4, s[40:43], s63 offen offset:2048\n"
        "buffer_load_dwordx4 a[8:11], v4, s[40:43], s60 offen\n"
        "buffer_load_dwordx4 a[12:15], v4, s[40:43], s60 offen offset:2048\n"
        "buffer_load_dwordx4 a[16:19], v4, s[40:43], s61 offen\n"
        "buffer_load_dwordx4 a[20:23], v4, s[40:43], s61 offen offset:2048\n"
        "buffer_load_dwordx4 a[32:35], v5, s[40:43], s60 offen\n"
        "buffer_load_dwordx4 a[36:39], v5, s[40:43], s60 offen offset:2048\n"
        "buffer_load_dwordx4 a[40:43], v5, s[40:43], s61 offen\n"
        "buffer_load_dwordx4 a[44:47], v5, s[40:43], s61 offen offset:2048\n"
        "buffer_load_dwordx4 a[48:51], v5, s[40:43], s62 offen\n"
        "buffer_load_dwordx4 a[52:55], v5, s[40:43], s62 offen offset:2048\n"
        "buffer_load_dwordx4 a[56:59], v5, s[40:43], s63 offen\n"
        "buffer_load_dwordx4 a[60:63], v5, s[40:43], s63 offen offset:2048\n"
        "buffer_load_dwordx4 a[64:67], v5, s[40:43], s64 offen\n"
        "buffer_load_dwordx4 a[68:71], v5, s[40:43], s64 offen offset:2048\n"
        "buffer_load_dwordx4 a[72:75], v5, s[40:43], s65 offen\n"
        "buffer_load_dwordx4 a[76:79], v5, s[40:43], s65 offen offset:2048\n"
        "buffer_load_dwordx4 a[80:83], v5, s[40:43], s66 offen\n"
        "buffer_load_dwordx4 a[84:87], v5, s[40:43], s66 offen offset:2048\n"
        "buffer_load_dwordx4 a[88:91], v6, s[40:43], s60 offen\n"
        "buffer_load_dwordx4 a[92:95], v6, s[40:43], s60 offen offset:2048\n"
        "buffer_load_dwordx4 a[96:99], v6, s[40:43], s61 offen\n"
        "buffer_load_dwordx4 a[100:103], v6, s[40:43], s61 offen offset:2048\n"
        "buffer_load_dwordx4 a[104:107], v6, s[40:43], s62 offen\n"
        "buffer_load_dwordx4 a[108:111], v6, s[40:43], s62 offen offset:2048\n"
        "buffer_load_dwordx4 a[112:115], v6, s[40:43], s63 offen\n"
        "buffer_load_dwordx4 a[116:119], v6, s[40:43], s63 offen offset:2048\n"
        "buffer_load_dwordx4 a[120:123], v6, s[40:43], s64 offen\n"
        "buffer_load_dwordx4 a[124:127], v6, s[40:43], s64 offen offset:2048\n"
        "buffer_load_dwordx4 a[128:131], v6, s[40:43], s65 offen\n"
        "buffer_load_dwordx4 a[132:135], v6, s[40:43], s65 offen offset:2048\n"
        "buffer_load_dwordx4 a[136:139], v6, s[40:43], s66 offen\n"
        "buffer_load_dwordx4 a[140:143], v6, s[40:43], s66 offen offset:2048\n"
        "buffer_load_dwordx4 a[144:147], v7, s[40:43], s60 offen\n"
        "buffer_load_dwordx4 a[148:151], v7, s[40:43], s60 offen offset:2048\n"
        "buffer_load_dwordx4 a[152:155], v7, s[40:43], s61 offen\n"
        "buffer_load_dwordx4 a[156:159], v7, s[40:43], s61 offen offset:2048\n"
        "buffer_load_dwordx4 a[160:163], v7, s[40:43], s62 offen\n"
        "buffer_load_dwordx4 a[164:167], v7, s[40:43], s62 offen offset:2048\n"
        "buffer_load_dwordx4 a[168:171], v7, s[40:43], s63 offen\n"
        "buffer_load_dwordx4 a[172:175], v7, s[40:43], s63 offen offset:2048\n"
        "buffer_load_dwordx4 a[176:179], v7, s[40:43], s64 offen\n"
        "buffer_load_dwordx4 a[180:183], v7, s[40:43], s64 offen offset:2048\n"
        "buffer_load_dwordx4 a[184:187], v7, s[40:43], s65 offen\n"
        "buffer_load_dwordx4 a[188:191], v7, s[40:43], s65 offen offset:2048\n"
        "buffer_load_dwordx4 a[192:195], v7, s[40:43], s66 offen\n"
        "buffer_load_dwordx4 a[196:199], v7, s[40:43], s66 offen offset:2048\n"
        "buffer_load_dwordx4 a[200:203], v8, s[40:43], s60 offen\n"
        "buffer_load_dwordx4 a[204:207], v8, s[40:43], s60 offen offset:2048\n"
        "buffer_load_dwordx4 a[208:211], v8, s[40:43], s61 offen\n"
        "buffer_load_dwordx4 a[212:215], v8, s[40:43], s61 offen offset:2048\n"
        "buffer_load_dwordx4 a[216:219], v8, s[40:43], s62 offen\n"
        "buffer_load_dwordx4 a[220:223], v8, s[40:43], s62 offen offset:2048\n"
        "buffer_load_dwordx4 a[224:227], v8, s[40:43], s63 offen\n"
        "buffer_load_dwordx4 a[228:231], v8, s[40:43], s63 offen offset:2048\n"
        "buffer_load_dwordx4 a[232:235], v8, s[40:43], s64 offen\n"
        "buffer_load_dwordx4 a[236:239], v8, s[40:43], s64 offen offset:2048\n"
        "buffer_load_dwordx4 a[240:243], v8, s[40:43], s65 offen\n"
        "buffer_load_dwordx4 a[244:247], v8, s[40:43], s65 offen offset:2048\n"
        "buffer_load_dwordx4 a[248:251], v8, s[40:43], s66 offen\n"
        "buffer_load_dwordx4 a[252:255], v8, s[40:43], s66 offen offset:2048\n"
        "s_add_u32 s53, s53, 3072\n"
        "s_add_u32 s54, s54, 3072\n"
        "v_mov_b32 v28, 0\n"
        "v_mov_b32 v29, 0\n"
        "v_mov_b32 v30, 0\n"
        "v_mov_b32 v31, 0\n"
        "v_mov_b32 v32, 0\n"
        "v_mov_b32 v33, 0\n"
        "v_mov_b32 v34, 0\n"
        "v_mov_b32 v35, 0\n"
        "v_mov_b32 v36, 0\n"
        "v_mov_b32 v37, 0\n"
        "v_mov_b32 v38, 0\n"
        "v_mov_b32 v39, 0\n"
        "v_mov_b32 v40, 0\n"
        "v_mov_b32 v41, 0\n"
        "v_mov_b32 v42, 0\n"
        "v_mov_b32 v43, 0\n"
        "v_mov_b32 v44, 0\n"
        "v_mov_b32 v45, 0\n"
        "v_mov_b32 v46, 0\n"
        "v_mov_b32 v47, 0\n"
        "v_mov_b32 v48, 0\n"
        "v_mov_b32 v49, 0\n"
        "v_mov_b32 v50, 0\n"
        "v_mov_b32 v51, 0\n"
        "v_mov_b32 v52, 0\n"
        "v_mov_b32 v53, 0\n"
        "v_mov_b32 v54, 0\n"
        "v_mov_b32 v55, 0\n"
        "v_mov_b32 v56, 0\n"
        "v_mov_b32 v57, 0\n"
        "v_mov_b32 v58, 0\n"
        "v_mov_b32 v59, 0\n"
        "v_mov_b32 v60, 0\n"
        "v_mov_b32 v61, 0\n"
        "v_mov_b32 v62, 0\n"
        "v_mov_b32 v63, 0\n"
        "v_mov_b32 v64, 0\n"
        "v_mov_b32 v65, 0\n"
        "v_mov_b32 v66, 0\n"
        "v_mov_b32 v67, 0\n"
        "v_mov_b32 v68, 0\n"
        "v_mov_b32 v69, 0\n"
        "v_mov_b32 v70, 0\n"
        "v_mov_b32 v71, 0\n"
        "buffer_load_dword v10, v9, s[40:43], 0 offen\n"
        "s_waitcnt vmcnt(0)\n"
        "s_mov_b32 s50, 0\n"
        "s_mov_b32 s66, 0x11111111\n"
        "s_mov_b32 s67, 0x11111111\n"
        "s_branch .Lr3_poll_%=\n"
        ".Lr3_loop_%=:\n"
        "s_waitcnt vmcnt(1)\n"
        "v_mov_b32 v28, 0\n"
        "v_mov_b32 v29, 0\n"
        "v_mov_b32 v30, 0\n"
        "v_mov_b32 v31, 0\n"
        "v_mov_b32 v32, 0\n"
        "v_mov_b32 v33, 0\n"
        "v_mov_b32 v34, 0\n"
        "v_mov_b32 v35, 0\n"
        "v_mov_b32 v36, 0\n"
        "v_mov_b32 v37, 0\n"
        "v_mov_b32 v38, 0\n"
        "v_mov_b32 v39, 0\n"
        "v_mov_b32 v40, 0\n"
        "v_mov_b32 v41, 0\n"
        "v_mov_b32 v42, 0\n"
        "v_mov_b32 v43, 0\n"
        "v_mov_b32 v44, 0\n"
        "v_mov_b32 v45, 0\n"
        "v_mov_b32 v46, 0\n"
        "v_mov_b32 v47, 0\n"
        "v_mov_b32 v48, 0\n"
        "v_mov_b32 v49, 0\n"
        "v_mov_b32 v50, 0\n"
        "v_mov_b32 v51, 0\n"
        "v_mov_b32 v52, 0\n"
        "v_mov_b32 v53, 0\n"
        "v_mov_b32 v54, 0\n"
        "v_mov_b32 v55, 0\n"
        "v_mov_b32 v56, 0\n"
        "v_mov_b32 v57, 0\n"
        "v_mov_b32 v58, 0\n"
        "v_mov_b32 v59, 0\n"
        "v_mov_b32 v85, v85\n"
        "v_mov_b32 v85, v85\n"
        "v_mov_b32 v85, v85\n"
        "v_mov_b32 v85, v85\n"
        "v_mov_b32 v85, v85\n"
        "v_mov_b32 v85, v85\n"
        "v_mov_b32 v85, v85\n"
        "v_mov_b32 v85, v85\n"
        "v_mov_b32 v85, v85\n"
        "v_mov_b32 v85, v85\n"
        "v_mov_b32 v85, v85\n"
        "v_mov_b32 v85, v85\n"
        "v_mov_b32 v85, v85\n"
        "v_mov_b32 v85, v85\n"
        "v_mov_b32 v85, v85\n"
        "v_mov_b32 v85, v85\n"
        "v_mov_b32 v85, v85\n"
        "v_mov_b32 v85, v85\n"
        "v_mov_b32 v85, v85\n"
        "v_mov_b32 v85, v85\n"
        "v_mov_b32 v85, v85\n"
        "v_mov_b32 v85, v85\n"
        "v_mov_b32 v85, v85\n"
        "v_mov_b32 v85, v85\n"
        "v_mov_b32 v85, v85\n"
        "v_mov_b32 v85, v85\n"
        "v_mov_b32 v85, v85\n"
        "v_mov_b32 v85, v85\n"
        "v_mov_b32 v85, v85\n"
        "v_mov_b32 v85, v85\n"
        "v_mov_b32 v85, v85\n"
        "v_mov_b32 v85, v85\n"
        "v_mov_b32 v85, v85\n"
        "v_mov_b32 v85, v85\n"
        "v_mov_b32 v85, v85\n"
        "v_mov_b32 v85, v85\n"
        "v_mov_b32 v85, v85\n"
        "v_mov_b32 v85, v85\n"
        "v_mov_b32 v85, v85\n"
        "v_mov_b32 v85, v85\n"
        "v_mov_b32 v85, v85\n"
        "v_mov_b32 v85, v85\n"
        "v_mov_b32 v85, v85\n"
        "v_mov_b32 v85, v85\n"
        "v_mov_b32 v85, v85\n"
        "v_mov_b32 v85, v85\n"
        "v_mov_b32 v85, v85\n"
        "v_mov_b32 v85, v85\n"
        "v_mov_b32 v85, v85\n"
        "v_mov_b32 v85, v85\n"
        "v_mov_b32 v85, v85\n"
        "v_mov_b32 v85, v85\n"
        "v_mov_b32 v85, v85\n"
        "v_mov_b32 v85, v85\n"
        "v_mov_b32 v85, v85\n"
        "v_mov_b32 v85, v85\n"
        "v_mov_b32 v85, v85\n"
        "v_mov_b32 v85, v85\n"
        "v_mov_b32 v85, v85\n"
        "v_mov_b32 v85, v85\n"
        "v_mov_b32 v85, v85\n"
        "v_mov_b32 v85, v85\n"
        "v_mov_b32 v85, v85\n"
        "v_mov_b32 v85, v85\n"
        "v_mov_b32 v85, v85\n"
        "v_mov_b32 v85, v85\n"
        "v_mov_b32 v85, v85\n"
        "v_mov_b32 v85, v85\n"
        "v_mov_b32 v85, v85\n"
        "v_mov_b32 v85, v85\n"
        "v_mov_b32 v85, v85\n"
        "v_mov_b32 v85, v85\n"
        "v_mov_b32 v85, v85\n"
        "v_mov_b32 v85, v85\n"
        "v_mov_b32 v85, v85\n"
        "v_mov_b32 v85, v85\n"
        "v_mov_b32 v85, v85\n"
        "v_mov_b32 v85, v85\n"
        ".Lr3_poll_%=:\n"
        "buffer_load_dwordx4 v[12:15], v72, s[40:43], s47 offen sc1\n"
        "buffer_load_dwordx4 v[0:3], v72, s[40:43], s49 offen sc1\n"
        "s_waitcnt vmcnt(0)\n"
        ".Lr3_mwait_%=:\n"
        "v_max3_u32 v60, v12, v13, v14\n"
        "v_max3_u32 v61, v15, v0, v1\n"
        "v_max3_u32 v60, v60, v2, v3\n"
        "ds_write_b128 v72, v[12:15] offset:4096\n"
        "v_smfmac_f32_16x16x64_bf16 v[28:31], v[12:15], v[112:119], v75\n"
        "v_max_u32 v60, v60, v61\n"
        "v_cmp_eq_u32 vcc, -1, v60\n"
        "v_smfmac_f32_16x16x64_bf16 v[32:35], v[12:15], v[168:175], v75\n"
        "s_cbranch_vccnz .Lr3_rm_%=\n"
        ".Lr3_mok_%=:\n"
        "v_smfmac_f32_16x16x64_bf16 v[36:39], v[12:15], v[224:231], v75\n"
        "v_smfmac_f32_16x16x64_bf16 v[40:43], v[12:15], a[0:7], v75\n"
        "s_waitcnt lgkmcnt(0)\n"
        "s_barrier\n"
        "ds_read_b128 v[4:7], v72 offset:2048\n"
        "ds_read_b128 v[8:11], v72 offset:3072\n"
        "ds_read_b128 v[16:19], v72 offset:5120\n"
        "ds_read_b128 v[20:23], v72 offset:6144\n"
        "ds_read_b128 v[24:27], v72 offset:7168\n"
        "v_smfmac_f32_16x16x64_bf16 v[28:31], v[0:3], v[88:95], v75\n"
        "buffer_store_dwordx4 v[64:67], v72, s[40:43], s53 offen sc1\n"
        "v_smfmac_f32_16x16x64_bf16 v[32:35], v[0:3], v[144:151], v75\n"
        "buffer_store_dwordx4 v[68:71], v72, s[40:43], s53 offen offset:1024 sc1\n"
        "v_smfmac_f32_16x16x64_bf16 v[36:39], v[0:3], v[200:207], v75\n"
        "s_lshl_b32 s58, s44, 12\n"
        "buffer_store_dword v75, v83, s[40:43], s58 offen sc1\n"
        "v_smfmac_f32_16x16x64_bf16 v[40:43], v[0:3], a[8:15], v75\n"
        "s_waitcnt lgkmcnt(4)\n"
        "v_smfmac_f32_16x16x64_bf16 v[40:43], v[4:7], a[16:23], v75\n"
        "s_mov_b32 s46, s47\n"
        "s_add_u32 s51, s45, s56\n"
        "s_add_u32 s57, s44, 1\n"
        "v_smfmac_f32_16x16x64_bf16 v[28:31], v[4:7], v[96:103], v75\n"
        "v_readlane_b32 s45, v85, s57\n"
        "s_lshl_b32 s58, s44, 18\n"
        "s_add_u32 s53, s54, s58\n"
        "v_smfmac_f32_16x16x64_bf16 v[32:35], v[4:7], v[152:159], v75\n"
        "s_xor_b32 s47, s45, 0x84000\n"
        "s_andn2_b32 s45, s45, 0x1000\n"
        "s_add_u32 s49, s47, 4294964224\n"
        "v_smfmac_f32_16x16x64_bf16 v[36:39], v[4:7], v[208:215], v75\n"
        "s_waitcnt lgkmcnt(3)\n"
        "v_smfmac_f32_16x16x64_bf16 v[28:31], v[8:11], v[104:111], v75\n"
        "s_waitcnt lgkmcnt(2)\n"
        "v_smfmac_f32_16x16x64_bf16 v[28:31], v[16:19], v[120:127], v75\n"
        "v_pk_add_f32 v[40:41], v[40:41], v[42:43]\n"
        "s_waitcnt lgkmcnt(1)\n"
        "v_smfmac_f32_16x16x64_bf16 v[28:31], v[20:23], v[128:135], v75\n"
        "v_cvt_pk_bf16_f32 v40, v40, v41\n"
        "s_waitcnt lgkmcnt(0)\n"
        "v_smfmac_f32_16x16x64_bf16 v[28:31], v[24:27], v[136:143], v75\n"
        "ds_write_b32 v74, v40 offset:3456\n"
        "v_smfmac_f32_16x16x64_bf16 v[32:35], v[8:11], v[160:167], v75\n"
        "v_smfmac_f32_16x16x64_bf16 v[32:35], v[16:19], v[176:183], v75\n"
        "v_pk_add_f32 v[28:29], v[28:29], v[30:31]\n"
        "v_smfmac_f32_16x16x64_bf16 v[32:35], v[20:23], v[184:191], v75\n"
        "v_cvt_pk_bf16_f32 v28, v28, v29\n"
        "v_smfmac_f32_16x16x64_bf16 v[32:35], v[24:27], v[192:199], v75\n"
        "ds_write_b32 v74, v28 offset:384\n"
        "v_smfmac_f32_16x16x64_bf16 v[36:39], v[8:11], v[216:223], v75\n"
        "v_smfmac_f32_16x16x64_bf16 v[36:39], v[16:19], v[232:239], v75\n"
        "v_pk_add_f32 v[32:33], v[32:33], v[34:35]\n"
        "v_smfmac_f32_16x16x64_bf16 v[36:39], v[20:23], v[240:247], v75\n"
        "v_cvt_pk_bf16_f32 v32, v32, v33\n"
        "v_smfmac_f32_16x16x64_bf16 v[36:39], v[24:27], v[248:255], v75\n"
        "ds_write_b32 v74, v32 offset:1408\n"
        "v_smfmac_f32_16x16x64_bf16 v[44:47], v[0:3], a[32:39], v75\n"
        "s_nop 3\n"
        "v_pk_add_f32 v[36:37], v[36:37], v[38:39]\n"
        "v_cvt_pk_bf16_f32 v36, v36, v37\n"
        "ds_write_b32 v74, v36 offset:2432\n"
        "v_smfmac_f32_16x16x64_bf16 v[44:47], v[4:7], a[40:47], v75\n"
        "v_smfmac_f32_16x16x64_bf16 v[44:47], v[8:11], a[48:55], v75\n"
        "s_waitcnt vmcnt(3) lgkmcnt(0)\n"
        "s_barrier\n"
        "ds_read_b64_tr_b16 v[68:69], v73 offset:3072\n"
        "ds_read_b64_tr_b16 v[70:71], v73 offset:3584\n"
        "v_smfmac_f32_16x16x64_bf16 v[44:47], v[12:15], a[56:63], v75\n"
        "v_mov_b32 v60, 0\n"
        "v_smfmac_f32_16x16x64_bf16 v[44:47], v[16:19], a[64:71], v75\n"
        "v_mov_b32 v61, 0\n"
        "v_smfmac_f32_16x16x64_bf16 v[44:47], v[20:23], a[72:79], v75\n"
        "v_mov_b32 v62, 0\n"
        "v_smfmac_f32_16x16x64_bf16 v[44:47], v[24:27], a[80:87], v75\n"
        "v_mov_b32 v63, 0\n"
        "v_smfmac_f32_16x16x64_bf16 v[48:51], v[0:3], a[88:95], v75\n"
        "v_smfmac_f32_16x16x64_bf16 v[48:51], v[4:7], a[96:103], v75\n"
        "v_smfmac_f32_16x16x64_bf16 v[48:51], v[8:11], a[104:111], v75\n"
        "v_pk_add_f32 v[44:45], v[44:45], v[46:47]\n"
        "v_smfmac_f32_16x16x64_bf16 v[48:51], v[12:15], a[112:119], v75\n"
        "v_cvt_pk_bf16_f32 v64, v44, v45\n"
        "v_smfmac_f32_16x16x64_bf16 v[48:51], v[16:19], a[120:127], v75\n"
        "ds_write_b32 v78, v64 offset:0\n"
        "v_smfmac_f32_16x16x64_bf16 v[48:51], v[20:23], a[128:135], v75\n"
        "v_smfmac_f32_16x16x64_bf16 v[48:51], v[24:27], a[136:143], v75\n"
        "v_smfmac_f32_16x16x64_bf16 v[52:55], v[0:3], a[144:151], v75\n"
        "v_smfmac_f32_16x16x64_bf16 v[52:55], v[4:7], a[152:159], v75\n"
        "v_smfmac_f32_16x16x64_bf16 v[52:55], v[8:11], a[160:167], v75\n"
        "v_pk_add_f32 v[48:49], v[48:49], v[50:51]\n"
        "v_smfmac_f32_16x16x64_bf16 v[52:55], v[12:15], a[168:175], v75\n"
        "v_cvt_pk_bf16_f32 v65, v48, v49\n"
        "v_smfmac_f32_16x16x64_bf16 v[52:55], v[16:19], a[176:183], v75\n"
        "ds_write_b32 v78, v65 offset:256\n"
        "v_smfmac_f32_16x16x64_bf16 v[52:55], v[20:23], a[184:191], v75\n"
        "v_smfmac_f32_16x16x64_bf16 v[52:55], v[24:27], a[192:199], v75\n"
        "v_smfmac_f32_16x16x64_bf16 v[56:59], v[0:3], a[200:207], v75\n"
        "v_smfmac_f32_16x16x64_bf16 v[56:59], v[4:7], a[208:215], v75\n"
        "v_smfmac_f32_16x16x64_bf16 v[56:59], v[8:11], a[216:223], v75\n"
        "v_pk_add_f32 v[52:53], v[52:53], v[54:55]\n"
        "v_smfmac_f32_16x16x64_bf16 v[56:59], v[12:15], a[224:231], v75\n"
        "v_cvt_pk_bf16_f32 v66, v52, v53\n"
        "v_smfmac_f32_16x16x64_bf16 v[56:59], v[16:19], a[232:239], v75\n"
        "ds_write_b32 v78, v66 offset:512\n"
        "buffer_load_dwordx2 v[40:41], v77, s[40:43], s46 offen sc1\n"
        "buffer_load_dwordx4 v[28:31], v76, s[40:43], s46 offen sc1\n"
        "buffer_load_dwordx4 v[32:35], v76, s[40:43], s46 offen offset:256 sc1\n"
        "v_smfmac_f32_16x16x64_bf16 v[56:59], v[20:23], a[240:247], v75\n"
        "v_smfmac_f32_16x16x64_bf16 v[56:59], v[24:27], a[248:255], v75\n"
        "s_nop 7\n"
        "v_pk_add_f32 v[56:57], v[56:57], v[58:59]\n"
        "v_cvt_pk_bf16_f32 v67, v56, v57\n"
        "ds_write_b32 v78, v67 offset:768\n"
        "s_waitcnt lgkmcnt(0)\n"
        "ds_read_b128 v[8:11], v72 offset:11264\n"
        "s_cmp_eq_u32 s44, 63\n"
        "s_cbranch_scc1 .Lr3_exit_%=\n"
        "s_add_u32 s44, s44, 1\n"
        "s_mov_b32 s57, 0\n"
        "s_mov_b32 s58, 0\n"
        "buffer_load_dwordx4 v[36:39], v72, s[40:43], s51 offen sc1\n"
        "v_mov_b32 v85, v85\n"
        "v_mov_b32 v85, v85\n"
        "v_mov_b32 v85, v85\n"
        "v_mov_b32 v85, v85\n"
        "v_mov_b32 v85, v85\n"
        "v_mov_b32 v85, v85\n"
        "v_mov_b32 v85, v85\n"
        "v_mov_b32 v85, v85\n"
        "v_mov_b32 v85, v85\n"
        "v_mov_b32 v85, v85\n"
        "v_mov_b32 v85, v85\n"
        "v_mov_b32 v85, v85\n"
        "buffer_store_dword v75, v81, s[40:43], s45 offen sc1\n"
        ".Lr3_swait_%=:\n"
        "s_waitcnt vmcnt(2) lgkmcnt(0)\n"
        ".Lr3_schain_%=:\n"
        "v_pk_add_f32 v[0:1], v[28:29], v[32:33]\n"
        "v_pk_add_f32 v[2:3], v[30:31], v[34:35]\n"
        "v_mov_b32 v13, v40\n"
        "v_max3_f32 v4, v0, v1, v13\n"
        "v_cmp_u_f32 vcc, v0, v13\n"
        "v_max3_f32 v4, v2, v3, v4\n"
        "s_cbranch_vccnz .Lr3_sbad_%=\n"
        ".Lr3_sgo_%=:\n"
        "s_nop 0\n"
        "v_max_f32_dpp v4, v4, v4 quad_perm:[2,3,0,1] row_mask:0xf bank_mask:0xf\n"
        "v_sub_f32 v12, v13, v4\n"
        "v_pk_add_f32 v[0:1], v[0:1], v[4:5] op_sel_hi:[1,0] neg_lo:[0,1] neg_hi:[0,1]\n"
        "v_exp_f32 v12, v12\n"
        "v_pk_add_f32 v[2:3], v[2:3], v[4:5] op_sel_hi:[1,0] neg_lo:[0,1] neg_hi:[0,1]\n"
        "v_exp_f32 v0, v0\n"
        "v_exp_f32 v1, v1\n"
        "v_exp_f32 v2, v2\n"
        "v_exp_f32 v3, v3\n"
        "v_fma_f32 v6, v12, v41, v0\n"
        "v_add_f32 v6, v6, v1\n"
        "v_add_f32 v7, v2, v3\n"
        "v_add_f32 v6, v6, v7\n"
        "s_nop 1\n"
        "v_add_f32_dpp v6, v6, v6 quad_perm:[2,3,0,1] row_mask:0xf bank_mask:0xf\n"
        "v_rcp_f32 v6, v6\n"
        "s_nop 0\n"
        "v_pk_mul_f32 v[0:1], v[0:1], v[6:7] op_sel_hi:[1,0]\n"
        "v_pk_mul_f32 v[2:3], v[2:3], v[6:7] op_sel_hi:[1,0]\n"
        "v_cvt_pk_bf16_f32 v0, v0, v1\n"
        "v_cvt_pk_bf16_f32 v1, v2, v3\n"
        "v_mul_f32 v12, v12, v6\n"
        "v_cvt_pk_bf16_f32 v12, v12, v12\n"
        ".Lr3_redo_%=:\n"
        "v_smfmac_f32_16x16x32_bf16 v[60:63], v[0:1], v[8:11], v75\n"
        "v_and_b32 v2, v12, v79\n"
        "v_and_b32 v3, v12, v80\n"
        "s_waitcnt vmcnt(1)\n"
        "s_nop 0\n"
        "v_smfmac_f32_16x16x32_bf16 v[60:63], v[2:3], v[36:39], v75\n"
        "s_nop 7\n"
        "v_pk_add_f32 v[4:5], v[60:61], v[62:63]\n"
        "ds_write2_b32 v82, v4, v5 offset0:6 offset1:38\n"
        "v_cmp_u_f32 vcc, v4, v5\n"
        "s_cbranch_vccnz .Lr3_obad_%=\n"
        ".Lr3_sok_%=:\n"
        "s_waitcnt lgkmcnt(0)\n"
        "s_barrier\n"
        "s_branch .Lr3_loop_%=\n"
        ".Lr3_rm_%=:\n"
        "s_add_u32 s59, s59, 1\n"
        "s_add_u32 s57, s57, 1\n"
        "s_cmp_eq_u32 s55, 0\n"
        "s_cbranch_scc1 .Lr3_mok_%=\n"
        "s_sub_u32 s55, s55, 1\n"
        "s_waitcnt lgkmcnt(0)\n"
        "buffer_load_dwordx4 v[12:15], v72, s[40:43], s47 offen sc1\n"
        "buffer_load_dwordx4 v[0:3], v72, s[40:43], s49 offen sc1\n"
        "s_nop 7\n"
        "v_mov_b32 v28, 0\n"
        "v_mov_b32 v29, 0\n"
        "v_mov_b32 v30, 0\n"
        "v_mov_b32 v31, 0\n"
        "v_mov_b32 v32, 0\n"
        "v_mov_b32 v33, 0\n"
        "v_mov_b32 v34, 0\n"
        "v_mov_b32 v35, 0\n"
        "s_waitcnt vmcnt(0)\n"
        "s_branch .Lr3_mwait_%=\n"
        ".Lr3_sbad_%=:\n"
        "s_add_u32 s57, s57, 1\n"
        "s_cmp_eq_u32 s55, 0\n"
        "s_cbranch_scc1 .Lr3_sgo_%=\n"
        ".Lr3_sslow_%=:\n"
        "s_sub_u32 s55, s55, 1\n"
        "buffer_load_dwordx4 v[28:31], v76, s[40:43], s46 offen sc1\n"
        "buffer_load_dwordx4 v[32:35], v76, s[40:43], s46 offen offset:256 sc1\n"
        "buffer_load_dwordx2 v[40:41], v77, s[40:43], s46 offen sc1\n"
        "s_waitcnt vmcnt(0)\n"
        "s_branch .Lr3_schain_%=\n"
        ".Lr3_obad_%=:\n"
        "s_cmp_eq_u32 s55, 0\n"
        "s_cbranch_scc1 .Lr3_sok_%=\n"
        "v_cmp_u_f32 vcc, v6, v6\n"
        "s_add_u32 s58, s58, 1\n"
        "v_mov_b32 v60, 0\n"
        "v_mov_b32 v61, 0\n"
        "v_mov_b32 v62, 0\n"
        "v_mov_b32 v63, 0\n"
        "s_cbranch_vccnz .Lr3_sslow_%=\n"
        "s_sub_u32 s55, s55, 1\n"
        "buffer_load_dwordx4 v[36:39], v72, s[40:43], s51 offen sc1\n"
        "s_waitcnt vmcnt(0)\n"
        "s_nop 0\n"
        "s_branch .Lr3_redo_%=\n"
        ".Lr3_exit_%=:\n"
        "s_nop 7\n"
        "buffer_store_dwordx4 v[64:67], v72, s[40:43], s53 offen sc1\n"
        "buffer_store_dwordx4 v[68:71], v72, s[40:43], s53 offen offset:1024 sc1\n"
        "s_waitcnt vmcnt(0) lgkmcnt(0)\n"
        "s_barrier\n"
        "s_mov_b32 s58, 266240\n"
        "buffer_store_dword v75, v83, s[40:43], s58 offen sc1\n"
        "s_mov_b32 s58, 262144\n"
        "buffer_store_dword v75, v83, s[40:43], s58 offen sc1\n"
        "s_waitcnt vmcnt(0)\n"
        "s_branch .Lrend_%=\n"
        ".Lr0b_entry_%=:\n"
        "v_mbcnt_lo_u32_b32 v0, -1, 0\n"
        "v_mbcnt_hi_u32_b32 v0, -1, v0\n"
        "v_lshlrev_b32 v0, 2, v0\n"
        "ds_read_b32 v72, v0 offset:17664\n"
        "ds_read_b32 v73, v0 offset:18688\n"
        "ds_read_b32 v74, v0 offset:19712\n"
        "ds_read_b32 v75, v0 offset:20736\n"
        "ds_read_b32 v76, v0 offset:21760\n"
        "ds_read_b32 v77, v0 offset:22784\n"
        "ds_read_b32 v78, v0 offset:23808\n"
        "ds_read_b32 v79, v0 offset:24832\n"
        "ds_read_b32 v80, v0 offset:25856\n"
        "ds_read_b32 v81, v0 offset:26880\n"
        "ds_read_b32 v82, v0 offset:27904\n"
        "ds_read_b32 v83, v0 offset:28928\n"
        "ds_read_b32 v84, v0 offset:29952\n"
        "ds_read_b32 v85, v0 offset:30976\n"
        "s_waitcnt lgkmcnt(0)\n"
        "ds_read_b32 v1, v0 offset:32000\n"
        "ds_read_b32 v2, v0 offset:33024\n"
        "ds_read_b32 v3, v0 offset:34048\n"
        "ds_read_b32 v4, v0 offset:35072\n"
        "ds_read_b32 v5, v0 offset:36096\n"
        "ds_read_b32 v6, v0 offset:37120\n"
        "ds_read_b32 v7, v0 offset:38144\n"
        "ds_read_b32 v8, v0 offset:39168\n"
        "ds_read_b32 v9, v0 offset:40192\n"
        "s_waitcnt lgkmcnt(0)\n"
        "buffer_load_dword v10, v9, s[40:43], 0 offen\n"
        "v_readlane_b32 s45, v85, 0\n"
        "s_xor_b32 s47, s45, 0x84000\n"
        "s_andn2_b32 s45, s45, 0x1000\n"
        "s_add_u32 s49, s47, 32768\n"
        "s_getpc_b64 s[58:59]\n"
        "v_lshlrev_b32 v12, 5, v0\n"
        "v_min_u32 v12, .Lr0b_exit_%=-.Lr0b_loop_%=-64, v12\n"
        "v_add_u32 v12, .Lr0b_loop_%=-.Lr0b_entry_%=, v12\n"
        "global_load_dword v12, v12, s[58:59]\n"
        "s_mov_b32 s59, 0\n"
        "s_mov_b32 s60, 0\n"
        "s_mov_b32 s61, 4096\n"
        "s_mov_b32 s62, 8192\n"
        "s_mov_b32 s63, 12288\n"
        "s_mov_b32 s64, 16384\n"
        "s_mov_b32 s65, 20480\n"
        "s_mov_b32 s66, 24576\n"
        "buffer_load_dwordx4 v[88:91], v1, s[40:43], s60 offen\n"
        "buffer_load_dwordx4 v[92:95], v1, s[40:43], s60 offen offset:2048\n"
        "buffer_load_dwordx4 v[96:99], v1, s[40:43], s61 offen\n"
        "buffer_load_dwordx4 v[100:103], v1, s[40:43], s61 offen offset:2048\n"
        "buffer_load_dwordx4 v[104:107], v1, s[40:43], s62 offen\n"
        "buffer_load_dwordx4 v[108:111], v1, s[40:43], s62 offen offset:2048\n"
        "buffer_load_dwordx4 v[112:115], v1, s[40:43], s63 offen\n"
        "buffer_load_dwordx4 v[116:119], v1, s[40:43], s63 offen offset:2048\n"
        "buffer_load_dwordx4 v[120:123], v1, s[40:43], s64 offen\n"
        "buffer_load_dwordx4 v[124:127], v1, s[40:43], s64 offen offset:2048\n"
        "buffer_load_dwordx4 v[128:131], v1, s[40:43], s65 offen\n"
        "buffer_load_dwordx4 v[132:135], v1, s[40:43], s65 offen offset:2048\n"
        "buffer_load_dwordx4 v[136:139], v1, s[40:43], s66 offen\n"
        "buffer_load_dwordx4 v[140:143], v1, s[40:43], s66 offen offset:2048\n"
        "buffer_load_dwordx4 v[144:147], v2, s[40:43], s60 offen\n"
        "buffer_load_dwordx4 v[148:151], v2, s[40:43], s60 offen offset:2048\n"
        "buffer_load_dwordx4 v[152:155], v2, s[40:43], s61 offen\n"
        "buffer_load_dwordx4 v[156:159], v2, s[40:43], s61 offen offset:2048\n"
        "buffer_load_dwordx4 v[160:163], v2, s[40:43], s62 offen\n"
        "buffer_load_dwordx4 v[164:167], v2, s[40:43], s62 offen offset:2048\n"
        "buffer_load_dwordx4 v[168:171], v2, s[40:43], s63 offen\n"
        "buffer_load_dwordx4 v[172:175], v2, s[40:43], s63 offen offset:2048\n"
        "buffer_load_dwordx4 v[176:179], v2, s[40:43], s64 offen\n"
        "buffer_load_dwordx4 v[180:183], v2, s[40:43], s64 offen offset:2048\n"
        "buffer_load_dwordx4 v[184:187], v2, s[40:43], s65 offen\n"
        "buffer_load_dwordx4 v[188:191], v2, s[40:43], s65 offen offset:2048\n"
        "buffer_load_dwordx4 v[192:195], v2, s[40:43], s66 offen\n"
        "buffer_load_dwordx4 v[196:199], v2, s[40:43], s66 offen offset:2048\n"
        "buffer_load_dwordx4 v[200:203], v3, s[40:43], s60 offen\n"
        "buffer_load_dwordx4 v[204:207], v3, s[40:43], s60 offen offset:2048\n"
        "buffer_load_dwordx4 v[208:211], v3, s[40:43], s61 offen\n"
        "buffer_load_dwordx4 v[212:215], v3, s[40:43], s61 offen offset:2048\n"
        "buffer_load_dwordx4 v[216:219], v3, s[40:43], s62 offen\n"
        "buffer_load_dwordx4 v[220:223], v3, s[40:43], s62 offen offset:2048\n"
        "buffer_load_dwordx4 v[224:227], v3, s[40:43], s63 offen\n"
        "buffer_load_dwordx4 v[228:231], v3, s[40:43], s63 offen offset:2048\n"
        "buffer_load_dwordx4 v[232:235], v3, s[40:43], s64 offen\n"
        "buffer_load_dwordx4 v[236:239], v3, s[40:43], s64 offen offset:2048\n"
        "buffer_load_dwordx4 v[240:243], v3, s[40:43], s65 offen\n"
        "buffer_load_dwordx4 v[244:247], v3, s[40:43], s65 offen offset:2048\n"
        "buffer_load_dwordx4 v[248:251], v3, s[40:43], s66 offen\n"
        "buffer_load_dwordx4 v[252:255], v3, s[40:43], s66 offen offset:2048\n"
        "buffer_load_dwordx4 a[0:3], v4, s[40:43], s60 offen\n"
        "buffer_load_dwordx4 a[4:7], v4, s[40:43], s60 offen offset:2048\n"
        "buffer_load_dwordx4 a[8:11], v4, s[40:43], s64 offen\n"
        "buffer_load_dwordx4 a[12:15], v4, s[40:43], s64 offen offset:2048\n"
        "buffer_load_dwordx4 a[16:19], v4, s[40:43], s62 offen\n"
        "buffer_load_dwordx4 a[20:23], v4, s[40:43], s62 offen offset:2048\n"
        "buffer_load_dwordx4 a[32:35], v5, s[40:43], s60 offen\n"
        "buffer_load_dwordx4 a[36:39], v5, s[40:43], s60 offen offset:2048\n"
        "buffer_load_dwordx4 a[40:43], v5, s[40:43], s61 offen\n"
        "buffer_load_dwordx4 a[44:47], v5, s[40:43], s61 offen offset:2048\n"
        "buffer_load_dwordx4 a[48:51], v5, s[40:43], s62 offen\n"
        "buffer_load_dwordx4 a[52:55], v5, s[40:43], s62 offen offset:2048\n"
        "buffer_load_dwordx4 a[56:59], v5, s[40:43], s63 offen\n"
        "buffer_load_dwordx4 a[60:63], v5, s[40:43], s63 offen offset:2048\n"
        "buffer_load_dwordx4 a[64:67], v5, s[40:43], s64 offen\n"
        "buffer_load_dwordx4 a[68:71], v5, s[40:43], s64 offen offset:2048\n"
        "buffer_load_dwordx4 a[72:75], v5, s[40:43], s65 offen\n"
        "buffer_load_dwordx4 a[76:79], v5, s[40:43], s65 offen offset:2048\n"
        "buffer_load_dwordx4 a[80:83], v5, s[40:43], s66 offen\n"
        "buffer_load_dwordx4 a[84:87], v5, s[40:43], s66 offen offset:2048\n"
        "buffer_load_dwordx4 a[88:91], v6, s[40:43], s60 offen\n"
        "buffer_load_dwordx4 a[92:95], v6, s[40:43], s60 offen offset:2048\n"
        "buffer_load_dwordx4 a[96:99], v6, s[40:43], s61 offen\n"
        "buffer_load_dwordx4 a[100:103], v6, s[40:43], s61 offen offset:2048\n"
        "buffer_load_dwordx4 a[104:107], v6, s[40:43], s62 offen\n"
        "buffer_load_dwordx4 a[108:111], v6, s[40:43], s62 offen offset:2048\n"
        "buffer_load_dwordx4 a[112:115], v6, s[40:43], s63 offen\n"
        "buffer_load_dwordx4 a[116:119], v6, s[40:43], s63 offen offset:2048\n"
        "buffer_load_dwordx4 a[120:123], v6, s[40:43], s64 offen\n"
        "buffer_load_dwordx4 a[124:127], v6, s[40:43], s64 offen offset:2048\n"
        "buffer_load_dwordx4 a[128:131], v6, s[40:43], s65 offen\n"
        "buffer_load_dwordx4 a[132:135], v6, s[40:43], s65 offen offset:2048\n"
        "buffer_load_dwordx4 a[136:139], v6, s[40:43], s66 offen\n"
        "buffer_load_dwordx4 a[140:143], v6, s[40:43], s66 offen offset:2048\n"
        "buffer_load_dwordx4 a[144:147], v7, s[40:43], s60 offen\n"
        "buffer_load_dwordx4 a[148:151], v7, s[40:43], s60 offen offset:2048\n"
        "buffer_load_dwordx4 a[152:155], v7, s[40:43], s61 offen\n"
        "buffer_load_dwordx4 a[156:159], v7, s[40:43], s61 offen offset:2048\n"
        "buffer_load_dwordx4 a[160:163], v7, s[40:43], s62 offen\n"
        "buffer_load_dwordx4 a[164:167], v7, s[40:43], s62 offen offset:2048\n"
        "buffer_load_dwordx4 a[168:171], v7, s[40:43], s63 offen\n"
        "buffer_load_dwordx4 a[172:175], v7, s[40:43], s63 offen offset:2048\n"
        "buffer_load_dwordx4 a[176:179], v7, s[40:43], s64 offen\n"
        "buffer_load_dwordx4 a[180:183], v7, s[40:43], s64 offen offset:2048\n"
        "buffer_load_dwordx4 a[184:187], v7, s[40:43], s65 offen\n"
        "buffer_load_dwordx4 a[188:191], v7, s[40:43], s65 offen offset:2048\n"
        "buffer_load_dwordx4 a[192:195], v7, s[40:43], s66 offen\n"
        "buffer_load_dwordx4 a[196:199], v7, s[40:43], s66 offen offset:2048\n"
        "buffer_load_dwordx4 a[200:203], v8, s[40:43], s60 offen\n"
        "buffer_load_dwordx4 a[204:207], v8, s[40:43], s60 offen offset:2048\n"
        "buffer_load_dwordx4 a[208:211], v8, s[40:43], s61 offen\n"
        "buffer_load_dwordx4 a[212:215], v8, s[40:43], s61 offen offset:2048\n"
        "buffer_load_dwordx4 a[216:219], v8, s[40:43], s62 offen\n"
        "buffer_load_dwordx4 a[220:223], v8, s[40:43], s62 offen offset:2048\n"
        "buffer_load_dwordx4 a[224:227], v8, s[40:43], s63 offen\n"
        "buffer_load_dwordx4 a[228:231], v8, s[40:43], s63 offen offset:2048\n"
        "buffer_load_dwordx4 a[232:235], v8, s[40:43], s64 offen\n"
        "buffer_load_dwordx4 a[236:239], v8, s[40:43], s64 offen offset:2048\n"
        "buffer_load_dwordx4 a[240:243], v8, s[40:43], s65 offen\n"
        "buffer_load_dwordx4 a[244:247], v8, s[40:43], s65 offen offset:2048\n"
        "buffer_load_dwordx4 a[248:251], v8, s[40:43], s66 offen\n"
        "buffer_load_dwordx4 a[252:255], v8, s[40:43], s66 offen offset:2048\n"
        "s_add_u32 s53, s53, 0\n"
        "s_add_u32 s54, s54, 0\n"
        "v_mov_b32 v28, 0\n"
        "v_mov_b32 v29, 0\n"
        "v_mov_b32 v30, 0\n"
        "v_mov_b32 v31, 0\n"
        "v_mov_b32 v32, 0\n"
        "v_mov_b32 v33, 0\n"
        "v_mov_b32 v34, 0\n"
        "v_mov_b32 v35, 0\n"
        "v_mov_b32 v36, 0\n"
        "v_mov_b32 v37, 0\n"
        "v_mov_b32 v38, 0\n"
        "v_mov_b32 v39, 0\n"
        "v_mov_b32 v40, 0\n"
        "v_mov_b32 v41, 0\n"
        "v_mov_b32 v42, 0\n"
        "v_mov_b32 v43, 0\n"
        "v_mov_b32 v44, 0\n"
        "v_mov_b32 v45, 0\n"
        "v_mov_b32 v46, 0\n"
        "v_mov_b32 v47, 0\n"
        "v_mov_b32 v48, 0\n"
        "v_mov_b32 v49, 0\n"
        "v_mov_b32 v50, 0\n"
        "v_mov_b32 v51, 0\n"
        "v_mov_b32 v52, 0\n"
        "v_mov_b32 v53, 0\n"
        "v_mov_b32 v54, 0\n"
        "v_mov_b32 v55, 0\n"
        "v_mov_b32 v56, 0\n"
        "v_mov_b32 v57, 0\n"
        "v_mov_b32 v58, 0\n"
        "v_mov_b32 v59, 0\n"
        "v_mov_b32 v60, 0\n"
        "v_mov_b32 v61, 0\n"
        "v_mov_b32 v62, 0\n"
        "v_mov_b32 v63, 0\n"
        "v_mov_b32 v64, 0\n"
        "v_mov_b32 v65, 0\n"
        "v_mov_b32 v66, 0\n"
        "v_mov_b32 v67, 0\n"
        "v_mov_b32 v68, 0\n"
        "v_mov_b32 v69, 0\n"
        "v_mov_b32 v70, 0\n"
        "v_mov_b32 v71, 0\n"
        "buffer_load_dword v10, v9, s[40:43], 0 offen\n"
        "s_waitcnt vmcnt(0)\n"
        "s_mov_b32 s50, 0\n"
        "s_mov_b32 s66, 0x11111111\n"
        "s_mov_b32 s67, 0x11111111\n"
        "s_branch .Lr0b_poll_%=\n"
        ".Lr0b_loop_%=:\n"
        "s_waitcnt vmcnt(1)\n"
        "v_mov_b32 v28, 0\n"
        "v_mov_b32 v29, 0\n"
        "v_mov_b32 v30, 0\n"
        "v_mov_b32 v31, 0\n"
        "v_mov_b32 v32, 0\n"
        "v_mov_b32 v33, 0\n"
        "v_mov_b32 v34, 0\n"
        "v_mov_b32 v35, 0\n"
        "v_mov_b32 v36, 0\n"
        "v_mov_b32 v37, 0\n"
        "v_mov_b32 v38, 0\n"
        "v_mov_b32 v39, 0\n"
        "v_mov_b32 v40, 0\n"
        "v_mov_b32 v41, 0\n"
        "v_mov_b32 v42, 0\n"
        "v_mov_b32 v43, 0\n"
        "v_mov_b32 v44, 0\n"
        "v_mov_b32 v45, 0\n"
        "v_mov_b32 v46, 0\n"
        "v_mov_b32 v47, 0\n"
        "v_mov_b32 v48, 0\n"
        "v_mov_b32 v49, 0\n"
        "v_mov_b32 v50, 0\n"
        "v_mov_b32 v51, 0\n"
        "v_mov_b32 v52, 0\n"
        "v_mov_b32 v53, 0\n"
        "v_mov_b32 v54, 0\n"
        "v_mov_b32 v55, 0\n"
        "v_mov_b32 v56, 0\n"
        "v_mov_b32 v57, 0\n"
        "v_mov_b32 v58, 0\n"
        "v_mov_b32 v59, 0\n"
        "v_mov_b32 v85, v85\n"
        "v_mov_b32 v85, v85\n"
        "v_mov_b32 v85, v85\n"
        "v_mov_b32 v85, v85\n"
        "v_mov_b32 v85, v85\n"
        "v_mov_b32 v85, v85\n"
        "v_mov_b32 v85, v85\n"
        "v_mov_b32 v85, v85\n"
        "v_mov_b32 v85, v85\n"
        "v_mov_b32 v85, v85\n"
        "v_mov_b32 v85, v85\n"
        "v_mov_b32 v85, v85\n"
        "v_mov_b32 v85, v85\n"
        "v_mov_b32 v85, v85\n"
        "v_mov_b32 v85, v85\n"
        "v_mov_b32 v85, v85\n"
        "v_mov_b32 v85, v85\n"
        "v_mov_b32 v85, v85\n"
        "v_mov_b32 v85, v85\n"
        "v_mov_b32 v85, v85\n"
        "v_mov_b32 v85, v85\n"
        "v_mov_b32 v85, v85\n"
        "v_mov_b32 v85, v85\n"
        "v_mov_b32 v85, v85\n"
        "v_mov_b32 v85, v85\n"
        "v_mov_b32 v85, v85\n"
        "v_mov_b32 v85, v85\n"
        "v_mov_b32 v85, v85\n"
        "v_mov_b32 v85, v85\n"
        "v_mov_b32 v85, v85\n"
        "v_mov_b32 v85, v85\n"
        "v_mov_b32 v85, v85\n"
        "v_mov_b32 v85, v85\n"
        "v_mov_b32 v85, v85\n"
        "v_mov_b32 v85, v85\n"
        "v_mov_b32 v85, v85\n"
        "v_mov_b32 v85, v85\n"
        "v_mov_b32 v85, v85\n"
        "v_mov_b32 v85, v85\n"
        "v_mov_b32 v85, v85\n"
        "v_mov_b32 v85, v85\n"
        "v_mov_b32 v85, v85\n"
        "v_mov_b32 v85, v85\n"
        ".Lr0b_poll_%=:\n"
        "buffer_load_dwordx4 v[0:3], v72, s[40:43], s47 offen sc1\n"
        "buffer_load_dwordx4 v[16:19], v72, s[40:43], s49 offen sc1\n"
        "s_waitcnt vmcnt(0)\n"
        ".Lr0b_mwait_%=:\n"
        "v_max3_u32 v60, v0, v1, v2\n"
        "v_max3_u32 v61, v3, v16, v17\n"
        "v_max3_u32 v60, v60, v18, v19\n"
        "ds_write_b128 v72, v[0:3] offset:4096\n"
        "ds_write_b128 v72, v[16:19] offset:8192\n"
        "v_smfmac_f32_16x16x64_bf16 v[28:31], v[0:3], v[88:95], v75\n"
        "v_max_u32 v60, v60, v61\n"
        "v_cmp_eq_u32 vcc, -1, v60\n"
        "v_smfmac_f32_16x16x64_bf16 v[32:35], v[0:3], v[144:151], v75\n"
        "s_cbranch_vccnz .Lr0b_rm_%=\n"
        ".Lr0b_mok_%=:\n"
        "v_smfmac_f32_16x16x64_bf16 v[36:39], v[0:3], v[200:207], v75\n"
        "v_smfmac_f32_16x16x64_bf16 v[40:43], v[0:3], a[0:7], v75\n"
        "s_waitcnt lgkmcnt(0)\n"
        "s_barrier\n"
        "ds_read_b128 v[8:11], v72 offset:6144\n"
        "ds_read_b128 v[4:7], v72 offset:5120\n"
        "ds_read_b128 v[12:15], v72 offset:7168\n"
        "ds_read_b128 v[20:23], v72 offset:9216\n"
        "ds_read_b128 v[24:27], v72 offset:10240\n"
        "v_smfmac_f32_16x16x64_bf16 v[28:31], v[16:19], v[120:127], v75\n"
        "buffer_store_dwordx4 v[64:67], v72, s[40:43], s53 offen sc1\n"
        "v_smfmac_f32_16x16x64_bf16 v[32:35], v[16:19], v[176:183], v75\n"
        "buffer_store_dwordx4 v[68:71], v72, s[40:43], s53 offen offset:1024 sc1\n"
        "v_smfmac_f32_16x16x64_bf16 v[36:39], v[16:19], v[232:239], v75\n"
        "v_smfmac_f32_16x16x64_bf16 v[40:43], v[16:19], a[8:15], v75\n"
        "ds_read_b128 v[64:67], v84\n"
        "s_waitcnt lgkmcnt(5)\n"
        "v_smfmac_f32_16x16x64_bf16 v[40:43], v[8:11], a[16:23], v75\n"
        "s_mov_b32 s46, s47\n"
        "s_add_u32 s51, s45, s56\n"
        "s_add_u32 s57, s44, 1\n"
        "v_smfmac_f32_16x16x64_bf16 v[28:31], v[8:11], v[104:111], v75\n"
        "v_readlane_b32 s45, v85, s57\n"
        "s_lshl_b32 s58, s44, 18\n"
        "s_add_u32 s53, s54, s58\n"
        "v_smfmac_f32_16x16x64_bf16 v[32:35], v[8:11], v[160:167], v75\n"
        "s_xor_b32 s47, s45, 0x84000\n"
        "s_andn2_b32 s45, s45, 0x1000\n"
        "s_add_u32 s49, s47, 32768\n"
        "v_smfmac_f32_16x16x64_bf16 v[36:39], v[8:11], v[216:223], v75\n"
        "s_add_u32 s52, s46, 65536\n"
        "s_waitcnt lgkmcnt(4)\n"
        "v_smfmac_f32_16x16x64_bf16 v[28:31], v[4:7], v[96:103], v75\n"
        "s_waitcnt lgkmcnt(3)\n"
        "v_smfmac_f32_16x16x64_bf16 v[28:31], v[12:15], v[112:119], v75\n"
        "v_pk_add_f32 v[40:41], v[40:41], v[42:43]\n"
        "s_waitcnt lgkmcnt(2)\n"
        "v_smfmac_f32_16x16x64_bf16 v[28:31], v[20:23], v[128:135], v75\n"
        "v_cvt_pk_bf16_f32 v40, v40, v41\n"
        "s_waitcnt lgkmcnt(1)\n"
        "v_smfmac_f32_16x16x64_bf16 v[28:31], v[24:27], v[136:143], v75\n"
        "ds_write_b32 v74, v40 offset:3072\n"
        "v_smfmac_f32_16x16x64_bf16 v[32:35], v[4:7], v[152:159], v75\n"
        "v_smfmac_f32_16x16x64_bf16 v[32:35], v[12:15], v[168:175], v75\n"
        "v_pk_add_f32 v[28:29], v[28:29], v[30:31]\n"
        "v_smfmac_f32_16x16x64_bf16 v[32:35], v[20:23], v[184:191], v75\n"
        "v_cvt_pk_bf16_f32 v28, v28, v29\n"
        "v_smfmac_f32_16x16x64_bf16 v[32:35], v[24:27], v[192:199], v75\n"
        "ds_write_b32 v74, v28 offset:0\n"
        "v_smfmac_f32_16x16x64_bf16 v[36:39], v[4:7], v[208:215], v75\n"
        "v_smfmac_f32_16x16x64_bf16 v[36:39], v[12:15], v[224:231], v75\n"
        "v_pk_add_f32 v[32:33], v[32:33], v[34:35]\n"
        "v_smfmac_f32_16x16x64_bf16 v[36:39], v[20:23], v[240:247], v75\n"
        "v_cvt_pk_bf16_f32 v32, v32, v33\n"
        "v_smfmac_f32_16x16x64_bf16 v[36:39], v[24:27], v[248:255], v75\n"
        "ds_write_b32 v74, v32 offset:1024\n"
        "v_smfmac_f32_16x16x64_bf16 v[44:47], v[0:3], a[32:39], v75\n"
        "s_nop 3\n"
        "v_pk_add_f32 v[36:37], v[36:37], v[38:39]\n"
        "v_cvt_pk_bf16_f32 v36, v36, v37\n"
        "ds_write_b32 v74, v36 offset:2048\n"
        "v_smfmac_f32_16x16x64_bf16 v[44:47], v[4:7], a[40:47], v75\n"
        "v_smfmac_f32_16x16x64_bf16 v[44:47], v[8:11], a[48:55], v75\n"
        "s_waitcnt vmcnt(2) lgkmcnt(0)\n"
        "s_barrier\n"
        "ds_read_b64_tr_b16 v[68:69], v73 offset:0\n"
        "ds_read_b64_tr_b16 v[70:71], v73 offset:512\n"
        "ds_read_b64_tr_b16 v[28:29], v73 offset:1024\n"
        "ds_read_b64_tr_b16 v[30:31], v73 offset:1536\n"
        "ds_read_b64_tr_b16 v[32:33], v73 offset:2048\n"
        "ds_read_b64_tr_b16 v[34:35], v73 offset:2560\n"
        "ds_read_b64_tr_b16 v[36:37], v73 offset:3072\n"
        "ds_read_b64_tr_b16 v[38:39], v73 offset:3584\n"
        "v_smfmac_f32_16x16x64_bf16 v[44:47], v[12:15], a[56:63], v75\n"
        "s_waitcnt lgkmcnt(6)\n"
        "v_mfma_f32_16x16x32_bf16 v[60:63], v[16:19], v[68:71], 0\n"
        "s_waitcnt lgkmcnt(4)\n"
        "v_mfma_f32_16x16x32_bf16 v[60:63], v[20:23], v[28:31], v[60:63]\n"
        "s_waitcnt lgkmcnt(2)\n"
        "v_mfma_f32_16x16x32_bf16 v[60:63], v[24:27], v[32:35], v[60:63]\n"
        "s_waitcnt lgkmcnt(0)\n"
        "v_mfma_f32_16x16x32_bf16 v[60:63], v[64:67], v[36:39], v[60:63]\n"
        "v_smfmac_f32_16x16x64_bf16 v[44:47], v[16:19], a[64:71], v75\n"
        "s_nop 3\n"
        "v_add_f32_dpp v40, v62, v60 row_ror:8 row_mask:0xf bank_mask:0x3\n"
        "s_nop 1\n"
        "v_add_f32_dpp v40, v61, v63 row_ror:8 row_mask:0xf bank_mask:0xc\n"
        "buffer_store_dword v40, v81, s[40:43], s52 offen sc1\n"
        "v_smfmac_f32_16x16x64_bf16 v[44:47], v[20:23], a[72:79], v75\n"
        "v_mov_b32 v60, 0\n"
        "v_smfmac_f32_16x16x64_bf16 v[44:47], v[24:27], a[80:87], v75\n"
        "v_mov_b32 v61, 0\n"
        "v_smfmac_f32_16x16x64_bf16 v[48:51], v[0:3], a[88:95], v75\n"
        "v_mov_b32 v62, 0\n"
        "v_smfmac_f32_16x16x64_bf16 v[48:51], v[4:7], a[96:103], v75\n"
        "v_mov_b32 v63, 0\n"
        "v_smfmac_f32_16x16x64_bf16 v[48:51], v[8:11], a[104:111], v75\n"
        "v_pk_add_f32 v[44:45], v[44:45], v[46:47]\n"
        "v_smfmac_f32_16x16x64_bf16 v[48:51], v[12:15], a[112:119], v75\n"
        "v_cvt_pk_bf16_f32 v64, v44, v45\n"
        "v_smfmac_f32_16x16x64_bf16 v[48:51], v[16:19], a[120:127], v75\n"
        "ds_write_b32 v78, v64 offset:0\n"
        "v_smfmac_f32_16x16x64_bf16 v[48:51], v[20:23], a[128:135], v75\n"
        "v_smfmac_f32_16x16x64_bf16 v[48:51], v[24:27], a[136:143], v75\n"
        "v_smfmac_f32_16x16x64_bf16 v[52:55], v[0:3], a[144:151], v75\n"
        "v_smfmac_f32_16x16x64_bf16 v[52:55], v[4:7], a[152:159], v75\n"
        "v_smfmac_f32_16x16x64_bf16 v[52:55], v[8:11], a[160:167], v75\n"
        "v_pk_add_f32 v[48:49], v[48:49], v[50:51]\n"
        "v_smfmac_f32_16x16x64_bf16 v[52:55], v[12:15], a[168:175], v75\n"
        "v_cvt_pk_bf16_f32 v65, v48, v49\n"
        "v_smfmac_f32_16x16x64_bf16 v[52:55], v[16:19], a[176:183], v75\n"
        "ds_write_b32 v78, v65 offset:256\n"
        "v_smfmac_f32_16x16x64_bf16 v[52:55], v[20:23], a[184:191], v75\n"
        "v_smfmac_f32_16x16x64_bf16 v[52:55], v[24:27], a[192:199], v75\n"
        "buffer_load_dwordx2 v[40:41], v77, s[40:43], s46 offen sc1\n"
        "buffer_load_dwordx4 v[28:31], v76, s[40:43], s46 offen sc1\n"
        "buffer_load_dwordx4 v[32:35], v76, s[40:43], s46 offen offset:256 sc1\n"
        "v_smfmac_f32_16x16x64_bf16 v[56:59], v[0:3], a[200:207], v75\n"
        "v_smfmac_f32_16x16x64_bf16 v[56:59], v[4:7], a[208:215], v75\n"
        "v_smfmac_f32_16x16x64_bf16 v[56:59], v[8:11], a[216:223], v75\n"
        "v_pk_add_f32 v[52:53], v[52:53], v[54:55]\n"
        "v_smfmac_f32_16x16x64_bf16 v[56:59], v[12:15], a[224:231], v75\n"
        "v_cvt_pk_bf16_f32 v66, v52, v53\n"
        "v_smfmac_f32_16x16x64_bf16 v[56:59], v[16:19], a[232:239], v75\n"
        "ds_write_b32 v78, v66 offset:512\n"
        "v_smfmac_f32_16x16x64_bf16 v[56:59], v[20:23], a[240:247], v75\n"
        "v_smfmac_f32_16x16x64_bf16 v[56:59], v[24:27], a[248:255], v75\n"
        "s_nop 7\n"
        "v_pk_add_f32 v[56:57], v[56:57], v[58:59]\n"
        "v_cvt_pk_bf16_f32 v67, v56, v57\n"
        "ds_write_b32 v78, v67 offset:768\n"
        "s_waitcnt lgkmcnt(0)\n"
        "ds_read_b128 v[8:11], v72 offset:11264\n"
        "s_cmp_eq_u32 s44, 63\n"
        "s_cbranch_scc1 .Lr0b_exit_%=\n"
        "s_add_u32 s44, s44, 1\n"
        "s_mov_b32 s57, 0\n"
        "s_mov_b32 s58, 0\n"
        "buffer_load_dwordx4 v[36:39], v72, s[40:43], s51 offen sc1\n"
        ".Lr0b_swait_%=:\n"
        "s_waitcnt vmcnt(1) lgkmcnt(0)\n"
        ".Lr0b_schain_%=:\n"
        "v_pk_add_f32 v[0:1], v[28:29], v[32:33]\n"
        "v_pk_add_f32 v[2:3], v[30:31], v[34:35]\n"
        "v_mov_b32 v13, v40\n"
        "v_max3_f32 v4, v0, v1, v13\n"
        "v_cmp_u_f32 vcc, v0, v13\n"
        "v_max3_f32 v4, v2, v3, v4\n"
        "s_cbranch_vccnz .Lr0b_sbad_%=\n"
        ".Lr0b_sgo_%=:\n"
        "s_nop 0\n"
        "v_max_f32_dpp v4, v4, v4 quad_perm:[2,3,0,1] row_mask:0xf bank_mask:0xf\n"
        "v_sub_f32 v12, v13, v4\n"
        "v_pk_add_f32 v[0:1], v[0:1], v[4:5] op_sel_hi:[1,0] neg_lo:[0,1] neg_hi:[0,1]\n"
        "v_exp_f32 v12, v12\n"
        "v_pk_add_f32 v[2:3], v[2:3], v[4:5] op_sel_hi:[1,0] neg_lo:[0,1] neg_hi:[0,1]\n"
        "v_exp_f32 v0, v0\n"
        "v_exp_f32 v1, v1\n"
        "v_exp_f32 v2, v2\n"
        "v_exp_f32 v3, v3\n"
        "v_fma_f32 v6, v12, v41, v0\n"
        "v_add_f32 v6, v6, v1\n"
        "v_add_f32 v7, v2, v3\n"
        "v_add_f32 v6, v6, v7\n"
        "s_nop 1\n"
        "v_add_f32_dpp v6, v6, v6 quad_perm:[2,3,0,1] row_mask:0xf bank_mask:0xf\n"
        "v_rcp_f32 v6, v6\n"
        "s_nop 0\n"
        "v_pk_mul_f32 v[0:1], v[0:1], v[6:7] op_sel_hi:[1,0]\n"
        "v_pk_mul_f32 v[2:3], v[2:3], v[6:7] op_sel_hi:[1,0]\n"
        "v_cvt_pk_bf16_f32 v0, v0, v1\n"
        "v_cvt_pk_bf16_f32 v1, v2, v3\n"
        "v_mul_f32 v12, v12, v6\n"
        "v_cvt_pk_bf16_f32 v12, v12, v12\n"
        ".Lr0b_redo_%=:\n"
        "v_smfmac_f32_16x16x32_bf16 v[60:63], v[0:1], v[8:11], v75\n"
        "v_and_b32 v2, v12, v79\n"
        "v_and_b32 v3, v12, v80\n"
        "s_waitcnt vmcnt(0)\n"
        "s_nop 0\n"
        "v_smfmac_f32_16x16x32_bf16 v[60:63], v[2:3], v[36:39], v75\n"
        "s_nop 7\n"
        "v_pk_add_f32 v[4:5], v[60:61], v[62:63]\n"
        "ds_write2_b32 v82, v4, v5 offset0:0 offset1:32\n"
        "v_cmp_u_f32 vcc, v4, v5\n"
        "s_cbranch_vccnz .Lr0b_obad_%=\n"
        ".Lr0b_sok_%=:\n"
        "s_waitcnt lgkmcnt(0)\n"
        "s_barrier\n"
        "ds_read_b128 v[0:3], v83\n"
        "ds_read_b128 v[4:7], v83 offset:16\n"
        "s_waitcnt lgkmcnt(1)\n"
        "v_pk_add_f32 v[0:1], v[0:1], v[2:3]\n"
        "s_waitcnt lgkmcnt(0)\n"
        "v_pk_add_f32 v[4:5], v[4:5], v[6:7]\n"
        "v_pk_add_f32 v[0:1], v[0:1], v[4:5]\n"
        "v_cvt_pk_bf16_f32 v0, v0, v1\n"
        "buffer_store_dword v0, v81, s[40:43], s47 offen sc1\n"
        "s_branch .Lr0b_loop_%=\n"
        ".Lr0b_rm_%=:\n"
        "s_add_u32 s59, s59, 1\n"
        "s_add_u32 s57, s57, 1\n"
        "s_cmp_eq_u32 s55, 0\n"
        "s_cbranch_scc1 .Lr0b_mok_%=\n"
        "s_sub_u32 s55, s55, 1\n"
        "s_waitcnt lgkmcnt(0)\n"
        "buffer_load_dwordx4 v[0:3], v72, s[40:43], s47 offen sc1\n"
        "buffer_load_dwordx4 v[16:19], v72, s[40:43], s49 offen sc1\n"
        "s_nop 7\n"
        "v_mov_b32 v28, 0\n"
        "v_mov_b32 v29, 0\n"
        "v_mov_b32 v30, 0\n"
        "v_mov_b32 v31, 0\n"
        "v_mov_b32 v32, 0\n"
        "v_mov_b32 v33, 0\n"
        "v_mov_b32 v34, 0\n"
        "v_mov_b32 v35, 0\n"
        "s_waitcnt vmcnt(0)\n"
        "s_branch .Lr0b_mwait_%=\n"
        ".Lr0b_sbad_%=:\n"
        "s_add_u32 s57, s57, 1\n"
        "s_cmp_eq_u32 s55, 0\n"
        "s_cbranch_scc1 .Lr0b_sgo_%=\n"
        ".Lr0b_sslow_%=:\n"
        "s_sub_u32 s55, s55, 1\n"
        "buffer_load_dwordx4 v[28:31], v76, s[40:43], s46 offen sc1\n"
        "buffer_load_dwordx4 v[32:35], v76, s[40:43], s46 offen offset:256 sc1\n"
        "buffer_load_dwordx2 v[40:41], v77, s[40:43], s46 offen sc1\n"
        "s_waitcnt vmcnt(0)\n"
        "s_branch .Lr0b_schain_%=\n"
        ".Lr0b_obad_%=:\n"
        "s_cmp_eq_u32 s55, 0\n"
        "s_cbranch_scc1 .Lr0b_sok_%=\n"
        "v_cmp_u_f32 vcc, v6, v6\n"
        "s_add_u32 s58, s58, 1\n"
        "v_mov_b32 v60, 0\n"
        "v_mov_b32 v61, 0\n"
        "v_mov_b32 v62, 0\n"
        "v_mov_b32 v63, 0\n"
        "s_cbranch_vccnz .Lr0b_sslow_%=\n"
        "s_sub_u32 s55, s55, 1\n"
        "buffer_load_dwordx4 v[36:39], v72, s[40:43], s51 offen sc1\n"
        "s_waitcnt vmcnt(0)\n"
        "s_nop 0\n"
        "s_branch .Lr0b_redo_%=\n"
        ".Lr0b_exit_%=:\n"
        "s_nop 7\n"
        "buffer_store_dwordx4 v[64:67], v72, s[40:43], s53 offen sc1\n"
        "buffer_store_dwordx4 v[68:71], v72, s[40:43], s53 offen offset:1024 sc1\n"
        "s_waitcnt vmcnt(0) lgkmcnt(0)\n"
        "s_barrier\n"
        "s_branch .Lrend_%=\n"
        ".Lrend_%=:\n"
        "s_endpgm\n"
        :
        : "s"(lo), "s"(hi), "s"(WS_BYTES), "s"(0), "s"(ob), "s"(sb), "s"(OFF_MISC + 8192u), "s"(var)
        : "memory", "vcc", "scc", "s40", "s41", "s42", "s43", "s44", "s45", "s46", "s47", "s48", "s49", "s50", "s51", "s52", "s53", "s54", "s55", "s56", "s57", "s58", "s59", "s60", "s61", "s62", "s63", "s64", "s65", "s66", "s67", "s68", "s69", "s70", "s71", "s72", "s73", "s74", "s75", "s76", "s77", "s78", "s79", "v255", "a255");
}

DI void helper_main(const int hid, unsigned char* ws, const unsigned tab) {
    const int tid = threadIdx.x, lane = tid & 63, w = __builtin_amdgcn_readfirstlane(tid >> 6);
    const int G = lane >> 4, n = lane & 15, sel = (n >> 1) & 1, row = 2 * (n >> 2) + (n & 1);
    const int h = hid & 15, par = hid >> 4;
    unsigned t[15];
    t[0] = B_MEMH + w * 2048 + lane * 16;
    t[1] = w < 3 ? t[0] + 1024 : t[0];
    t[2] = OFF_KQF + h * 14336 + w * 4096 + lane * 16;
    t[3] = w < 3 ? t[2] + 2048 : OOR;
    t[4] = OFF_VOT + (unsigned)(h * 28 + 7 * w) * 1024u + lane * 16;
    t[5] = OFF_VOT + (unsigned)(h * 28 + 7 * w + 1) * 1024u + (lane ^ 16) * 16;
    t[6] = w * 1280 + 2 * G * 160 + n * 4;
    t[7] = row * 160 + G * 32 + sel * 16;
    t[8] = oblk((7 * w + G) >> 1) + ((7 * w + G) & 1) * 4096 + h * 256 + n * 16;
    t[9] = G < 3 ? oblk((7 * w + 4 + G) >> 1) + ((7 * w + G) & 1) * 4096 + h * 256 + n * 16 : OOR;
    t[10] = (G == (n >> 2) && sel == 0) ? B_ML + h * 128 + row * 8 : OOR;
    t[11] = B_TRG + h * 128;
    t[12] = sel ? 0xEEEEu : 0x4444u;
    t[13] = OFF_FLAG + h * 4 + (lane == 0 ? 0 : (lane == 1 ? 64 : 1024));
    t[14] = tab;
#pragma unroll
    for (int i = 0; i < 15; ++i) ldsw32(L_TBL + i * 1024 + tid * 4, t[i]);
    const unsigned lo = (unsigned)(uintptr_t)ws, hi = (unsigned)((uintptr_t)ws >> 32);
    asm volatile(
        "s_mov_b32 s40, %0\n"
        "s_and_b32 s41, %1, 0xffff\n"
        "s_mov_b32 s42, %2\n"
        "s_mov_b32 s43, 0x00020000\n"
        "s_mov_b32 s44, %4\n"
        "s_mov_b32 s55, 0x40000\n"
        "s_mov_b32 s59, 0\n"
        "s_mov_b64 exec, -1\n"
        "s_mov_b32 s52, 0xaaaaaaaa\n"
        "s_mov_b32 s53, 0xaaaaaaaa\n"
        "v_mbcnt_lo_u32_b32 v0, -1, 0\n"
        "v_mbcnt_hi_u32_b32 v0, -1, v0\n"
        "s_lshl_b32 s58, %5, 8\n"
        "v_lshl_add_u32 v0, v0, 2, s58\n"
        "ds_read_b32 v140, v0 offset:17664\n"
        "ds_read_b32 v141, v0 offset:18688\n"
        "ds_read_b32 v142, v0 offset:19712\n"
        "ds_read_b32 v143, v0 offset:20736\n"
        "ds_read_b32 v144, v0 offset:21760\n"
        "ds_read_b32 v145, v0 offset:22784\n"
        "ds_read_b32 v146, v0 offset:23808\n"
        "ds_read_b32 v147, v0 offset:24832\n"
        "ds_read_b32 v148, v0 offset:25856\n"
        "ds_read_b32 v149, v0 offset:26880\n"
        "ds_read_b32 v150, v0 offset:27904\n"
        "ds_read_b32 v151, v0 offset:28928\n"
        "ds_read_b32 v152, v0 offset:29952\n"
        "ds_read_b32 v153, v0 offset:30976\n"
        "ds_read_b32 v154, v0 offset:32000\n"
        "v_mov_b32 v89, 0x3f003f00\n"
        "v_mov_b32 v91, 0x3f003f00\n"
        "v_mov_b32 v93, 0x3f003f00\n"
        "v_mov_b32 v95, 0x3f003f00\n"
        "s_waitcnt lgkmcnt(0)\n"
        "s_cmp_eq_u32 %5, 3\n"
        "s_cbranch_scc1 .Lh1_loop_%=\n"
        ".Lh0_loop_%=:\n"
        "s_lshl_b32 s58, s44, 9\n"
        ".Lh0_fl_%=:\n"
        "buffer_load_dword v28, v153, s[40:43], s58 offen sc1\n"
        "s_cmp_eq_u32 s55, 0\n"
        "s_cbranch_scc1 .Lh0_flok_%=\n"
        "s_sub_u32 s55, s55, 1\n"
        "s_waitcnt vmcnt(0)\n"
        "v_cmp_eq_u32 vcc, 0, v28\n"
        "s_cbranch_vccz .Lh0_flok_%=\n"
        "s_sleep 2\n"
        "s_branch .Lh0_fl_%=\n"
        ".Lh0_flok_%=:\n"
        "s_mul_i32 s46, s44, 458752\n"
        "s_add_u32 s47, s46, 229376\n"
        "s_add_u32 s48, s46, 4096\n"
        "buffer_load_dwordx4 v[32:35], v142, s[40:43], s46 offen offset:0 sc1\n"
        "buffer_load_dwordx4 v[36:39], v142, s[40:43], s46 offen offset:1024 sc1\n"
        "buffer_load_dwordx4 v[40:43], v143, s[40:43], s46 offen offset:0 sc1\n"
        "buffer_load_dwordx4 v[44:47], v143, s[40:43], s46 offen offset:1024 sc1\n"
        "buffer_load_dwordx4 v[48:51], v142, s[40:43], s47 offen offset:0 sc1\n"
        "buffer_load_dwordx4 v[52:55], v142, s[40:43], s47 offen offset:1024 sc1\n"
        "buffer_load_dwordx4 v[56:59], v143, s[40:43], s47 offen offset:0 sc1\n"
        "buffer_load_dwordx4 v[60:63], v143, s[40:43], s47 offen offset:1024 sc1\n"
        "buffer_load_dwordx4 v[160:163], v144, s[40:43], s46 offen offset:0 sc1\n"
        "buffer_load_dwordx4 v[164:167], v145, s[40:43], s46 offen offset:0 sc1\n"
        "buffer_load_dwordx4 v[168:171], v144, s[40:43], s46 offen offset:2048 sc1\n"
        "buffer_load_dwordx4 v[172:175], v145, s[40:43], s46 offen offset:2048 sc1\n"
        "buffer_load_dwordx4 v[176:179], v144, s[40:43], s48 offen offset:0 sc1\n"
        "buffer_load_dwordx4 v[180:183], v145, s[40:43], s48 offen offset:0 sc1\n"
        "buffer_load_dwordx4 v[184:187], v144, s[40:43], s48 offen offset:2048 sc1\n"
        "v_readlane_b32 s45, v154, s44\n"
        "s_xor_b32 s49, s45, 0x84000\n"
        "s_andn2_b32 s45, s45, 0x1000\n"
        "v_mov_b32 v8, 0\n"
        "v_mov_b32 v9, 0\n"
        "v_mov_b32 v10, 0\n"
        "v_mov_b32 v11, 0\n"
        "v_mov_b32 v12, 0\n"
        "v_mov_b32 v13, 0\n"
        "v_mov_b32 v14, 0\n"
        "v_mov_b32 v15, 0\n"
        "v_mov_b32 v16, 0\n"
        "v_mov_b32 v17, 0\n"
        "v_mov_b32 v18, 0\n"
        "v_mov_b32 v19, 0\n"
        "v_mov_b32 v20, 0\n"
        "v_mov_b32 v21, 0\n"
        "v_mov_b32 v22, 0\n"
        "v_mov_b32 v23, 0\n"
        "v_mov_b32 v192, 0\n"
        "v_mov_b32 v193, 0\n"
        "v_mov_b32 v194, 0\n"
        "v_mov_b32 v195, 0\n"
        "v_mov_b32 v196, 0\n"
        "v_mov_b32 v197, 0\n"
        "v_mov_b32 v198, 0\n"
        "v_mov_b32 v199, 0\n"
        "v_mov_b32 v200, 0\n"
        "v_mov_b32 v201, 0\n"
        "v_mov_b32 v202, 0\n"
        "v_mov_b32 v203, 0\n"
        "v_mov_b32 v204, 0\n"
        "v_mov_b32 v205, 0\n"
        "v_mov_b32 v206, 0\n"
        "v_mov_b32 v207, 0\n"
        "v_mov_b32 v208, 0\n"
        "v_mov_b32 v209, 0\n"
        "v_mov_b32 v210, 0\n"
        "v_mov_b32 v211, 0\n"
        "v_mov_b32 v212, 0\n"
        "v_mov_b32 v213, 0\n"
        "v_mov_b32 v214, 0\n"
        "v_mov_b32 v215, 0\n"
        "s_waitcnt vmcnt(0)\n"
        "v_mov_b32 v64, v160\n"
        "v_mov_b32 v65, v164\n"
        "v_mov_b32 v66, v161\n"
        "v_mov_b32 v67, v165\n"
        "v_mov_b32 v68, v162\n"
        "v_mov_b32 v69, v166\n"
        "v_mov_b32 v70, v163\n"
        "v_mov_b32 v71, v167\n"
        "v_mov_b32 v72, v168\n"
        "v_mov_b32 v73, v172\n"
        "v_mov_b32 v74, v169\n"
        "v_mov_b32 v75, v173\n"
        "v_mov_b32 v76, v170\n"
        "v_mov_b32 v77, v174\n"
        "v_mov_b32 v78, v171\n"
        "v_mov_b32 v79, v175\n"
        "v_mov_b32 v80, v176\n"
        "v_mov_b32 v81, v180\n"
        "v_mov_b32 v82, v177\n"
        "v_mov_b32 v83, v181\n"
        "v_mov_b32 v84, v178\n"
        "v_mov_b32 v85, v182\n"
        "v_mov_b32 v86, v179\n"
        "v_mov_b32 v87, v183\n"
        "v_mov_b32 v88, v184\n"
        "v_mov_b32 v90, v185\n"
        "v_mov_b32 v92, v186\n"
        "v_mov_b32 v94, v187\n"
        "s_cmp_lg_u32 %5, 0\n"
        "s_cbranch_scc1 .Lh0_x0_%=\n"
        "v_swap_b32 v46, v60\n"
        "v_swap_b32 v47, v61\n"
        ".Lh0_x0_%=:\n"
        "s_cmp_lg_u32 %5, 2\n"
        "s_cbranch_scc1 .Lh0_x2_%=\n"
        "v_swap_b32 v42, v56\n"
        "v_swap_b32 v43, v57\n"
        ".Lh0_x2_%=:\n"
        "s_barrier\n"
        "v_mov_b32 v28, -1\n"
        "s_nop 1\n"
        "buffer_load_dword v28, v151, s[40:43], s45 offen sc1\n"
        "s_nop 15\n"
        "buffer_load_dword v28, v151, s[40:43], s45 offen sc1\n"
        "s_nop 15\n"
        ".Lh0_tr_%=:\n"
        "buffer_load_dword v28, v151, s[40:43], s45 offen sc1\n"
        "s_cmp_eq_u32 s55, 0\n"
        "s_cbranch_scc1 .Lh0_trok_%=\n"
        "s_sub_u32 s55, s55, 1\n"
        "s_waitcnt vmcnt(2)\n"
        "v_cmp_eq_u32 vcc, -1, v28\n"
        "s_cbranch_vccnz .Lh0_tr_%=\n"
        ".Lh0_trok_%=:\n"
        "s_nop 15\n"
        "s_nop 15\n"
        "s_nop 15\n"
        "s_nop 15\n"
        "s_nop 15\n"
        "s_nop 15\n"
        "s_nop 15\n"
        "s_nop 15\n"
        "buffer_load_dwordx4 v[96:99], v140, s[40:43], s45 offen sc1\n"
        "buffer_load_dwordx4 v[100:103], v141, s[40:43], s45 offen sc1\n"
        "s_nop 15\n"
        "s_nop 15\n"
        "s_nop 15\n"
        "buffer_load_dwordx4 v[104:107], v140, s[40:43], s45 offen sc1\n"
        "buffer_load_dwordx4 v[108:111], v141, s[40:43], s45 offen sc1\n"
        "s_nop 15\n"
        "s_nop 15\n"
        "s_nop 15\n"
        "buffer_load_dwordx4 v[112:115], v140, s[40:43], s45 offen sc1\n"
        "buffer_load_dwordx4 v[116:119], v141, s[40:43], s45 offen sc1\n"
        ".Lh0_p0_%=:\n"
        "s_waitcnt vmcnt(4)\n"
        "v_smfmac_f32_16x16x64_bf16 v[192:195], v[96:99], v[32:39], v152\n"
        "v_smfmac_f32_16x16x64_bf16 v[196:199], v[96:99], v[48:55], v152\n"
        "v_smfmac_f32_16x16x64_bf16 v[192:195], v[100:103], v[40:47], v152\n"
        "v_smfmac_f32_16x16x64_bf16 v[196:199], v[100:103], v[56:63], v152\n"
        "s_nop 3\n"
        "v_pk_add_f32 v[0:1], v[192:193], v[194:195]\n"
        "ds_write2_b32 v146, v0, v1 offset0:0 offset1:40\n"
        "s_nop 1\n"
        "v_pk_add_f32 v[2:3], v[196:197], v[198:199]\n"
        "ds_write2_b32 v146, v2, v3 offset0:16 offset1:56\n"
        "v_cmp_u_f32 vcc, v2, v3\n"
        "s_cbranch_vccz .Lh0_go_%=\n"
        "buffer_load_dwordx4 v[96:99], v140, s[40:43], s45 offen sc1\n"
        "buffer_load_dwordx4 v[100:103], v141, s[40:43], s45 offen sc1\n"
        "s_cmp_eq_u32 s55, 0\n"
        "s_cbranch_scc1 .Lh0_go_%=\n"
        "s_sub_u32 s55, s55, 1\n"
        "s_add_u32 s59, s59, 1\n"
        "v_mov_b32 v192, 0\n"
        "v_mov_b32 v193, 0\n"
        "v_mov_b32 v194, 0\n"
        "v_mov_b32 v195, 0\n"
        "v_mov_b32 v196, 0\n"
        "v_mov_b32 v197, 0\n"
        "v_mov_b32 v198, 0\n"
        "v_mov_b32 v199, 0\n"
        ".Lh0_p1_%=:\n"
        "s_waitcnt vmcnt(4)\n"
        "v_smfmac_f32_16x16x64_bf16 v[200:203], v[104:107], v[32:39], v152\n"
        "v_smfmac_f32_16x16x64_bf16 v[204:207], v[104:107], v[48:55], v152\n"
        "v_smfmac_f32_16x16x64_bf16 v[200:203], v[108:111], v[40:47], v152\n"
        "v_smfmac_f32_16x16x64_bf16 v[204:207], v[108:111], v[56:63], v152\n"
        "s_nop 3\n"
        "v_pk_add_f32 v[0:1], v[200:201], v[202:203]\n"
        "ds_write2_b32 v146, v0, v1 offset0:0 offset1:40\n"
        "s_nop 1\n"
        "v_pk_add_f32 v[2:3], v[204:205], v[206:207]\n"
        "ds_write2_b32 v146, v2, v3 offset0:16 offset1:56\n"
        "v_cmp_u_f32 vcc, v2, v3\n"
        "s_cbranch_vccz .Lh0_go_%=\n"
        "buffer_load_dwordx4 v[104:107], v140, s[40:43], s45 offen sc1\n"
        "buffer_load_dwordx4 v[108:111], v141, s[40:43], s45 offen sc1\n"
        "s_cmp_eq_u32 s55, 0\n"
        "s_cbranch_scc1 .Lh0_go_%=\n"
        "s_sub_u32 s55, s55, 1\n"
        "s_add_u32 s59, s59, 1\n"
        "v_mov_b32 v200, 0\n"
        "v_mov_b32 v201, 0\n"
        "v_mov_b32 v202, 0\n"
        "v_mov_b32 v203, 0\n"
        "v_mov_b32 v204, 0\n"
        "v_mov_b32 v205, 0\n"
        "v_mov_b32 v206, 0\n"
        "v_mov_b32 v207, 0\n"
        ".Lh0_p2_%=:\n"
        "s_waitcnt vmcnt(4)\n"
        "v_smfmac_f32_16x16x64_bf16 v[208:211], v[112:115], v[32:39], v152\n"
        "v_smfmac_f32_16x16x64_bf16 v[212:215], v[112:115], v[48:55], v152\n"
        "v_smfmac_f32_16x16x64_bf16 v[208:211], v[116:119], v[40:47], v152\n"
        "v_smfmac_f32_16x16x64_bf16 v[212:215], v[116:119], v[56:63], v152\n"
        "s_nop 3\n"
        "v_pk_add_f32 v[0:1], v[208:209], v[210:211]\n"
        "ds_write2_b32 v146, v0, v1 offset0:0 offset1:40\n"
        "s_nop 1\n"
        "v_pk_add_f32 v[2:3], v[212:213], v[214:215]\n"
        "ds_write2_b32 v146, v2, v3 offset0:16 offset1:56\n"
        "v_cmp_u_f32 vcc, v2, v3\n"
        "s_cbranch_vccz .Lh0_go_%=\n"
        "buffer_load_dwordx4 v[112:115], v140, s[40:43], s45 offen sc1\n"
        "buffer_load_dwordx4 v[116:119], v141, s[40:43], s45 offen sc1\n"
        "s_cmp_eq_u32 s55, 0\n"
        "s_cbranch_scc1 .Lh0_go_%=\n"
        "s_sub_u32 s55, s55, 1\n"
        "s_add_u32 s59, s59, 1\n"
        "v_mov_b32 v208, 0\n"
        "v_mov_b32 v209, 0\n"
        "v_mov_b32 v210, 0\n"
        "v_mov_b32 v211, 0\n"
        "v_mov_b32 v212, 0\n"
        "v_mov_b32 v213, 0\n"
        "v_mov_b32 v214, 0\n"
        "v_mov_b32 v215, 0\n"
        "s_nop 1\n"
        "s_branch .Lh0_p0_%=\n"
        ".Lh0_go_%=:\n"
        "s_waitcnt lgkmcnt(0)\n"
        "s_barrier\n"
        "ds_read_b128 v[120:123], v147 offset:0\n"
        "ds_read_b128 v[124:127], v147 offset:1280\n"
        "ds_read_b128 v[128:131], v147 offset:2560\n"
        "ds_read_b128 v[132:135], v147 offset:3840\n"
        "s_waitcnt lgkmcnt(2)\n"
        "v_pk_add_f32 v[0:1], v[120:121], v[124:125]\n"
        "v_pk_add_f32 v[2:3], v[122:123], v[126:127]\n"
        "s_waitcnt lgkmcnt(0)\n"
        "v_pk_add_f32 v[4:5], v[128:129], v[132:133]\n"
        "v_pk_add_f32 v[6:7], v[130:131], v[134:135]\n"
        "v_pk_add_f32 v[0:1], v[0:1], v[4:5]\n"
        "v_pk_add_f32 v[2:3], v[2:3], v[6:7]\n"
        "v_max_f32 v4, v0, v1\n"
        "v_max3_f32 v4, v2, v3, v4\n"
        "s_nop 1\n"
        "v_max_f32_dpp v5, v4, v4 quad_perm:[2,3,0,1] row_mask:0xf bank_mask:0xf\n"
        "v_max_f32_dpp v4, v4, v4 quad_perm:[2,3,0,1] row_mask:0xf bank_mask:0xf\n"
        "s_nop 1\n"
        "v_permlane16_swap_b32 v4, v5\n"
        "v_max_f32 v6, v4, v5\n"
        "v_max_f32 v4, v4, v5\n"
        "s_nop 1\n"
        "v_permlane32_swap_b32 v4, v6\n"
        "v_max_f32 v4, v4, v6\n"
        "v_pk_add_f32 v[0:1], v[0:1], v[4:5] op_sel_hi:[1,0] neg_lo:[0,1] neg_hi:[0,1]\n"
        "v_pk_add_f32 v[2:3], v[2:3], v[4:5] op_sel_hi:[1,0] neg_lo:[0,1] neg_hi:[0,1]\n"
        "v_exp_f32 v0, v0\n"
        "v_exp_f32 v1, v1\n"
        "v_exp_f32 v2, v2\n"
        "v_exp_f32 v3, v3\n"
        "v_cvt_pk_bf16_f32 v24, v0, v1\n"
        "v_cvt_pk_bf16_f32 v25, v2, v3\n"
        "s_nop 0\n"
        "v_mov_b32_dpp v26, v24 quad_perm:[2,3,0,1] row_mask:0xf bank_mask:0xf\n"
        "v_mov_b32_dpp v27, v25 quad_perm:[2,3,0,1] row_mask:0xf bank_mask:0xf\n"
        "s_nop 1\n"
        "v_smfmac_f32_16x16x64_bf16 v[8:11], v[24:27], v[64:71], v152\n"
        "v_smfmac_f32_16x16x64_bf16 v[12:15], v[24:27], v[72:79], v152\n"
        "v_smfmac_f32_16x16x64_bf16 v[16:19], v[24:27], v[80:87], v152\n"
        "v_smfmac_f32_16x16x64_bf16 v[20:23], v[24:27], v[88:95], v152\n"
        "v_cvt_pk_bf16_f32 v120, v8, v9\n"
        "v_cvt_pk_bf16_f32 v121, v10, v11\n"
        "v_cvt_pk_bf16_f32 v122, v12, v13\n"
        "v_cvt_pk_bf16_f32 v123, v14, v15\n"
        "v_permlane16_swap_b32 v120, v121\n"
        "v_cvt_pk_bf16_f32 v124, v16, v17\n"
        "v_cvt_pk_bf16_f32 v125, v18, v19\n"
        "v_permlane16_swap_b32 v122, v123\n"
        "v_cvt_pk_bf16_f32 v126, v20, v21\n"
        "v_cvt_pk_bf16_f32 v127, v22, v23\n"
        "v_permlane32_swap_b32 v120, v122\n"
        "v_permlane32_swap_b32 v121, v123\n"
        "v_permlane16_swap_b32 v124, v125\n"
        "v_permlane16_swap_b32 v126, v127\n"
        "buffer_store_dwordx4 v[120:123], v148, s[40:43], s45 offen sc1\n"
        "s_nop 0\n"
        "v_permlane32_swap_b32 v124, v126\n"
        "v_permlane32_swap_b32 v125, v127\n"
        "buffer_store_dwordx4 v[124:127], v149, s[40:43], s45 offen sc1\n"
        "s_add_u32 s44, s44, 2\n"
        "s_cmp_lt_u32 s44, 63\n"
        "s_nop 7\n"
        "s_cbranch_scc1 .Lh0_loop_%=\n"
        "s_waitcnt vmcnt(0) lgkmcnt(0)\n"
        "s_branch .Lhend_%=\n"
        ".Lh1_loop_%=:\n"
        "s_lshl_b32 s58, s44, 9\n"
        ".Lh1_fl_%=:\n"
        "buffer_load_dword v28, v153, s[40:43], s58 offen sc1\n"
        "s_cmp_eq_u32 s55, 0\n"
        "s_cbranch_scc1 .Lh1_flok_%=\n"
        "s_sub_u32 s55, s55, 1\n"
        "s_waitcnt vmcnt(0)\n"
        "v_cmp_eq_u32 vcc, 0, v28\n"
        "s_cbranch_vccz .Lh1_flok_%=\n"
        "s_sleep 2\n"
        "s_branch .Lh1_fl_%=\n"
        ".Lh1_flok_%=:\n"
        "s_mul_i32 s46, s44, 458752\n"
        "s_add_u32 s47, s46, 229376\n"
        "s_add_u32 s48, s46, 4096\n"
        "buffer_load_dwordx4 v[32:35], v142, s[40:43], s46 offen offset:0 sc1\n"
        "buffer_load_dwordx4 v[36:39], v142, s[40:43], s46 offen offset:1024 sc1\n"
        "buffer_load_dwordx4 v[40:43], v143, s[40:43], s46 offen offset:0 sc1\n"
        "buffer_load_dwordx4 v[44:47], v143, s[40:43], s46 offen offset:1024 sc1\n"
        "buffer_load_dwordx4 v[48:51], v142, s[40:43], s47 offen offset:0 sc1\n"
        "buffer_load_dwordx4 v[52:55], v142, s[40:43], s47 offen offset:1024 sc1\n"
        "buffer_load_dwordx4 v[56:59], v143, s[40:43], s47 offen offset:0 sc1\n"
        "buffer_load_dwordx4 v[60:63], v143, s[40:43], s47 offen offset:1024 sc1\n"
        "buffer_load_dwordx4 v[160:163], v144, s[40:43], s46 offen offset:0 sc1\n"
        "buffer_load_dwordx4 v[164:167], v145, s[40:43], s46 offen offset:0 sc1\n"
        "buffer_load_dwordx4 v[168:171], v144, s[40:43], s46 offen offset:2048 sc1\n"
        "buffer_load_dwordx4 v[172:175], v145, s[40:43], s46 offen offset:2048 sc1\n"
        "buffer_load_dwordx4 v[176:179], v144, s[40:43], s48 offen offset:0 sc1\n"
        "buffer_load_dwordx4 v[180:183], v145, s[40:43], s48 offen offset:0 sc1\n"
        "buffer_load_dwordx4 v[184:187], v144, s[40:43], s48 offen offset:2048 sc1\n"
        "v_readlane_b32 s45, v154, s44\n"
        "s_xor_b32 s49, s45, 0x84000\n"
        "s_andn2_b32 s45, s45, 0x1000\n"
        "v_mov_b32 v8, 0\n"
        "v_mov_b32 v9, 0\n"
        "v_mov_b32 v10, 0\n"
        "v_mov_b32 v11, 0\n"
        "v_mov_b32 v12, 0\n"
        "v_mov_b32 v13, 0\n"
        "v_mov_b32 v14, 0\n"
        "v_mov_b32 v15, 0\n"
        "v_mov_b32 v16, 0\n"
        "v_mov_b32 v17, 0\n"
        "v_mov_b32 v18, 0\n"
        "v_mov_b32 v19, 0\n"
        "v_mov_b32 v20, 0\n"
        "v_mov_b32 v21, 0\n"
        "v_mov_b32 v22, 0\n"
        "v_mov_b32 v23, 0\n"
        "v_mov_b32 v192, 0\n"
        "v_mov_b32 v193, 0\n"
        "v_mov_b32 v194, 0\n"
        "v_mov_b32 v195, 0\n"
        "v_mov_b32 v196, 0\n"
        "v_mov_b32 v197, 0\n"
        "v_mov_b32 v198, 0\n"
        "v_mov_b32 v199, 0\n"
        "v_mov_b32 v200, 0\n"
        "v_mov_b32 v201, 0\n"
        "v_mov_b32 v202, 0\n"
        "v_mov_b32 v203, 0\n"
        "v_mov_b32 v204, 0\n"
        "v_mov_b32 v205, 0\n"
        "v_mov_b32 v206, 0\n"
        "v_mov_b32 v207, 0\n"
        "v_mov_b32 v208, 0\n"
        "v_mov_b32 v209, 0\n"
        "v_mov_b32 v210, 0\n"
        "v_mov_b32 v211, 0\n"
        "v_mov_b32 v212, 0\n"
        "v_mov_b32 v213, 0\n"
        "v_mov_b32 v214, 0\n"
        "v_mov_b32 v215, 0\n"
        "s_waitcnt vmcnt(0)\n"
        "v_mov_b32 v64, v160\n"
        "v_mov_b32 v65, v164\n"
        "v_mov_b32 v66, v161\n"
        "v_mov_b32 v67, v165\n"
        "v_mov_b32 v68, v162\n"
        "v_mov_b32 v69, v166\n"
        "v_mov_b32 v70, v163\n"
        "v_mov_b32 v71, v167\n"
        "v_mov_b32 v72, v168\n"
        "v_mov_b32 v73, v172\n"
        "v_mov_b32 v74, v169\n"
        "v_mov_b32 v75, v173\n"
        "v_mov_b32 v76, v170\n"
        "v_mov_b32 v77, v174\n"
        "v_mov_b32 v78, v171\n"
        "v_mov_b32 v79, v175\n"
        "v_mov_b32 v80, v176\n"
        "v_mov_b32 v81, v180\n"
        "v_mov_b32 v82, v177\n"
        "v_mov_b32 v83, v181\n"
        "v_mov_b32 v84, v178\n"
        "v_mov_b32 v85, v182\n"
        "v_mov_b32 v86, v179\n"
        "v_mov_b32 v87, v183\n"
        "v_mov_b32 v88, v184\n"
        "v_mov_b32 v90, v185\n"
        "v_mov_b32 v92, v186\n"
        "v_mov_b32 v94, v187\n"
        "s_barrier\n"
        "v_mov_b32 v28, -1\n"
        "s_nop 1\n"
        "buffer_load_dword v28, v151, s[40:43], s45 offen sc1\n"
        "s_nop 15\n"
        "buffer_load_dword v28, v151, s[40:43], s45 offen sc1\n"
        "s_nop 15\n"
        ".Lh1_tr_%=:\n"
        "buffer_load_dword v28, v151, s[40:43], s45 offen sc1\n"
        "s_cmp_eq_u32 s55, 0\n"
        "s_cbranch_scc1 .Lh1_trok_%=\n"
        "s_sub_u32 s55, s55, 1\n"
        "s_waitcnt vmcnt(2)\n"
        "v_cmp_eq_u32 vcc, -1, v28\n"
        "s_cbranch_vccnz .Lh1_tr_%=\n"
        ".Lh1_trok_%=:\n"
        "s_nop 15\n"
        "s_nop 15\n"
        "s_nop 15\n"
        "s_nop 15\n"
        "s_nop 15\n"
        "s_nop 15\n"
        "s_nop 15\n"
        "s_nop 15\n"
        "buffer_load_dwordx4 v[96:99], v140, s[40:43], s45 offen sc1\n"
        "buffer_load_dwordx4 v[100:103], v141, s[40:43], s45 offen sc1\n"
        "s_nop 15\n"
        "s_nop 15\n"
        "s_nop 15\n"
        "buffer_load_dwordx4 v[104:107], v140, s[40:43], s45 offen sc1\n"
        "buffer_load_dwordx4 v[108:111], v141, s[40:43], s45 offen sc1\n"
        "s_nop 15\n"
        "s_nop 15\n"
        "s_nop 15\n"
        "buffer_load_dwordx4 v[112:115], v140, s[40:43], s45 offen sc1\n"
        "buffer_load_dwordx4 v[116:119], v141, s[40:43], s45 offen sc1\n"
        ".Lh1_p0_%=:\n"
        "s_waitcnt vmcnt(4)\n"
        "v_smfmac_f32_16x16x64_bf16 v[192:195], v[96:99], v[32:39], v152\n"
        "v_smfmac_f32_16x16x64_bf16 v[196:199], v[96:99], v[48:55], v152\n"
        "v_smfmac_f32_16x16x64_bf16 v[192:195], v[100:103], v[40:47], v152\n"
        "v_smfmac_f32_16x16x64_bf16 v[196:199], v[100:103], v[56:63], v152\n"
        "s_nop 3\n"
        "v_pk_add_f32 v[0:1], v[192:193], v[194:195]\n"
        "ds_write2_b32 v146, v0, v1 offset0:0 offset1:40\n"
        "s_nop 1\n"
        "v_pk_add_f32 v[2:3], v[196:197], v[198:199]\n"
        "ds_write2_b32 v146, v2, v3 offset0:16 offset1:56\n"
        "v_cmp_u_f32 vcc, v2, v3\n"
        "s_cbranch_vccz .Lh1_go_%=\n"
        "buffer_load_dwordx4 v[96:99], v140, s[40:43], s45 offen sc1\n"
        "buffer_load_dwordx4 v[100:103], v141, s[40:43], s45 offen sc1\n"
        "s_cmp_eq_u32 s55, 0\n"
        "s_cbranch_scc1 .Lh1_go_%=\n"
        "s_sub_u32 s55, s55, 1\n"
        "s_add_u32 s59, s59, 1\n"
        "v_mov_b32 v192, 0\n"
        "v_mov_b32 v193, 0\n"
        "v_mov_b32 v194, 0\n"
        "v_mov_b32 v195, 0\n"
        "v_mov_b32 v196, 0\n"
        "v_mov_b32 v197, 0\n"
        "v_mov_b32 v198, 0\n"
        "v_mov_b32 v199, 0\n"
        ".Lh1_p1_%=:\n"
        "s_waitcnt vmcnt(4)\n"
        "v_smfmac_f32_16x16x64_bf16 v[200:203], v[104:107], v[32:39], v152\n"
        "v_smfmac_f32_16x16x64_bf16 v[204:207], v[104:107], v[48:55], v152\n"
        "v_smfmac_f32_16x16x64_bf16 v[200:203], v[108:111], v[40:47], v152\n"
        "v_smfmac_f32_16x16x64_bf16 v[204:207], v[108:111], v[56:63], v152\n"
        "s_nop 3\n"
        "v_pk_add_f32 v[0:1], v[200:201], v[202:203]\n"
        "ds_write2_b32 v146, v0, v1 offset0:0 offset1:40\n"
        "s_nop 1\n"
        "v_pk_add_f32 v[2:3], v[204:205], v[206:207]\n"
        "ds_write2_b32 v146, v2, v3 offset0:16 offset1:56\n"
        "v_cmp_u_f32 vcc, v2, v3\n"
        "s_cbranch_vccz .Lh1_go_%=\n"
        "buffer_load_dwordx4 v[104:107], v140, s[40:43], s45 offen sc1\n"
        "buffer_load_dwordx4 v[108:111], v141, s[40:43], s45 offen sc1\n"
        "s_cmp_eq_u32 s55, 0\n"
        "s_cbranch_scc1 .Lh1_go_%=\n"
        "s_sub_u32 s55, s55, 1\n"
        "s_add_u32 s59, s59, 1\n"
        "v_mov_b32 v200, 0\n"
        "v_mov_b32 v201, 0\n"
        "v_mov_b32 v202, 0\n"
        "v_mov_b32 v203, 0\n"
        "v_mov_b32 v204, 0\n"
        "v_mov_b32 v205, 0\n"
        "v_mov_b32 v206, 0\n"
        "v_mov_b32 v207, 0\n"
        ".Lh1_p2_%=:\n"
        "s_waitcnt vmcnt(4)\n"
        "v_smfmac_f32_16x16x64_bf16 v[208:211], v[112:115], v[32:39], v152\n"
        "v_smfmac_f32_16x16x64_bf16 v[212:215], v[112:115], v[48:55], v152\n"
        "v_smfmac_f32_16x16x64_bf16 v[208:211], v[116:119], v[40:47], v152\n"
        "v_smfmac_f32_16x16x64_bf16 v[212:215], v[116:119], v[56:63], v152\n"
        "s_nop 3\n"
        "v_pk_add_f32 v[0:1], v[208:209], v[210:211]\n"
        "ds_write2_b32 v146, v0, v1 offset0:0 offset1:40\n"
        "s_nop 1\n"
        "v_pk_add_f32 v[2:3], v[212:213], v[214:215]\n"
        "ds_write2_b32 v146, v2, v3 offset0:16 offset1:56\n"
        "v_cmp_u_f32 vcc, v2, v3\n"
        "s_cbranch_vccz .Lh1_go_%=\n"
        "buffer_load_dwordx4 v[112:115], v140, s[40:43], s45 offen sc1\n"
        "buffer_load_dwordx4 v[116:119], v141, s[40:43], s45 offen sc1\n"
        "s_cmp_eq_u32 s55, 0\n"
        "s_cbranch_scc1 .Lh1_go_%=\n"
        "s_sub_u32 s55, s55, 1\n"
        "s_add_u32 s59, s59, 1\n"
        "v_mov_b32 v208, 0\n"
        "v_mov_b32 v209, 0\n"
        "v_mov_b32 v210, 0\n"
        "v_mov_b32 v211, 0\n"
        "v_mov_b32 v212, 0\n"
        "v_mov_b32 v213, 0\n"
        "v_mov_b32 v214, 0\n"
        "v_mov_b32 v215, 0\n"
        "s_nop 1\n"
        "s_branch .Lh1_p0_%=\n"
        ".Lh1_go_%=:\n"
        "s_waitcnt lgkmcnt(0)\n"
        "s_barrier\n"
        "ds_read_b128 v[120:123], v147 offset:0\n"
        "ds_read_b128 v[124:127], v147 offset:1280\n"
        "ds_read_b128 v[128:131], v147 offset:2560\n"
        "ds_read_b128 v[132:135], v147 offset:3840\n"
        "s_waitcnt lgkmcnt(2)\n"
        "v_pk_add_f32 v[0:1], v[120:121], v[124:125]\n"
        "v_pk_add_f32 v[2:3], v[122:123], v[126:127]\n"
        "s_waitcnt lgkmcnt(0)\n"
        "v_pk_add_f32 v[4:5], v[128:129], v[132:133]\n"
        "v_pk_add_f32 v[6:7], v[130:131], v[134:135]\n"
        "v_pk_add_f32 v[0:1], v[0:1], v[4:5]\n"
        "v_pk_add_f32 v[2:3], v[2:3], v[6:7]\n"
        "v_max_f32 v4, v0, v1\n"
        "v_max3_f32 v4, v2, v3, v4\n"
        "s_nop 1\n"
        "v_max_f32_dpp v5, v4, v4 quad_perm:[2,3,0,1] row_mask:0xf bank_mask:0xf\n"
        "v_max_f32_dpp v4, v4, v4 quad_perm:[2,3,0,1] row_mask:0xf bank_mask:0xf\n"
        "s_nop 1\n"
        "v_permlane16_swap_b32 v4, v5\n"
        "v_max_f32 v6, v4, v5\n"
        "v_max_f32 v4, v4, v5\n"
        "s_nop 1\n"
        "v_permlane32_swap_b32 v4, v6\n"
        "v_max_f32 v4, v4, v6\n"
        "v_pk_add_f32 v[0:1], v[0:1], v[4:5] op_sel_hi:[1,0] neg_lo:[0,1] neg_hi:[0,1]\n"
        "v_pk_add_f32 v[2:3], v[2:3], v[4:5] op_sel_hi:[1,0] neg_lo:[0,1] neg_hi:[0,1]\n"
        "v_exp_f32 v0, v0\n"
        "v_exp_f32 v1, v1\n"
        "v_exp_f32 v2, v2\n"
        "v_exp_f32 v3, v3\n"
        "v_cvt_pk_bf16_f32 v24, v0, v1\n"
        "v_cvt_pk_bf16_f32 v25, v2, v3\n"
        "s_nop 0\n"
        "v_mov_b32_dpp v26, v24 quad_perm:[2,3,0,1] row_mask:0xf bank_mask:0xf\n"
        "v_mov_b32_dpp v27, v25 quad_perm:[2,3,0,1] row_mask:0xf bank_mask:0xf\n"
        "s_nop 1\n"
        "v_smfmac_f32_16x16x64_bf16 v[20:23], v[24:27], v[88:95], v152\n"
        "v_smfmac_f32_16x16x64_bf16 v[8:11], v[24:27], v[64:71], v152\n"
        "v_smfmac_f32_16x16x64_bf16 v[12:15], v[24:27], v[72:79], v152\n"
        "v_smfmac_f32_16x16x64_bf16 v[16:19], v[24:27], v[80:87], v152\n"
        "v_cndmask_b32 v5, v22, v23, s[52:53]\n"
        "buffer_store_dwordx2 v[4:5], v150, s[40:43], s49 offen sc1\n"
        "v_cvt_pk_bf16_f32 v120, v8, v9\n"
        "v_cvt_pk_bf16_f32 v121, v10, v11\n"
        "v_cvt_pk_bf16_f32 v122, v12, v13\n"
        "v_cvt_pk_bf16_f32 v123, v14, v15\n"
        "v_permlane16_swap_b32 v120, v121\n"
        "s_nop 0\n"
        "v_cvt_pk_bf16_f32 v124, v16, v17\n"
        "v_cvt_pk_bf16_f32 v125, v18, v19\n"
        "v_permlane16_swap_b32 v122, v123\n"
        "v_cvt_pk_bf16_f32 v126, v20, v21\n"
        "v_cvt_pk_bf16_f32 v127, v22, v23\n"
        "v_permlane32_swap_b32 v120, v122\n"
        "v_permlane32_swap_b32 v121, v123\n"
        "v_permlane16_swap_b32 v124, v125\n"
        "v_permlane16_swap_b32 v126, v127\n"
        "buffer_store_dwordx4 v[120:123], v148, s[40:43], s45 offen sc1\n"
        "s_nop 0\n"
        "v_permlane32_swap_b32 v124, v126\n"
        "v_permlane32_swap_b32 v125, v127\n"
        "buffer_store_dwordx4 v[124:127], v149, s[40:43], s45 offen sc1\n"
        "s_add_u32 s44, s44, 2\n"
        "s_cmp_lt_u32 s44, 63\n"
        "s_nop 7\n"
        "s_cbranch_scc1 .Lh1_loop_%=\n"
        "s_waitcnt vmcnt(0) lgkmcnt(0)\n"
        ".Lhend_%=:\n"
        "s_endpgm\n"
        :
        : "s"(lo), "s"(hi), "s"(WS_BYTES), "s"(0), "s"(par), "s"(w)
        : "memory", "vcc", "scc", "s40", "s41", "s42", "s43", "s44", "s45", "s46", "s47", "s48", "s49", "s50", "s51", "s52", "s53", "s54", "s55", "s56", "s57", "s58", "s59", "s60", "s61", "s62", "s63", "s64", "s65", "s66", "s67", "s68", "s69", "s70", "s71", "s72", "s73", "s74", "s75", "s76", "s77", "s78", "s79", "v255", "a255");
}

#define TAILV 2
#define PLACE 6
__host__ __device__ inline int role_of(int b) {
    const int xcd = b & 7, idx = b >> 3;
    if (xcd == 0) return idx;
    if (xcd == 2) return 32 + idx;
    return xcd < 4 ? 64 + 128 + (xcd >> 1) * 32 + idx : 64 + (xcd - 4) * 32 + idx;
}
constexpr unsigned L_MAIN_BYTES = L_WORK_BYTES;

__global__ void __launch_bounds__(256, 1) k2_main(const __bf16* __restrict__ x, const __bf16* __restrict__ w1, const __bf16* __restrict__ w3,
                                                  const __bf16* __restrict__ w2, __bf16* __restrict__ out, unsigned char* __restrict__ ws) {
    __shared__ __attribute__((aligned(16))) unsigned char smem[L_MAIN_BYTES];
    const int slot = role_of(blockIdx.x);
    if ((unsigned)(uintptr_t)(LDS_AS unsigned char*)smem != 0u) return;
    const unsigned tab = ((const unsigned*)(ws + OFF_MISC))[threadIdx.x & 63];
    if (slot >= 64) {
        const int wi = slot - 64, wv = __builtin_amdgcn_readfirstlane(threadIdx.x >> 6);
        const rsrc_t rs = mk_rsrc(ws, WS_BYTES);
        WTS(0);
        if (wi < 80) stage1<0>(wi / 5, wi % 5, 5, w1, w3, ws, tab, wv);
        else if (wi < 160) stage1<1>((wi - 80) / 5, (wi - 80) % 5, 5, w1, w3, ws, tab, wv);
        else stage1<2>((wi - 160) >> 2, wi & 3, 4, w1, w3, ws, tab, wv);
        WTS(1);
        if (wi >= 96 && wi < 128) stage2((wi >> 3) & 3, wi & 7, w2, ws, wv);
        WTS(2);
        const int t2 = wi < 56 ? wi + 192 : ((wi >= 64 && (wi & 31) < 4) ? 248 + ((wi >> 5) & 1) * 4 + (wi & 3) : -1), m2 = wi < 56 ? 3 : (wi < 128 ? 1 : 2);
#pragma unroll 1
        for (int r = 0; r < (t2 >= 0 ? 2 : 1); ++r) {
            const int task = r ? t2 : wi;
            out_task(task >> 2, task & 3, r ? m2 : 3, x, out, rs, wv);
            WTS(3 + r);
        }
        return;
    }
    if (slot < 32) rec_main(slot, ws, tab);
    else helper_main(slot - 32, ws, tab);
}

extern "C" void kernel_launch(void* const* d_in, const int* in_sizes, int n_in, void* d_out, int out_size, void* d_ws, size_t ws_size,
                              hipStream_t stream) {
    unsigned char* ws = (unsigned char*)(((uintptr_t)d_ws + 2097151) & ~(uintptr_t)2097151);
    k1_prep<<<256, 256, 0, stream>>>((const __bf16*)d_in[0], (const __bf16*)d_in[1], (const __bf16*)d_in[2], (const __bf16*)d_in[3],
                                     (const __bf16*)d_in[4], (const __bf16*)d_in[5], ws);
    k2_main<<<256, 256, 0, stream>>>((const __bf16*)d_in[0], (const __bf16*)d_in[8], (const __bf16*)d_in[9], (const __bf16*)d_in[10],
                                     (__bf16*)d_out, ws);
}
```
